```python
import torch

def _softmax(x, axis=-1):
    return torch.softmax(x, dim=axis)

def _gelu(x):
    return torch.nn.functional.gelu(x, approximate='tanh')

def _silu(x):
    return torch.nn.functional.silu(x)

def _sigmoid(x):
    return torch.sigmoid(x)

def _layernorm(x, axis=-1, eps=1e-05):
    m = x.mean(dim=axis, keepdim=True)
    v = x.var(dim=axis, keepdim=True, unbiased=False)
    return (x - m) / torch.sqrt(v + eps)

def _scatter_add(tgt, idx, src, axis=0):
    return tgt.clone().index_add_(axis, idx, src)

def _segment_sum(src, seg, n):
    out = torch.zeros((n,) + src.shape[1:], dtype=src.dtype)
    return out.index_add_(0, seg, src)

def _pad(x, pw):
    flat = tuple((v for pair in reversed(pw) for v in pair))
    return torch.nn.functional.pad(x, flat)
kernel_name = 'adaLN_cond__mha_block__glu_gated_geglu__prompt'

def setup_inputs():

    _w = lambda a: (lambda t: t.to(torch.bfloat16) if t.is_floating_point() else t)(torch.as_tensor(a))

    def _A_setup():
        B, S, D, H = (1, 2048, 448, 16)
        return {'x': _w((lambda *s: torch.randn(*s, dtype=torch.bfloat16) * 0.05)(B, S, D)), 'wqkv': _w((lambda *s: torch.randn(*s, dtype=torch.bfloat16) * 0.05)(D, 3 * D)), 'wo': _w((lambda *s: torch.randn(*s, dtype=torch.bfloat16) * 0.05)(D, D))}
    _A_in = _A_setup()

    def _B_setup():
        B, S, Dm, Df = (1, 2048, 448, 1792)
        return {'x': _w((lambda *s: torch.randn(*s, dtype=torch.bfloat16) * 0.05)(B, S, Dm)), 'w_gate': _w((lambda *s: torch.randn(*s, dtype=torch.bfloat16) * 0.8)(Dm, Df) * Dm ** (-0.5)), 'w_up': _w((lambda *s: torch.randn(*s, dtype=torch.bfloat16) * 0.8)(Dm, Df) * Dm ** (-0.5)), 'w_down': _w((lambda *s: torch.randn(*s, dtype=torch.bfloat16) * 0.8)(Df, Dm) * Df ** (-0.5))}
    _B_in = _B_setup()
    return {'A_x': _A_in['x'], 'A_wqkv': _A_in['wqkv'], 'A_wo': _A_in['wo'], 'B_x': _B_in['x'], 'B_w_gate': _B_in['w_gate'], 'B_w_up': _B_in['w_up'], 'B_w_down': _B_in['w_down'], 'c': _w((lambda *s: torch.randn(*s, dtype=torch.bfloat16) * 0.1)(1, 448) * 8.0), 'w_mod': _w((lambda *s: torch.randn(*s, dtype=torch.bfloat16) * 0.1)(448, 6 * 448) * 448 ** (-0.5) * 8.0)}

def reference(A_x, A_wqkv, A_wo, B_x, B_w_gate, B_w_up, B_w_down, c, w_mod):

    def _A_ref(x, wqkv, wo):
        B, S, D = x.shape
        H = 16
        Dh = D // H
        qkv = (lambda x, *d: x.permute(*d))((x @ wqkv).reshape(B, S, 3, H, Dh), 2, 0, 3, 1, 4)
        q, k, v = (qkv[0], qkv[1], qkv[2])
        scores = torch.einsum('bhqd,bhkd->bhqk', q, k) * Dh ** (-0.5)
        p = _softmax(scores, axis=-1)
        o = torch.einsum('bhqk,bhkd->bhqd', p, v)
        return (lambda x, *d: x.permute(*d))(o, 0, 2, 1, 3).reshape(B, S, D) @ wo

    def _B_ref(x, w_gate, w_up, w_down):
        g = torch.einsum('bsd,df->bsf', x, w_gate)
        u = torch.einsum('bsd,df->bsf', x, w_up)
        h = _gelu(g) * u
        return torch.einsum('bsf,fd->bsd', h, w_down)
    x = A_x
    mod = (_silu(c) @ w_mod).reshape(c.shape[0], 6, 1, -1)
    sa, ca, ga, sb, cb, gb = (mod[:, 0].clone(), mod[:, 1].clone(), mod[:, 2].clone(), mod[:, 3].clone(), mod[:, 4].clone(), mod[:, 5].clone())
    h = _layernorm(x, axis=-1) * (1 + ca) + sa
    x = x + ga * _A_ref(h, A_wqkv, A_wo)
    h = _layernorm(x, axis=-1) * (1 + cb) + sb
    x = x + gb * _B_ref(h, B_w_gate, B_w_up, B_w_down)
    return x
```

```cpp
#include <hip/hip_runtime.h>
#include <hip/hip_bf16.h>
#include <stdint.h>

typedef unsigned short u16;
constexpr int S = 2048, D = 448, H = 16, DH = 28, DF = 1792;

__device__ __forceinline__ float bf2f(u16 v) { return __uint_as_float(((unsigned)v) << 16); }
__device__ __forceinline__ u16 f2bf(float f) { unsigned u = __float_as_uint(f); u += 0x7fffu + ((u >> 16) & 1u); return (u16)(u >> 16); }
__device__ __forceinline__ float rb(float f) { return bf2f(f2bf(f)); }

__global__ void k_mod(const u16* c, const u16* wmod, float* mod) {
  int n = blockIdx.x * blockDim.x + threadIdx.x;
  if (n >= 6 * D) return;
  float acc = 0.f;
  for (int k = 0; k < D; ++k) { float cv = bf2f(c[k]); float s = rb(cv / (1.f + __expf(-cv))); acc += s * bf2f(wmod[k * 6 * D + n]); }
  mod[n] = rb(acc);
}
__global__ void k_ln(const u16* x, const float* shift, const float* scale, u16* h) {
  int row = blockIdx.x; int lane = threadIdx.x;
  float v[7]; float s = 0.f;
  for (int i = 0; i < 7; ++i) { v[i] = bf2f(x[row * D + lane + 64 * i]); s += v[i]; }
  for (int o = 32; o; o >>= 1) s += __shfl_xor(s, o);
  float m = s / D; float q = 0.f;
  for (int i = 0; i < 7; ++i) { float d = v[i] - m; q += d * d; }
  for (int o = 32; o; o >>= 1) q += __shfl_xor(q, o);
  float rstd = rsqrtf(q / D + 1e-5f);
  for (int i = 0; i < 7; ++i) { int cidx = lane + 64 * i; h[row * D + cidx] = f2bf((v[i] - m) * rstd * (1.f + scale[cidx]) + shift[cidx]); }
}
__global__ void k_gemm(const u16* A, const u16* B, u16* C, int M, int N, int K, int mode, const u16* res, const float* gate) {
  __shared__ float As[16][17], Bs[16][17];
  int tx = threadIdx.x, ty = threadIdx.y; int row = blockIdx.y * 16 + ty, col = blockIdx.x * 16 + tx;
  float acc = 0.f;
  for (int k0 = 0; k0 < K; k0 += 16) {
    As[ty][tx] = bf2f(A[row * K + k0 + tx]); Bs[ty][tx] = bf2f(B[(k0 + ty) * N + col]);
    __syncthreads();
    for (int k = 0; k < 16; ++k) acc += As[ty][k] * Bs[k][tx];
    __syncthreads();
  }
  if (mode == 0) C[row * N + col] = f2bf(acc);
  else C[row * N + col] = f2bf(bf2f(res[row * N + col]) + rb(gate[col] * rb(acc)));
}
__global__ void k_attn(const u16* qkv, u16* o) {
  int h = blockIdx.y; int q = blockIdx.x * 64 + threadIdx.x;
  float qv[DH], acc[DH]; float m = -1e30f, l = 0.f;
  for (int d = 0; d < DH; ++d) { qv[d] = bf2f(qkv[q * 3 * D + h * DH + d]) * 0.1889822365f; acc[d] = 0.f; }
  __shared__ float Ks[64][DH], Vs[64][DH];
  for (int k0 = 0; k0 < S; k0 += 64) {
    __syncthreads();
    for (int d = 0; d < DH; ++d) { Ks[threadIdx.x][d] = bf2f(qkv[(k0 + threadIdx.x) * 3 * D + D + h * DH + d]); Vs[threadIdx.x][d] = bf2f(qkv[(k0 + threadIdx.x) * 3 * D + 2 * D + h * DH + d]); }
    __syncthreads();
    for (int k = 0; k < 64; ++k) {
      float s = 0.f;
      for (int d = 0; d < DH; ++d) s += qv[d] * Ks[k][d];
      float mn = fmaxf(m, s); float a = __expf(m - mn), p = __expf(s - mn);
      l = l * a + p;
      for (int d = 0; d < DH; ++d) acc[d] = acc[d] * a + p * Vs[k][d];
      m = mn;
    }
  }
  for (int d = 0; d < DH; ++d) o[q * D + h * DH + d] = f2bf(acc[d] / l);
}
__global__ void k_geglu(const u16* g, const u16* u, u16* h, int n) {
  int i = blockIdx.x * blockDim.x + threadIdx.x; if (i >= n) return;
  float x = bf2f(g[i]); float t = tanhf(0.7978845608f * (x + 0.044715f * x * x * x));
  h[i] = f2bf(rb(0.5f * x * (1.f + t)) * bf2f(u[i]));
}

extern "C" void kernel_launch(void* const* d_in, const int* in_sizes, int n_in, void* d_out, int out_size, void* d_ws, size_t ws_size, hipStream_t stream) {
  const u16* x = (const u16*)d_in[0]; const u16* wqkv = (const u16*)d_in[1]; const u16* wo = (const u16*)d_in[2];
  const u16* wg = (const u16*)d_in[4]; const u16* wu = (const u16*)d_in[5]; const u16* wd = (const u16*)d_in[6];
  const u16* c = (const u16*)d_in[7]; const u16* wmod = (const u16*)d_in[8];
  char* ws = (char*)d_ws;
  float* mod = (float*)ws; u16* h = (u16*)(ws + (1 << 20)); u16* qkv = (u16*)(ws + (4 << 20)); u16* o = (u16*)(ws + (12 << 20));
  u16* x2 = (u16*)(ws + (16 << 20)); u16* g = (u16*)(ws + (20 << 20)); u16* u = (u16*)(ws + (30 << 20)); u16* hm = (u16*)(ws + (40 << 20));
  k_mod<<<(6 * D + 63) / 64, 64, 0, stream>>>(c, wmod, mod);
  k_ln<<<S, 64, 0, stream>>>(x, mod, mod + D, h);
  k_gemm<<<dim3(3 * D / 16, S / 16), dim3(16, 16), 0, stream>>>(h, wqkv, qkv, S, 3 * D, D, 0, nullptr, nullptr);
  k_attn<<<dim3(S / 64, H), 64, 0, stream>>>(qkv, o);
  k_gemm<<<dim3(D / 16, S / 16), dim3(16, 16), 0, stream>>>(o, wo, x2, S, D, D, 1, x, mod + 2 * D);
  k_ln<<<S, 64, 0, stream>>>(x2, mod + 3 * D, mod + 4 * D, h);
  k_gemm<<<dim3(DF / 16, S / 16), dim3(16, 16), 0, stream>>>(h, wg, g, S, DF, D, 0, nullptr, nullptr);
  k_gemm<<<dim3(DF / 16, S / 16), dim3(16, 16), 0, stream>>>(h, wu, u, S, DF, D, 0, nullptr, nullptr);
  k_geglu<<<(S * DF + 255) / 256, 256, 0, stream>>>(g, u, hm, S * DF);
  k_gemm<<<dim3(D / 16, S / 16), dim3(16, 16), 0, stream>>>(hm, wd, (u16*)d_out, S, D, DF, 1, x2, mod + 5 * D);
}
```

```cpp
#include <hip/hip_runtime.h>
#include <stdint.h>

typedef unsigned short u16;
typedef unsigned u32;
typedef unsigned long long u64;
typedef __attribute__((ext_vector_type(2))) unsigned u32x2;
typedef __attribute__((ext_vector_type(4))) unsigned u32x4;
typedef __attribute__((ext_vector_type(2))) float f32x2;
typedef __attribute__((ext_vector_type(4))) float f32x4;
typedef __attribute__((ext_vector_type(16))) float f32x16;
typedef __attribute__((ext_vector_type(2))) __bf16 bf16x2;
typedef __attribute__((ext_vector_type(8))) __bf16 bf16x8;
typedef __attribute__((ext_vector_type(4))) short s16x4;
#define DEV __device__ __forceinline__
#define GAS __attribute__((address_space(1)))
#define LAS __attribute__((address_space(3)))
#define RLX_AGENT __ATOMIC_RELAXED, __HIP_MEMORY_SCOPE_AGENT

constexpr u32 OFF_MODG = 4096;
constexpr u32 OFF_X2G = 65536, OFF_UV = 13467648  , OFF_WQT = 524288, OFF_WOT = 1900544, OFF_WGU = 2359296, OFF_WDT = 5570560;
constexpr u32 OFF_KV = 7176192, OFF_OB = 11370496, OFF_X2 = 15302656, OFF_HB = 17137664, OFF_XH = 24477696;
constexpr u32 OFF_XF = 26312704, OFF_WQF = OFF_XF + 512 * 128, OFF_KVG = OFF_WQF + 1024 * 128, OFF_OF = OFF_KVG + 1024 * 128, OFF_HF = OFF_OF + 2048 * 128, OFF_PREPF = OFF_HF + 2048 * 128, WS_USED = OFF_PREPF + 256 * 128;
constexpr u32 OFF_STAMP = 29360128;
constexpr u32 L_STG = 90112  , L_Q = 131072, L_GAM = 139264, L_QN = 142848;
constexpr u32 L_PARK = 64512 + 1024  ;
constexpr u32 L_SP2 = 151552  , L_MODP = L_SP2, L_MX = L_SP2, L_LP = L_SP2, L_UV = 155648  , L_ST = 157696  , L_G2 = 159488  ;
constexpr u32 L_SYNC = 160000;
constexpr u32 LDS_BYTES = 163840;
constexpr int CODE_BYTES = 38000;
constexpr int SPIN_CAP = 1 << 15;
#define FIRST_POLL 280
#define KV_SLEEP 1
#define PREP_START 220
#define POLL_T 0
#define POLL_SLEEP 3

DEV float bflo(u32 w) { return __uint_as_float(w << 16); }
DEV float bfhi(u32 w) { return __uint_as_float(w & 0xffff0000u); }
DEV u32 pk2(float a, float b) { f32x2 v = {a, b}; return __builtin_bit_cast(u32, __builtin_convertvector(v, bf16x2)); }
DEV float rbf(float a) { return bflo(pk2(a, 0.f)); }
DEV float fma1(float a, float b, float c) { float r; asm("v_fma_f32 %0, %1, %2, %3" : "=v"(r) : "v"(a), "v"(b), "v"(c)); return r; }
DEV float dot2(u32 a, u32 b, float c) { return __builtin_amdgcn_fdot2_f32_bf16(__builtin_bit_cast(bf16x2, a), __builtin_bit_cast(bf16x2, b), c, false); }
DEV f32x16 mfma32(u32x4 a, u32x4 b, f32x16 c) { return __builtin_amdgcn_mfma_f32_32x32x16_bf16(__builtin_bit_cast(bf16x8, a), __builtin_bit_cast(bf16x8, b), c, 0, 0, 0); }
DEV f32x4 mfma16(u32x4 a, u32x4 b, f32x4 c) { return __builtin_amdgcn_mfma_f32_16x16x32_bf16(__builtin_bit_cast(bf16x8, a), __builtin_bit_cast(bf16x8, b), c, 0, 0, 0); }
DEV float xhalf_sum(float v) { auto r = __builtin_amdgcn_permlane32_swap(__float_as_uint(v), __float_as_uint(v), false, false); return __uint_as_float(r[0]) + __uint_as_float(r[1]); }
DEV float xhalf_max(float v) { auto r = __builtin_amdgcn_permlane32_swap(__float_as_uint(v), __float_as_uint(v), false, false); return fmaxf(__uint_as_float(r[0]), __uint_as_float(r[1])); }
#define DPPF(v, ctrl) __uint_as_float(__builtin_amdgcn_update_dpp(0u, __float_as_uint(v), ctrl, 0xf, 0xf, false))
DEV float wave_max(float v) {
  v = fmaxf(v, DPPF(v, 0x121)); v = fmaxf(v, DPPF(v, 0x122)); v = fmaxf(v, DPPF(v, 0x124)); v = fmaxf(v, DPPF(v, 0x128));
  auto r = __builtin_amdgcn_permlane16_swap(__float_as_uint(v), __float_as_uint(v), false, false);
  return xhalf_max(fmaxf(__uint_as_float(r[0]), __uint_as_float(r[1])));
}
#define WAIT_VM(n) asm volatile("s_waitcnt vmcnt(" #n ")" ::: "memory")
#define BAR() asm volatile("s_waitcnt lgkmcnt(0)\n\ts_barrier" ::: "memory")
#define SB() __builtin_amdgcn_sched_barrier(0)
#define CFENCE() asm volatile("" ::: "memory")
template <bool SC1> DEV void dma16(const void* sbase, u32 voff, u32 lds_dst) {
  u32 keep;
  if (SC1) asm volatile("s_mov_b32 %0, m0\n\ts_mov_b32 m0, %3\n\ts_nop 2\n\tglobal_load_lds_dwordx4 %1, %2 sc1\n\ts_mov_b32 m0, %0" : "=&s"(keep) : "v"(voff), "s"(sbase), "s"(lds_dst) : "memory");
  else asm volatile("s_mov_b32 %0, m0\n\ts_mov_b32 m0, %3\n\ts_nop 2\n\tglobal_load_lds_dwordx4 %1, %2\n\ts_mov_b32 m0, %0" : "=&s"(keep) : "v"(voff), "s"(sbase), "s"(lds_dst) : "memory");
}
DEV void dma16x4(const void* sbase, u32 voff, u32 lds_dst) {
  u32 keep;
  asm volatile("s_mov_b32 %0, m0\n\ts_mov_b32 m0, %3\n\ts_nop 2\n\tglobal_load_lds_dwordx4 %1, %2 sc1\n\tglobal_load_lds_dwordx4 %1, %2 offset:1024 sc1\n\t"
               "global_load_lds_dwordx4 %1, %2 offset:2048 sc1\n\tglobal_load_lds_dwordx4 %1, %2 offset:3072 sc1\n\ts_mov_b32 m0, %0" : "=&s"(keep) : "v"(voff), "s"(sbase), "s"(lds_dst) : "memory");
}
DEV void dma4_touch(const void* sbase, u32 voff, u32 lds_dst) {
  u32 keep;
  asm volatile("s_mov_b32 %0, m0\n\ts_mov_b32 m0, %3\n\ts_nop 2\n\tglobal_load_lds_dword %1, %2 sc1\n\ts_mov_b32 m0, %0" : "=&s"(keep) : "v"(voff), "s"(sbase), "s"(lds_dst) : "memory");
}
DEV u32x4 lds128(u32 a) { return *(const LAS u32x4*)a; }
DEV f32x4 lds128f(u32 a) { return *(const LAS f32x4*)a; }
DEV float lds32f(u32 a) { return *(const LAS float*)a; }
DEV u32x2 ldstr(u32 a) { return __builtin_bit_cast(u32x2, __builtin_amdgcn_ds_read_tr16_b64_v4i16((LAS s16x4*)a)); }
DEV u32x4 cat(u32x2 a, u32x2 b) { return (u32x4){a.x, a.y, b.x, b.y}; }
typedef __amdgpu_buffer_rsrc_t rsrc_t;
DEV u32x4 ld128(rsrc_t r, u32 voff, u32 soff) { return __builtin_amdgcn_raw_buffer_load_b128(r, voff, soff, 16); }
DEV u32 ld32(rsrc_t r, u32 voff, u32 soff) { return __builtin_amdgcn_raw_buffer_load_b32(r, voff, soff, 16); }
DEV void st128(rsrc_t r, u32 voff, u32 soff, u32x4 v) {
  __builtin_amdgcn_raw_buffer_store_b128(v, r, voff, soff, 16);
  asm volatile("s_nop 1" :: "v"(v) : "memory");
}
DEV void st32(rsrc_t r, u32 voff, u32 soff, u32 v) { __builtin_amdgcn_raw_buffer_store_b32(v, r, voff, soff, 16); }
DEV u32 poll32(const char* p) { return __hip_atomic_load((GAS u32*)p, RLX_AGENT); }
DEV u64 poll64(const char* p) { return __hip_atomic_load((GAS u64*)p, RLX_AGENT); }
DEV void flag32(char* p, u32 v) { __hip_atomic_store((GAS u32*)p, v, RLX_AGENT); }
DEV void flag64(char* p, u32 val, u32 tag) { __hip_atomic_store((GAS u64*)p, ((u64)tag << 32) | val, RLX_AGENT); }
DEV void wait_flags(const char* p, u32 tag) {
#pragma nounroll
  for (int i = 0; i < SPIN_CAP; ++i) { if (__all(poll32(p) == tag)) break; __builtin_amdgcn_s_sleep(POLL_SLEEP); }
  CFENCE();
}
DEV u32 wait_gran(const char* p, u32 tag) {
  u64 g = 0;
#pragma nounroll
  for (int i = 0; i < SPIN_CAP; ++i) { g = poll64(p); if (__all((u32)(g >> 32) == tag)) break; __builtin_amdgcn_s_sleep(POLL_SLEEP); }
  CFENCE();
  return (u32)g;
}
DEV u32 lds_ld(u32 a) { return __hip_atomic_load((LAS u32*)a, __ATOMIC_RELAXED, __HIP_MEMORY_SCOPE_WORKGROUP); }
DEV void lds_st(u32 a, u32 v) { __hip_atomic_store((LAS u32*)a, v, __ATOMIC_RELAXED, __HIP_MEMORY_SCOPE_WORKGROUP); }
DEV u32 lds_inc(u32 a) { return __builtin_amdgcn_readfirstlane(__hip_atomic_fetch_add((LAS u32*)a, 1u, __ATOMIC_RELAXED, __HIP_MEMORY_SCOPE_WORKGROUP)); }
DEV void lds_wait(u32 a, u32 want) {
#pragma nounroll
  for (int i = 0; i < (SPIN_CAP << 6); ++i) { if (__builtin_amdgcn_readfirstlane(lds_ld(a)) >= want) break; __builtin_amdgcn_s_sleep(1); }
  CFENCE();
}
#define WAIT_LGKM() asm volatile("s_waitcnt lgkmcnt(0)" ::: "memory")
#define STAMP(i) do {} while (0)
#define STAMPL(i, s) STAMP(i)
#define GATE_MOD lds_wait(lb + L_SYNC + 36, 1)

__global__ __launch_bounds__(512, 2) void fused_block(const u16* __restrict__ x, const u16* __restrict__ wqkv, const u16* __restrict__ wo,
                                                      const u16* __restrict__ wg, const u16* __restrict__ wu, const u16* __restrict__ wd,
                                                      const u16* __restrict__ cvec, const u16* __restrict__ wmod, u16* __restrict__ out, char* ws) {
  __shared__ __attribute__((aligned(16))) char smem[LDS_BYTES];
  const int b = blockIdx.x, tid = threadIdx.x, lane = tid & 63;
  const int wave = __builtin_amdgcn_readfirstlane(tid >> 6);
  const int l31 = lane & 31, hi = lane >> 5;
  const int xg = b & 7, slot = b >> 3;
  const int ai = 4 * (xg >> 1) + (slot & 3), ah = 8 * (xg & 1) + (slot >> 2);
  const u32 lb = (u32)(uintptr_t)(LAS char*)smem;
  const rsrc_t wsr = __builtin_amdgcn_make_buffer_rsrc(ws, 0, WS_USED, 0x00020000);
  STAMP(0);
  {
    u64 pc; asm volatile("s_getpc_b64 %0" : "=s"(pc));
    if (wave == 0) {
      const u32 off = min((u32)(slot * 1280 + min(lane, 9) * 128), (u32)CODE_BYTES);
      u32 keep;
      asm volatile("s_mov_b32 %0, m0\n\ts_mov_b32 m0, %3\n\ts_nop 2\n\tglobal_load_lds_dword %1, %2\n\ts_mov_b32 m0, %0" : "=&s"(keep) : "v"(off), "s"(pc), "s"(lb + L_ST) : "memory");
    }
  }
  if (wave == 1) {
    const u32 off = min(lane, 12) << 21;
    u32 keep;
    asm volatile("s_mov_b32 %0, m0\n\ts_mov_b32 m0, %3\n\ts_nop 2\n\tglobal_load_lds_dword %1, %2\n\ts_mov_b32 m0, %0" : "=&s"(keep) : "v"(off), "s"(ws), "s"(lb + L_ST + 256) : "memory");
  }
  if (wave == 2 && lane < 16) lds_st(lb + L_SYNC + lane * 4, 0u);
  const u64 t_entry = __builtin_amdgcn_s_memrealtime();
  const u32 epoch_v = poll32(ws);
  u32 tag;

  f32x16 accQ, accK, accV;
  const int tq4 = (lane & 15) >> 2, tp4 = lane & 3, grp = lane >> 4;
  if (b < 42) {
    u32x4 wv[7];
    const u32 cv = cvec[56 * wave + min(lane, 55)];
#pragma unroll
    for (int n = 0; n < 7; ++n) wv[n] = *(const u32x4*)((const char*)wmod + (size_t)(56 * wave + 8 * n + (lane >> 3)) * 5376 + 128 * b + 16 * (lane & 7));
    BAR();
    const float cf = bflo(cv);
    const float sl = rbf(cf * __builtin_amdgcn_rcpf(1.f + __builtin_amdgcn_exp2f(-1.4426950408889634f * cf)));
    float acc[8] = {0.f, 0.f, 0.f, 0.f, 0.f, 0.f, 0.f, 0.f};
#pragma unroll
    for (int n = 0; n < 7; ++n) {
      const float sn = __uint_as_float(__builtin_amdgcn_ds_bpermute((8 * n + (lane >> 3)) * 4, __float_as_uint(sl)));
#pragma unroll
      for (int j = 0; j < 4; ++j) { acc[2 * j] = fmaf(sn, bflo(wv[n][j]), acc[2 * j]); acc[2 * j + 1] = fmaf(sn, bfhi(wv[n][j]), acc[2 * j + 1]); }
    }

#pragma unroll
    for (int j = 0; j < 8; ++j) acc[j] += DPPF(acc[j], 0x128);
#pragma unroll
    for (int j = 0; j < 4; ++j) { auto r = __builtin_amdgcn_permlane16_swap(__float_as_uint(acc[j]), __float_as_uint(acc[j + 4]), false, false); acc[j] = __uint_as_float(r[0]) + __uint_as_float(r[1]); }
#pragma unroll
    for (int j = 0; j < 2; ++j) { auto r = __builtin_amdgcn_permlane32_swap(__float_as_uint(acc[j]), __float_as_uint(acc[j + 2]), false, false); acc[j] = __uint_as_float(r[0]) + __uint_as_float(r[1]); }
    if (!(lane & 8)) *(LAS f32x2*)(lb + L_MODP + (wave * 64 + 8 * (lane & 7) + 2 * hi + 4 * (grp & 1)) * 4) = (f32x2){acc[0], acc[1]};
    WAIT_LGKM();
    u32 arrived = 0;
    if (lane == 0) arrived = __hip_atomic_fetch_add((LAS u32*)(lb + L_SYNC + 12), 1u, __ATOMIC_RELAXED, __HIP_MEMORY_SCOPE_WORKGROUP);
    if (__builtin_amdgcn_readfirstlane(arrived) == 7) {
      CFENCE();
      f32x2 t = {0.f, 0.f};
#pragma unroll
      for (int p = 0; p < 8; ++p) t += *(const LAS f32x2*)(lb + L_MODP + (p * 64 + 2 * l31) * 4);
      const u32 pv = pk2(t[0], t[1]), nv = __builtin_amdgcn_update_dpp(0u, pv, 0xB1, 0xf, 0xf, false);
      st128(wsr, (lane < 32 && !(lane & 1)) ? (u32)(32 * b + lane) * 8 : 0x80000000u, OFF_MODG, (u32x4){pv, epoch_v + 1u, nv, epoch_v + 1u});
      WAIT_VM(0);
      lds_st(lb + L_SYNC + 32, 1u);
    }
  } else if (wave < 3 && 3 * (b - 42) + wave < 512) {
    const int xgp = 3 * (b - 42) + wave, row = 4 * xgp + (lane & 3), c0i = lane >> 2;
    u32x4 xr[4];
#pragma unroll
    for (int n = 0; n < 4; ++n) xr[n] = *(const u32x4*)((const char*)x + (size_t)row * 896 + min(c0i + 16 * n, 55) * 16);
    BAR();
    if (c0i >= 8) xr[3] = (u32x4){0u, 0u, 0u, 0u};
    float s1[2] = {0.f, 0.f}, s2[2] = {0.f, 0.f};
#pragma unroll
    for (int n = 0; n < 4; ++n)
#pragma unroll
      for (int j = 0; j < 4; ++j) { s1[j & 1] = dot2(xr[n][j], 0x3F803F80u, s1[j & 1]); s2[j & 1] = dot2(xr[n][j], xr[n][j], s2[j & 1]); }
    float t1 = s1[0] + s1[1], t2 = s2[0] + s2[1];
    t1 += DPPF(t1, 0x124); t2 += DPPF(t2, 0x124); t1 += DPPF(t1, 0x128); t2 += DPPF(t2, 0x128);
    { auto r = __builtin_amdgcn_permlane16_swap(__float_as_uint(t1), __float_as_uint(t2), false, false); t1 = __uint_as_float(r[0]) + __uint_as_float(r[1]); }
    t1 = xhalf_sum(t1);
    { auto r = __builtin_amdgcn_permlane16_swap(__float_as_uint(t1), __float_as_uint(t1), false, false); t1 = __uint_as_float(r[0]); t2 = __uint_as_float(r[1]); }
    const float mean = t1 * (1.f / 448.f), ex2 = t2 * (1.f / 448.f);
    const float rstd = __builtin_amdgcn_rsqf(fmaxf(ex2 - mean * mean, 0.f) + 1e-5f), c0 = -mean * rstd;
    asm volatile("" :: "v"(c0));

#pragma unroll
    for (int n = 0; n < 4; ++n) {
      u32x4 o;
#pragma unroll
      for (int j = 0; j < 4; ++j) asm("v_cvt_pk_f16_f32 %0, %1, %2" : "=v"(o[j]) : "v"(fmaf(bflo(xr[n][j]), rstd, c0)), "v"(fmaf(bfhi(xr[n][j]), rstd, c0)));
      const u32 c = c0i + 16 * n;
      st128(wsr, c < 56 ? (c >> 1) * 1024 + ((row & 31) + 32 * (c & 1)) * 16 + (u32)(row >> 5) * 28672 : 0x80000000u, OFF_XH, o);
    }
    WAIT_VM(0);
    if (lane == 0) flag32(ws + OFF_XF + xgp * 128, epoch_v + 1u);
  } else if ((wave == 4 || wave == 5) && b < 210) {
    const int u = b - 42, part = u / 56, rem = u - part * 56, hp = rem / 7, kg = rem - hp * 7, s0 = 4 * kg + 2 * (wave - 4);
    const char* src = (const char*)wqkv + (size_t)(16 * s0) * 2688 + part * 896 + hp * 112;
    const u32 stg = lb + L_Q + (wave - 4) * 4096;
#pragma unroll
    for (int i = 0; i < 4; ++i) { const u32 c = lane & 7; dma16<false>(src + i * 8 * 2688, (lane >> 3) * 2688 + 16 * (c == 7 ? 6 : c), stg + i * 1024); }
    BAR();
    WAIT_VM(0);

#pragma unroll
    for (int e = 0; e < 4; ++e) {
      const u32 a = stg + (16 * (e >> 1) + 8 * (grp >> 1) + tq4) * 128 + (e & 1) * 56 + (grp & 1) * 32 + tp4 * 8;
      u32x4 f = cat(ldstr(a), ldstr(a + 512));
      if (l31 >= 28) f = (u32x4){0u, 0u, 0u, 0u};
      st128(wsr, lane * 16, OFF_WQT + (u32)((2 * hp + (e & 1)) * 84 + 3 * (s0 + (e >> 1)) + part) * 1024, f);
    }
    WAIT_VM(0);
    if (lane < 2) flag32(ws + OFF_WQF + ((2 * hp + lane) * 64 + 3 * (s0 >> 1) + part) * 128, epoch_v + 1u);
  } else {
    BAR();
  }
  tag = __builtin_amdgcn_readfirstlane(epoch_v) + 1u;
  STAMPL(7, "unit done");
  const int cg = b / 7, kg = b - cg * 7;
  if (wave >= 6) {
#pragma nounroll
    for (int i = 0; i < 4096 && __builtin_amdgcn_s_memrealtime() - t_entry < PREP_START; ++i) __builtin_amdgcn_s_sleep(1);
#pragma unroll
    for (int ph = 0; ph < 2; ++ph) {
      const int pw = wave & 1, tw = 2 * ph + pw, p = 4 * b + tw;
      const u32 stg = lb + L_STG + tw * 6144;
      if (b < 196) {
#pragma unroll
        for (int i = 0; i < 2; ++i) {
          const u32 vo = (u32)(64 * kg + 32 * ph + 16 * i + (lane >> 2)) * 3584 + (64 * cg + 32 * pw) * 2 + 16 * (lane & 3);
          dma16<false>(wg, vo, stg + (2 * i) * 1024); dma16<false>(wu, vo, stg + (2 * i + 1) * 1024);
        }
      }
      if (p < 784) {
        const int ds_ = p / 7, np = p - ds_ * 7;
#pragma unroll
        for (int i = 0; i < 2; ++i) dma16<false>(wd, (u32)(16 * ds_ + (lane >> 2)) * 896 + np * 128 + i * 64 + 16 * (lane & 3), stg + (4 + i) * 1024);
      } else if (p < 1008) {
        const int q = p - 784, h = q / 14, nq = q - h * 14;
#pragma unroll
        for (int i = 0; i < 2; ++i) dma16<false>(wo, (u32)min(28 * h + 16 * i + (lane >> 2), 447) * 896 + nq * 64 + 16 * (lane & 3), stg + (4 + i) * 1024);
      }
    }
    STAMPL(6, "prep issued");
    WAIT_VM(0);
    if (lane == 0) __hip_atomic_fetch_add((LAS u32*)(lb + L_SYNC + 52), 1u, __ATOMIC_RELAXED, __HIP_MEMORY_SCOPE_WORKGROUP);
    STAMPL(5, "prep landed");
  } else if (wave >= 3) {
#pragma nounroll
    for (int i = 0; i < 4096 && __builtin_amdgcn_s_memrealtime() - t_entry < FIRST_POLL; ++i) __builtin_amdgcn_s_sleep(1);
  }

  if (wave < 4) {
    if (wave == 3) { wait_flags(ws + OFF_XF + (32 * ai + l31) * 128, tag); GATE_MOD; lds_st(lb + L_SYNC, 1u); }
    else lds_wait(lb + L_SYNC, 1);

    STAMPL(3, "xhat go");
    u32x4 xf[28], pf[6]; u32 pfo[6], uvo[2]; float uvv[2];
    constexpr int XPRE = 12;
#define LDX(s) xf[s] = ld128(wsr, lane * 16 + ((s) & 3) * 1024, OFF_XH + (u32)((4 * ai + wave) * 28 + ((s) & ~3)) * 1024)
#pragma unroll
    for (int s = 0; s < XPRE; ++s) { LDX(s); SB(); }
    {
      const int pw = wave & 1, ph = (wave >> 1) & 1, p = 4 * b + wave;
      const u32 stg = lb + L_STG + wave * 6144;
      const bool isd = p < 784;
      const int ds_ = p / 7, np = p - ds_ * 7, q = p - 784, h = q / 14, nq = q - h * 14;
      lds_wait(lb + L_SYNC + 52, 2);
      const int pi = ((tp4 & 1) << 1) | (tp4 >> 1);
      float uu[2] = {0.f, 0.f}, vv[2] = {0.f, 0.f};
#pragma unroll
      for (int i = 0; i < 2; ++i) {
        const u32x4 be = lds128(lb + L_G2 + (2 * ph + i) * 32 + hi * 16), ga = lds128(lb + L_G2 + 128 + (2 * ph + i) * 32 + hi * 16);
#pragma unroll
        for (int e = 0; e < 2; ++e) {
          const u32 a = stg + (2 * i + (grp & 1)) * 1024 + (8 * (grp >> 1) + tq4) * 64 + e * 32 + pi * 8;
          u32x4 f = cat(ldstr(a), ldstr(a + 256));
#pragma unroll
          for (int j = 0; j < 4; ++j) {
            const float w0 = bflo(f[j]), w1 = bfhi(f[j]);
            vv[e] = fma1(w0, bflo(be[j]), vv[e]); vv[e] = fma1(w1, bfhi(be[j]), vv[e]);
            f[j] = pk2(w0 * bflo(ga[j]), w1 * bfhi(ga[j]));
            uu[e] += bflo(f[j]); uu[e] += bfhi(f[j]);
          }
          pf[2 * i + e] = f;
          pfo[2 * i + e] = b < 196 ? OFF_WGU + (u32)((4 * cg + 2 * pw + e) * 28 + 4 * kg + 2 * ph + i) * 1024 : 0x80000000u;
        }
      }
#pragma unroll
      for (int e = 0; e < 2; ++e) {
        const float us = xhalf_sum(uu[e]), vs = xhalf_sum(vv[e]);
        uvv[e] = hi ? vs : us; uvo[e] = b < 196 ? OFF_UV + (u32)((2 * kg + ph) * 112 + 4 * cg + 2 * pw + e) * 256 : 0x80000000u;
      }
#pragma unroll
      for (int i = 0; i < 2; ++i) {
        const u32 a = isd ? stg + (4 + i) * 1024 + (8 * (grp >> 1) + tq4) * 64 + (grp & 1) * 32 + tp4 * 8
                          : stg + 4096 + (16 * (grp >> 1) + 4 * (grp & 1) + tq4) * 64 + i * 32 + tp4 * 8;
        u32x4 f = cat(ldstr(a), ldstr(a + (isd ? 256 : 512)));
        if (!isd && grp == 3) { f[2] = 0u; f[3] = 0u; }
        pf[4 + i] = f;
        pfo[4 + i] = isd ? OFF_WDT + (u32)((2 * np + i) * 112 + ds_) * 1024 : p < 1008 ? OFF_WOT + (u32)((2 * nq + i) * 16 + h) * 1024 : 0x80000000u;
      }
      asm volatile("" : "+v"(pf[0]), "+v"(pf[1]), "+v"(pf[2]), "+v"(pf[3]), "+v"(pf[4]), "+v"(pf[5]), "+v"(uvv[0]), "+v"(uvv[1]));
      STAMPL(10, "prep tiled");
    }
    lds_wait(lb + L_SYNC + 4, 4);

    STAMPL(8, "W1 ready");
    accQ = (f32x16){}; accK = (f32x16){}; accV = (f32x16){};
    {
      u32 gb = lb + L_GAM + hi * 16; asm volatile("" : "+v"(gb));
      u32 wb = lb + lane * 16, wb2 = wb + 43008; asm volatile("" : "+v"(wb), "+v"(wb2));
      u32x4 wf[3][3], hf[2], gs[3], be[3]; float ht[8];
#define RDG(s) gs[(s) % 3] = lds128(gb + (s) * 32)
#define RDB(s) be[(s) % 3] = lds128(gb + 896 + (s) * 32)
#define RDW1(s, p) wf[(s) % 3][p] = lds128(((s) < 14 ? wb : wb2) + (3 * ((s) % 14) + (p)) * 1024)
#define RDW(s) do { RDW1(s, 0); RDW1(s, 1); RDW1(s, 2); } while (0)
#define DOTS(s, h) do { _Pragma("unroll") for (int j = 2 * (h); j < 2 * (h) + 2; ++j) { \
        asm volatile("v_fma_mix_f32 %0, %1, %2, %3 op_sel_hi:[1,1,1]" : "=v"(ht[2 * j]) : "v"(xf[s][j]), "v"(gs[(s) % 3][j]), "v"(be[(s) % 3][j])); \
        asm volatile("v_fma_mix_f32 %0, %1, %2, %3 op_sel:[1,1,1] op_sel_hi:[1,1,1]" : "=v"(ht[2 * j + 1]) : "v"(xf[s][j]), "v"(gs[(s) % 3][j]), "v"(be[(s) % 3][j])); } } while (0)
#define CVTS(s) do { hf[(s) & 1] = (u32x4){pk2(ht[0], ht[1]), pk2(ht[2], ht[3]), pk2(ht[4], ht[5]), pk2(ht[6], ht[7])}; asm volatile("" : "+v"(hf[(s) & 1])); } while (0)
      RDG(0); RDB(0); RDW(0); RDG(1); RDB(1); RDW(1); RDG(2); RDB(2); RDW(2); DOTS(0, 0); DOTS(0, 1); CVTS(0); SB();
#pragma unroll
      for (int s = 0; s < 28; ++s) {
        if (s == 1) lds_wait(lb + L_SYNC + 8, 3);
        if (s == 13) lds_wait(lb + L_SYNC + 24, 3);
        SB(); accQ = mfma32(wf[s % 3][0], hf[s & 1], accQ); SB();
        if (s < 25) RDG(s + 3);
        if (s < 25) RDW1(s + 3, 0);
        if (s < 27) DOTS(s + 1, 0);
        SB(); accK = mfma32(wf[s % 3][1], hf[s & 1], accK); SB();
        if (s < 25) RDB(s + 3);
        if (s < 25) RDW1(s + 3, 1);
        if (s < 27) DOTS(s + 1, 1);
        SB(); accV = mfma32(hf[s & 1], wf[s % 3][2], accV); SB();
        if (s < 25) RDW1(s + 3, 2);
        if (s + XPRE < 28) LDX(s + XPRE);
        else if (s < 22) st128(wsr, lane * 16 + pfo[s - 16], 0, pf[s - 16]);
        else if (s < 24) st32(wsr, lane * 4 + uvo[s - 22], 0, __float_as_uint(uvv[s - 22]));
        if (s < 27) CVTS(s + 1);
        SB();
      }
    }
    STAMPL(5, "qkv mfma done");
    const float unscale = __uint_as_float(__builtin_amdgcn_readfirstlane(lds_ld(lb + L_SYNC + 28)));
    if (unscale != 1.f) { accQ *= unscale; accK *= unscale; accV *= unscale; }
    u32x4 k0, k1, v0, v1, q0, q1;
#pragma unroll
    for (int j = 0; j < 4; ++j) {
      k0[j] = pk2(accK[2 * j], accK[2 * j + 1]); k1[j] = pk2(accK[8 + 2 * j], accK[9 + 2 * j]);
      v0[j] = pk2(accV[2 * j], accV[2 * j + 1]); v1[j] = pk2(accV[8 + 2 * j], accV[9 + 2 * j]);
    }
    if (hi) { k1[2] = 0x3F803F80u; k1[3] = 0x00003F80u; }
    if (l31 == 28) { v0 = (u32x4){0x3F803F80u, 0x3F803F80u, 0x3F803F80u, 0x3F803F80u}; v1 = v0; }
    const u32 kvo = OFF_KV + (u32)(ah * 64 + ai * 4 + wave) * 4096;
    st128(wsr, lane * 16, kvo, k0); st128(wsr, lane * 16 + 1024, kvo, k1); st128(wsr, lane * 16 + 2048, kvo, v0); st128(wsr, lane * 16 + 3072, kvo, v1);
    SB();
    float kn = 0.f, qn = 0.f;
    constexpr float QS = 0.18898223650461362f * 1.4426950408889634f;
#pragma unroll
    for (int r = 0; r < 16; ++r) { kn = fmaf(accK[r], accK[r], kn); accQ[r] *= QS; qn = fmaf(accQ[r], accQ[r], qn); }
#pragma unroll
    for (int j = 0; j < 4; ++j) { q0[j] = pk2(accQ[2 * j], accQ[2 * j + 1]); q1[j] = pk2(accQ[8 + 2 * j], accQ[9 + 2 * j]); }
    *(LAS u32x4*)(lb + L_Q + wave * 2048 + lane * 16) = q0; *(LAS u32x4*)(lb + L_Q + wave * 2048 + 1024 + lane * 16) = q1;
    qn = xhalf_sum(qn); kn = wave_max(xhalf_sum(kn));
    if (!hi) *(LAS float*)(lb + L_QN + (wave * 32 + l31) * 4) = qn;
    WAIT_VM(0);
    if (lane == 0) flag64(ws + OFF_KVG + (ah * 64 + ai * 4 + wave) * 128, __float_as_uint(kn), tag);
    if (b < 42) lds_wait(lb + L_SYNC + 32, 1);
    if (lane == 0 && __hip_atomic_fetch_add((LAS u32*)(lb + L_SYNC + 44), 1u, __ATOMIC_RELAXED, __HIP_MEMORY_SCOPE_WORKGROUP) == 3u) flag32(ws + OFF_PREPF + b * 128, tag);
    STAMPL(6, "kv flagged");
  } else {
    if (wave == 4) {
      u64 gv[8];
      __builtin_amdgcn_s_setprio(3);
#pragma nounroll
      for (int i = 0; i < SPIN_CAP; ++i) {
        bool ok = true;
#pragma unroll
        for (int n = 0; n < 7; ++n) gv[n] = poll64(ws + OFF_MODG + (64 * n + lane) * 8);
        gv[7] = poll64(ws + OFF_MODG + (672 + 224 * hi + 32 * kg + l31) * 8);
#pragma unroll
        for (int n = 0; n < 8; ++n) ok = ok && (u32)(gv[n] >> 32) == tag;
        if (__all(ok)) break;
        __builtin_amdgcn_s_sleep(POLL_SLEEP);
      }
      CFENCE();
      __builtin_amdgcn_s_setprio(0);
      *(LAS u32*)(lb + L_G2 + lane * 4) = hi ? pk2(1.f + bflo((u32)gv[7]), 1.f + bfhi((u32)gv[7])) : (u32)gv[7];
      WAIT_LGKM();
      lds_st(lb + L_SYNC + 36, 1u);

      float lo[7], hi2[7], mxv = 0.f;
#pragma unroll
      for (int n = 0; n < 7; ++n) {
        u32 v = (u32)gv[n];
        if (64 * n + lane >= 224) v = pk2(1.f + bflo(v), 1.f + bfhi(v));
        lo[n] = bflo(v); hi2[n] = bfhi(v); mxv = fmaxf(mxv, fmaxf(fabsf(lo[n]), fabsf(hi2[n])));
      }
      const u32 ex = max((int)(__float_as_uint(wave_max(mxv)) >> 23) - (127 + 14), 0);
      const float scl = __uint_as_float((127u - min(ex, 114u)) << 23);
#pragma unroll
      for (int n = 0; n < 7; ++n) {
        u32 o; asm("v_cvt_pk_f16_f32 %0, %1, %2" : "=v"(o) : "v"(lo[n] * scl), "v"(hi2[n] * scl));
        const int g = 64 * n + lane;
        *(LAS u32*)(lb + L_GAM + (g >= 224 ? g - 224 : g + 224) * 4) = o;
      }
      lds_st(lb + L_SYNC + 28, (127u + min(ex, 114u)) << 23);
      WAIT_LGKM();
      if (lane == 0) __hip_atomic_fetch_add((LAS u32*)(lb + L_SYNC + 4), 1u, __ATOMIC_RELAXED, __HIP_MEMORY_SCOPE_WORKGROUP);
    } else {
      if (wave == 5) { wait_flags(ws + OFF_WQF + (ah * 64 + min(lane, 41)) * 128, tag); GATE_MOD; lds_st(lb + L_SYNC + 48, 1u); }
      else lds_wait(lb + L_SYNC + 48, 1);

      const char* wsrc = ws + OFF_WQT + (size_t)ah * 84 * 1024 + (wave - 5) * 4096;
#pragma unroll
      for (int n = 0; n < 7; ++n) dma16x4(wsrc + n * 12288, lane * 16, lb + (wave - 5) * 4096 + n * 12288);
    }
    if (wave >= 5) {
      WAIT_VM(24);
      if (lane == 0) __hip_atomic_fetch_add((LAS u32*)(lb + L_SYNC + 4), 1u, __ATOMIC_RELAXED, __HIP_MEMORY_SCOPE_WORKGROUP);
      WAIT_VM(12);
      if (lane == 0) __hip_atomic_fetch_add((LAS u32*)(lb + L_SYNC + 8), 1u, __ATOMIC_RELAXED, __HIP_MEMORY_SCOPE_WORKGROUP);
      WAIT_VM(0);
      if (lane == 0) __hip_atomic_fetch_add((LAS u32*)(lb + L_SYNC + 24), 1u, __ATOMIC_RELAXED, __HIP_MEMORY_SCOPE_WORKGROUP);
      STAMPL(9, "W landed");
    }
  }
  BAR();
  STAMPL(11, "B3");

  const int ort = 8 * xg + (slot >> 2), ocq = slot & 3;
  const int gi = ai, gfj = ah;
  u32x4 wo_f[16], wgu[28], xres, gaA, gaB;
  {
    u32x4 qf[4][2];
#pragma unroll
    for (int t = 0; t < 4; ++t) { qf[t][0] = lds128(lb + L_Q + t * 2048 + lane * 16); qf[t][1] = lds128(lb + L_Q + t * 2048 + 1024 + lane * 16); }
    float qn2[4];
#pragma unroll
    for (int t = 0; t < 4; ++t) qn2[t] = lds32f(lb + L_QN + (t * 32 + l31) * 4);
    WAIT_LGKM();
    if (lane == 0) __hip_atomic_fetch_add((LAS u32*)(lb + L_SYNC + 40), 1u, __ATOMIC_RELAXED, __HIP_MEMORY_SCOPE_WORKGROUP);
    if (wave == 3) {
      __builtin_amdgcn_s_sleep(KV_SLEEP);
      const float km = wave_max(__uint_as_float(wait_gran(ws + OFF_KVG + (ah * 64 + lane) * 128, tag)));
      lds_st(lb + L_SYNC + 60, __float_as_uint(km));
      WAIT_LGKM();
      lds_st(lb + L_SYNC + 56, 1u);
    } else lds_wait(lb + L_SYNC + 56, 1);
    const float kmax2 = __uint_as_float(lds_ld(lb + L_SYNC + 60));
    STAMPL(12, "kv flags seen");
    const u32 kvb = OFF_KV + (u32)(ah * 64 + 8 * wave) * 4096;
    u32x4 kf[2][2], vf[2][2];
    const u32 kvl = lane * 16;
#define LDKV(D, kt, off) asm volatile("buffer_load_dwordx4 %0, %2, %3, %4 offen offset:" #off " sc1\n\tbuffer_load_dwordx4 %1, %2, %3, %4 offen offset:" #off "+1024 sc1" \
      : "=&v"(D[(kt) & 1][0]), "=&v"(D[(kt) & 1][1]) : "v"(kvl), "s"(wsr), "s"(kvb + (kt) * 4096) : "memory")
#define WAITN(n) asm volatile("s_waitcnt vmcnt(%0)" :: "i"(n) : "memory")
#define PINKV(D, kt) asm volatile("" : "+v"(D[(kt) & 1][0]), "+v"(D[(kt) & 1][1]))
#define WAITKV(D, kt, n) do { WAITN(n); SB(); PINKV(D, kt); } while (0)
#define WAITKV2(D, kt, n0, n1) do { if (wave < 4) WAITN(n0); else WAITN(n1); SB(); PINKV(D, kt); } while (0)
    asm volatile("s_nop 4");
    LDKV(kf, 0, 0); LDKV(vf, 0, 2048); LDKV(kf, 1, 0);
    float sh[4]; bool safe = true;
#pragma unroll
    for (int t = 0; t < 4; ++t) { sh[t] = 1.02f * __builtin_amdgcn_sqrtf(qn2[t] * kmax2); safe = safe && (sh[t] <= 48.f); }
    if (!__all(safe)) {
      BAR();
      float mx[4] = {-INFINITY, -INFINITY, -INFINITY, -INFINITY};
#pragma unroll 1
      for (int kt = 0; kt < 8; ++kt) {
        const u32x4 ka = ld128(wsr, lane * 16, kvb + kt * 4096), kb = ld128(wsr, lane * 16 + 1024, kvb + kt * 4096);
#pragma unroll
        for (int t = 0; t < 4; ++t) {
          f32x16 s = mfma32(ka, qf[t][0], (f32x16){}); s = mfma32(kb, qf[t][1], s);
#pragma unroll
          for (int r = 0; r < 16; ++r) mx[t] = fmaxf(mx[t], s[r]);
        }
      }
#pragma unroll
      for (int t = 0; t < 4; ++t) { const float m = xhalf_max(mx[t]); if (!hi) *(LAS float*)(lb + L_MX + (wave * 128 + t * 32 + l31) * 4) = m; }
      BAR();
#pragma unroll
      for (int t = 0; t < 4; ++t) {
        float m = -INFINITY;
#pragma unroll
        for (int w = 0; w < 8; ++w) m = fmaxf(m, lds32f(lb + L_MX + (w * 128 + t * 32 + l31) * 4));
        sh[t] = m;
      }
    }
    if (hi) {
#pragma unroll
      for (int t = 0; t < 4; ++t) {
        const float a = rbf(-sh[t]), r1 = -sh[t] - a, c = rbf(r1);
        qf[t][1][2] = pk2(a, c); qf[t][1][3] = pk2(r1 - c, 0.f);
      }
    }
    f32x16 o[4] = {(f32x16){}, (f32x16){}, (f32x16){}, (f32x16){}};
    WAITKV(kf, 0, 4);
    f32x16 sc = mfma32(kf[0][0], qf[0][0], (f32x16){}); sc = mfma32(kf[0][1], qf[0][1], sc);
    u32x4 p0, p1;
#define EXP2(v) __builtin_amdgcn_exp2f(v)
    float ec, ed;
#define GRP(r, PREV, MF, CUR) do { float ea = EXP2(sc[r]), eb = EXP2(sc[(r) + 1]); asm volatile("" : "+v"(ea), "+v"(eb)); PREV; SB(); MF; SB(); \
      ec = EXP2(sc[(r) + 2]); ed = EXP2(sc[(r) + 3]); asm volatile("" : "+v"(ec), "+v"(ed)); { u32 pa = pk2(ea, eb); asm volatile("" : "+v"(pa)); CUR = pa; } SB(); } while (0)
#define PKP(D) do { u32 pb = pk2(ec, ed); asm volatile("" : "+v"(pb)); D = pb; } while (0)
    u32 pwb = lb + wave * 8192 + lane * 16, lwb = lb + L_LP + wave * 512 + l31 * 4; asm volatile("" : "+v"(pwb), "+v"(lwb));
#define WRP(t) do { _Pragma("unroll") for (int g = 0; g < 2; ++g) *(LAS u32x4*)(pwb + ((t) * 2 + g) * 1024) = (u32x4){pk2(o[t][8 * g], o[t][8 * g + 1]), pk2(o[t][8 * g + 2], o[t][8 * g + 3]), pk2(o[t][8 * g + 4], o[t][8 * g + 5]), pk2(o[t][8 * g + 6], o[t][8 * g + 7])}; \
      if (hi) *(LAS float*)(lwb + (t) * 128) = o[t][12]; } while (0)
    const int w6 = min(wave, 6);
    const u32 wo_base = OFF_WOT + (u32)(7 * ocq + w6) * 16384, wg_base = OFF_WGU + (u32)(7 * gfj + w6) * 28672;
    auto nslot = [](int kt, int t, bool) constexpr { const int sl = 4 * (kt - 2) + t;
      return (t == 0 && kt < 7 ? 2 : 0) + (t == 2 && kt < 6 ? 2 : 0) + (kt >= 2 ? (sl == 0) + (sl < 16) + (sl >= 6 && sl < 18) : 0); };
    auto nafter = [&](int k0, int t0, int k1, int t1, bool young) constexpr { int n = -2; for (int i = 4 * k0 + t0; i <= 4 * k1 + t1; ++i) n += nslot(i >> 2, i & 3, young); return n; };
#pragma unroll
    for (int kt = 0; kt < 8; ++kt) {
      if (kt == 0) __builtin_amdgcn_s_setprio(3);
      if (kt == 2) __builtin_amdgcn_s_setprio(2);
      if (kt == 4) __builtin_amdgcn_s_setprio(1);
      if (kt == 6 && wave < 4) __builtin_amdgcn_s_setprio(0);
      if (kt == 7) __builtin_amdgcn_s_setprio(0);
#pragma unroll
      for (int t = 0; t < 4; ++t) {
        f32x16 sn;
        const bool more = t < 3 || kt < 7, first = t == 0 && kt == 0;
        const int kn = t < 3 ? (kt & 1) : ((kt + 1) & 1), tn = (t + 1) & 3;
        if (t == 3 && kt < 7) {
          if (kt == 0) WAITKV(kf, 1, nafter(0, 0, 0, 2, false) + 2);
          else if (kt == 1) WAITKV(kf, 2, nafter(0, 2, 1, 2, false));
          else WAITKV(kf, kt + 1, nafter(kt - 1, 2, kt, 2, false));
        }
        GRP(0, if (!first) PKP(p1[3]), if (more) sn = mfma32(kf[kn][0], qf[tn][0], (f32x16){}), p0[0]);
        GRP(4, PKP(p0[1]), if (more) sn = mfma32(kf[kn][1], qf[tn][1], sn), p0[2]);
        GRP(8, PKP(p0[3]), if (t > 0) o[t - 1] = mfma32(vf[kt & 1][1], p1, o[t - 1]); else if (kt > 0) o[3] = mfma32(vf[(kt + 1) & 1][1], p1, o[3]), p1[0]);
        if (t == 0 && kt < 7) LDKV(vf, kt + 1, 2048);
        if (t == 2 && kt < 6) LDKV(kf, kt + 2, 0);
        if (kt == 1 && t == 0 && wave == 3) {
#pragma nounroll
          for (int i = 0; i < SPIN_CAP; ++i) {
            const u32 a = poll32(ws + OFF_PREPF + lane * 128), c = poll32(ws + OFF_PREPF + (64 + lane) * 128), d = poll32(ws + OFF_PREPF + (128 + lane) * 128), e = poll32(ws + OFF_PREPF + (192 + lane) * 128);
            if (__all(a == tag && c == tag && d == tag && e == tag)) break;
            __builtin_amdgcn_s_sleep(8);
          }
          CFENCE();
          if (lane == 0) __hip_atomic_fetch_add((LAS u32*)(lb + L_SYNC + 40), 1u, __ATOMIC_RELAXED, __HIP_MEMORY_SCOPE_WORKGROUP);
        }
        if (t == 0) {
          if (kt == 2) STAMPL(1, "kt2");
          if (kt == 4) STAMPL(2, "kt4");
          if (kt == 6) STAMPL(4, "kt6");
        }
        if (kt >= 2) {
          const int sl = 4 * (kt - 2) + t;
          if (sl == 0) {
            lds_wait(lb + L_SYNC + 40, 9);
            dma4_touch(ws + wg_base + 12288, ((slot & 3) * 32 + l31) * 128 + hi * 64, lb + L_ST + w6 * 256);
          }
          if (sl < 10) wo_f[sl] = ld128(wsr, lane * 16, wo_base + sl * 1024);
          else if (sl < 16) wgu[sl - 10] = ld128(wsr, lane * 16, wg_base + (sl - 10) * 1024);
          if (sl >= 6 && sl < 18) dma16<true>(ws + (sl < 12 ? wo_base + (4 + sl) * 1024 : wg_base + (sl - 6) * 1024), lane * 16, lb + L_PARK + w6 * 12288 + (sl - 6) * 1024);
        }
        if (t == 0) {
          if (kt == 0) WAITKV(vf, 0, nafter(0, 0, 0, 0, false) + 4);
          else if (kt == 1) WAITKV(vf, 1, nafter(0, 0, 1, 0, false));
          else WAITKV(vf, kt, nafter(kt - 1, 0, kt, 0, false));
        }
        SB();
        GRP(12, PKP(p1[1]), o[t] = mfma32(vf[kt & 1][0], p0, o[t]), p1[2]);
        if (kt == 7 && t > 0) { WRP(t - 1); SB(); }
        if (more) sc = sn;
      }
    }
    PKP(p1[3]);
    o[3] = mfma32(vf[1][1], p1, o[3]);
    WRP(3);
    STAMPL(13, "attn done");
    BAR();
    const int qt = wave >> 1, sf = wave & 1;
    u32x4 pa[8]; float pl[8];
    {
      u32 base = lb + qt * 2048 + sf * 1024 + lane * 16, lbase = lb + L_LP + qt * 128 + l31 * 4;
      asm volatile("" : "+v"(base), "+v"(lbase));
#pragma unroll
      for (int w = 0; w < 8; ++w) { pa[w] = lds128(base + w * 8192); pl[w] = lds32f(lbase + w * 512); }
      SB();
    }
    float a8[8]; u32 sel0 = 0x00003F80u, sel1 = 0x3F800000u; asm volatile("" : "+v"(sel0), "+v"(sel1));
#pragma unroll
    for (int j = 0; j < 4; ++j) {
      a8[2 * j] = bflo(pa[0][j]); a8[2 * j + 1] = bfhi(pa[0][j]);
#pragma unroll
      for (int w = 1; w < 8; ++w) { a8[2 * j] = dot2(pa[w][j], sel0, a8[2 * j]); a8[2 * j + 1] = dot2(pa[w][j], sel1, a8[2 * j + 1]); }
    }
    const float l = ((pl[0] + pl[4]) + (pl[1] + pl[5])) + ((pl[2] + pl[6]) + (pl[3] + pl[7]));
    const float rl = __builtin_amdgcn_rcpf(l);
    const u32x4 of = {pk2(a8[0] * rl, a8[1] * rl), pk2(a8[2] * rl, a8[3] * rl), pk2(a8[4] * rl, a8[5] * rl), pk2(a8[6] * rl, a8[7] * rl)};
    st128(wsr, (l31 >> 4) * 1024 + ((l31 & 15) + 16 * (2 * sf + hi)) * 16, OFF_OB + (u32)((4 * ai + qt) * 16 + ah) * 2048, of);
    WAIT_VM(0);
    if (lane == 0) flag32(ws + OFF_OF + (((4 * ai + qt) * 16 + ah) * 2 + sf) * 128, tag);
    BAR();
    STAMPL(14, "O flagged");
  }

  u32 uvp[14];
  const int otok = 16 * ((lane >> 4) & 1) + (lane & 15), ofe = 112 * ocq + 16 * min(wave, 6) + 8 * hi;
  if (wave < 7) {
    xres = *(const u32x4*)(x + (size_t)(32 * ort + otok) * 448 + ofe);
    gaA = ld128(wsr, ofe * 4, OFF_MODG + 448 * 8); gaB = ld128(wsr, ofe * 4 + 16, OFF_MODG + 448 * 8);
#pragma unroll
    for (int k = 0; k < 14; ++k) uvp[k] = ld32(wsr, lane * 4, OFF_UV + (u32)(k * 112 + 7 * gfj + wave) * 256);
#pragma unroll
    for (int s = 12; s < 28; ++s) wgu[s] = ld128(wsr, lane * 16 + (s & 3) * 1024, OFF_WGU + (u32)((7 * gfj + wave) * 28 + (s & ~3)) * 1024);
#pragma unroll
    for (int j = 0; j < 6; ++j) wo_f[10 + j] = lds128(lb + L_PARK + wave * 12288 + j * 1024 + lane * 16);
#pragma unroll
    for (int j = 6; j < 12; ++j) wgu[j] = lds128(lb + L_PARK + wave * 12288 + j * 1024 + lane * 16);
    lds_wait(lb + L_SYNC + 16, 1);
  } else {
    wait_flags(ws + OFF_OF + (ort * 32 + l31) * 128, tag);
    lds_st(lb + L_SYNC + 16, 1u);
  }
  STAMPL(15, "O flags seen");
  {
    const char* osrc = ws + OFF_OB + (size_t)(ort * 16 + 2 * wave) * 2048;
    dma16x4(osrc, lane * 16, lb + 4 * wave * 1024);
  }
  __builtin_amdgcn_s_waitcnt(0x0F70);
  CFENCE();
  BAR();
  STAMPL(16, "O landed");
  if (wave < 7) {
    f32x4 y0 = {0.f, 0.f, 0.f, 0.f}, y1 = y0;
    u32x4 oa[8];
#pragma unroll
    for (int i = 0; i < 6; ++i) oa[i] = lds128(lb + i * 1024 + lane * 16);
#pragma unroll
    for (int h = 0; h < 16; ++h) {
      if (h < 13) { oa[(2 * h + 6) & 7] = lds128(lb + (2 * h + 6) * 1024 + lane * 16); oa[(2 * h + 7) & 7] = lds128(lb + (2 * h + 7) * 1024 + lane * 16); }
      SB(); y0 = mfma16(wo_f[h], oa[(2 * h) & 7], y0); y1 = mfma16(wo_f[h], oa[(2 * h + 1) & 7], y1); SB();
    }
    float yv[8];
#pragma unroll
    for (int r = 0; r < 4; ++r) { auto sw = __builtin_amdgcn_permlane16_swap(__float_as_uint(y0[r]), __float_as_uint(y1[r]), false, false); yv[r] = __uint_as_float(sw[0]); yv[4 + r] = __uint_as_float(sw[1]); }
    const u32 gw[4] = {gaA[0], gaA[2], gaB[0], gaB[2]};
    u32x4 x2p; float s1 = 0.f, s2 = 0.f;
#pragma unroll
    for (int j = 0; j < 4; ++j) {
      x2p[j] = pk2(fmaf(bflo(gw[j]), yv[2 * j], bflo(xres[j])), fmaf(bfhi(gw[j]), yv[2 * j + 1], bfhi(xres[j])));
      s1 = dot2(x2p[j], 0x3F803F80u, s1); s2 = dot2(x2p[j], x2p[j], s2);
    }
    const u32 fo = (u32)(ort * 28 + 7 * ocq + wave) * 1024;
    st128(wsr, (otok + 32 * hi) * 16, OFF_X2 + fo, x2p);
    s1 = xhalf_sum(s1); s2 = xhalf_sum(s2);
    *(LAS float*)(lb + L_ST + wave * 256 + (hi * 32 + otok) * 4) = hi ? s2 : s1;
    WAIT_VM(0);
  }
  BAR();
  if (wave == 7) {
    float t = 0.f;
#pragma unroll
    for (int w = 0; w < 7; ++w) t += lds32f(lb + L_ST + w * 256 + lane * 4);
    const u32 nv = __builtin_amdgcn_update_dpp(0u, __float_as_uint(t), 0xB1, 0xf, 0xf, false);
    st128(wsr, !(lane & 1) ? (u32)lane * 8 : 0x80000000u, OFF_X2G + (u32)(ort * 4 + ocq) * 512, (u32x4){__float_as_uint(t), tag, nv, tag});
  } else {
    float uvs = 0.f;
#pragma unroll
    for (int k = 0; k < 14; ++k) uvs += __uint_as_float(uvp[k]);
    *(LAS float*)(lb + L_UV + wave * 256 + lane * 4) = uvs;
  }
  STAMPL(17, "x2 flagged");

  const int dsl = slot & 15, dq = 2 * (slot >> 4) + dsl / 7, dc = dsl % 7;
  const bool dact = dsl < 14;
  u32x4 wdf[28], x2r, gbA, gbB;
  if (wave == 7) {
    u64 g[16]; const char* gp = ws + OFF_X2G + (16 * gi * 64 + lane) * 8;
#pragma nounroll
    for (int i = 0; i < SPIN_CAP; ++i) {
      bool ok = true;
#pragma unroll
      for (int p = 0; p < 16; ++p) g[p] = poll64(gp + p * 512);
#pragma unroll
      for (int p = 0; p < 16; ++p) ok = ok && (u32)(g[p] >> 32) == tag;
      if (__all(ok)) break;
      __builtin_amdgcn_s_sleep(1);
    }
    CFENCE();
#pragma unroll
    for (int p = 0; p < 16; ++p) *(LAS u32*)(lb + L_SP2 + p * 256 + lane * 4) = (u32)g[p];
  }
  BAR();
  STAMPL(18, "x2 flags seen");
  {
    const char* asrc = ws + OFF_X2 + (size_t)(4 * gi) * 28 * 1024;
#pragma unroll
    for (int n = 0; n < 4; ++n) { const int g = min(wave + 8 * n, 27); dma16x4(asrc + g * 4096, lane * 16, lb + g * 4096); }
  }
  WAIT_VM(12);
  BAR();
  STAMPL(19, "A0 landed");
  const int dm = wave >> 2, dn = (wave >> 1) & 1, dgp = wave & 1;
  const u32 wdo = OFF_WDT + (u32)(2 * dc * 112 + 14 * wave) * 1024;
  if (wave < 7) {
    u32 sb = lb + L_SP2 + l31 * 4, ub = lb + L_UV + wave * 256 + hi * 16; asm volatile("" : "+v"(sb), "+v"(ub));
    u32 ab = lb + lane * 16, ab2 = ab + 57344; asm volatile("" : "+v"(ab), "+v"(ab2));
    f32x16 acc[2]; u32x4 af[4]; f32x4 uv[4]; float st[8], rs = 0.f, nm = 0.f, hv[8];
#define EP_RD(T) do { _Pragma("unroll") for (int c = 0; c < 4; ++c) { st[c] = lds32f(sb + ((T) * 4 + c) * 256); st[4 + c] = lds32f(sb + ((T) * 4 + c) * 256 + 128); } } while (0)
#define EP_UV(g) do { uv[0] = lds128f(ub + 32 * (g)); uv[1] = lds128f(ub + 32 * (g) + 64); uv[2] = lds128f(ub + 32 * (g) + 128); uv[3] = lds128f(ub + 32 * (g) + 192); } while (0)
#define EP_ST() do { float m = ((st[0] + st[1]) + (st[2] + st[3])) * (1.f / 448.f); asm volatile("" : "+v"(m)); const float e2 = (((st[4] + st[5]) + st[6]) + st[7]) * (1.f / 448.f); \
      rs = __builtin_amdgcn_rsqf(fmaxf(e2 - m * m, 0.f) + 1e-5f); nm = -m * rs; } while (0)
    float gt, up, ez;
    constexpr float K1 = -2.f * 0.7978845608028654f * 1.4426950408889634f, K3 = K1 * 0.044715f;
#define EP_A(A, e) do { const float tg = fma1(nm, uv[0][(e) & 3], uv[2][(e) & 3]), tu = fma1(nm, uv[1][(e) & 3], uv[3][(e) & 3]); gt = fma1(A[e], rs, tg); up = fma1(A[8 + (e)], rs, tu); asm volatile("" : "+v"(gt), "+v"(up)); } while (0)
#define EP_B() do { ez = __builtin_amdgcn_exp2f(gt * fmaf(gt * gt, K3, K1)); up *= gt; asm volatile("" : "+v"(ez), "+v"(up)); } while (0)
#define EP_C(e) do { hv[e] = up * __builtin_amdgcn_rcpf(1.f + ez); asm volatile("" : "+v"(hv[e])); } while (0)
#define EP_EL(A, e) do { EP_A(A, e); EP_B(); EP_C(e); } while (0)
#define EP_OUT(T) st128(wsr, lane * 16, OFF_HB + (u32)((4 * gi + (T)) * 112 + 7 * gfj + wave) * 1024, (u32x4){pk2(hv[0], hv[1]), pk2(hv[2], hv[3]), pk2(hv[4], hv[5]), pk2(hv[6], hv[7])})
#pragma unroll
    for (int i = 0; i < 3; ++i) af[i] = lds128(ab + i * 1024);
#pragma unroll
    for (int t = 0; t < 4; ++t) {
#pragma unroll
      for (int s = 0; s < 28; ++s) {
        const int n = t * 28 + s;
        if (n == 25) { STAMPL(28, "gu n25"); WAIT_VM(0); BAR();  }
        if (n + 3 < 112) af[(n + 3) & 3] = (n + 3 < 56) ? lds128(ab + (n + 3) * 1024) : lds128(ab2 + (n + 3 - 56) * 1024);
        SB();
        if (s == 0) acc[t & 1] = mfma32(wgu[s], af[n & 3], (f32x16){}); else acc[t & 1] = mfma32(wgu[s], af[n & 3], acc[t & 1]);
        SB();
        if (t > 0) {
          if (s == 0) { EP_RD(t - 1); EP_UV(0); }
          if (s == 2) EP_ST();
          if (s >= 3 && s < 27) { if (s % 3 == 0) EP_A(acc[(t - 1) & 1], s / 3 - 1); else if (s % 3 == 1) EP_B(); else EP_C(s / 3 - 1); }
          if (s == 13) EP_UV(1);
          if (s == 27) EP_OUT(t - 1);
          if (s == 27 && t == 1) STAMPL(20, "gu tile1");
          if (s == 27 && t == 2) STAMPL(21, "gu tile2");
          if (s == 27 && t == 3) STAMPL(22, "gu tile3");
          if (s % 4 == 1 && s < 24) { const int f = 6 * (t - 1) + s / 4; wdf[f] = ld128(wsr, lane * 16, wdo + (u32)((f & 1) * 112 + (f >> 1)) * 1024); }
          SB();
        }
      }
    }
    asm volatile("s_nop 11" : "+v"(acc[1]));
    EP_RD(3); EP_UV(0); EP_ST();
    {
      f32x4 uw[4];
      uw[0] = lds128f(ub + 32); uw[1] = lds128f(ub + 96); uw[2] = lds128f(ub + 160); uw[3] = lds128f(ub + 224);
      float g8[8], u8[8];
#pragma unroll
      for (int e = 0; e < 8; ++e) {
        const f32x4* W = e < 4 ? uv : uw;
        g8[e] = fma1(acc[1][e], rs, fma1(nm, W[0][e & 3], W[2][e & 3])); u8[e] = fma1(acc[1][8 + e], rs, fma1(nm, W[1][e & 3], W[3][e & 3]));
      }
#pragma unroll
      for (int e = 0; e < 8; ++e) hv[e] = (g8[e] * u8[e]) * __builtin_amdgcn_rcpf(1.f + __builtin_amdgcn_exp2f(g8[e] * fmaf(g8[e] * g8[e], K3, K1)));
    }
    EP_OUT(3);
    WAIT_VM(0);
    if (lane == 0) flag32(ws + OFF_HF + ((gi * 16 + gfj) * 8 + wave) * 128, tag);
  } else {
    WAIT_VM(0); BAR();
#pragma unroll
    for (int f = 0; f < 18; ++f) wdf[f] = ld128(wsr, lane * 16, wdo + (u32)((f & 1) * 112 + (f >> 1)) * 1024);
  }
  STAMPL(23, "h flagged");
  if (dact) {
    BAR();
#pragma unroll
    for (int f = 18; f < 28; ++f) wdf[f] = ld128(wsr, lane * 16, wdo + (u32)((f & 1) * 112 + (f >> 1)) * 1024);
    const int drt = 8 * xg + 2 * dq, dcol = 64 * dc + 32 * dn + 16 * dgp + 8 * hi;
    gbA = ld128(wsr, dcol * 4, OFF_MODG + 1120 * 8); gbB = ld128(wsr, dcol * 4 + 16, OFF_MODG + 1120 * 8);
    wait_flags(ws + OFF_HF + (((drt >> 2) * 16 + 2 * wave + ((lane >> 3) & 1)) * 8 + min(lane & 7, 6)) * 128, tag);
    STAMPL(24, "h flags seen");
    u32x4 hf[20];
#define LDH(i) hf[(i) % 20] = ld128(wsr, lane * 16 + (((i) % 14) & 3) * 1024, OFF_HB + (u32)((drt + (i) / 14) * 112 + 14 * wave + (((i) % 14) & ~3)) * 1024)
#pragma unroll
    for (int i = 0; i < 20; ++i) { LDH(i); SB(); }
    x2r = ld128(wsr, lane * 16, OFF_X2 + (u32)((drt + dm) * 28 + 4 * dc + 2 * dn + dgp) * 1024);
    f32x16 c0, c1;
    u32 pwb = lb + wave * 16384 + lane * 16; asm volatile("" : "+v"(pwb));
#pragma unroll
    for (int i = 0; i < 28; ++i) {
      const int m = i / 14, j = i % 14;
      SB();
      if (j == 0) { c0 = mfma32(wdf[0], hf[i % 20], (f32x16){}); c1 = mfma32(wdf[1], hf[i % 20], (f32x16){}); }
      else { c0 = mfma32(wdf[2 * j], hf[i % 20], c0); c1 = mfma32(wdf[2 * j + 1], hf[i % 20], c1); }
      SB();
      if (i < 8) LDH(i + 20);
      if (j == 13) {
#pragma unroll
        for (int g = 0; g < 4; ++g) {
          *(LAS f32x4*)(pwb + ((2 * m) * 4 + g) * 1024) = (f32x4){c0[4 * g], c0[4 * g + 1], c0[4 * g + 2], c0[4 * g + 3]};
          *(LAS f32x4*)(pwb + ((2 * m + 1) * 4 + g) * 1024) = (f32x4){c1[4 * g], c1[4 * g + 1], c1[4 * g + 2], c1[4 * g + 3]};
        }
      }
    }
    STAMPL(25, "down mfma done");
    BAR();
    f32x4 pa[8], pb[8];
    {
      u32 base = lb + (2 * dm + dn) * 4096 + dgp * 2048 + lane * 16, base2 = base + 65536;
      asm volatile("" : "+v"(base), "+v"(base2));
#pragma unroll
      for (int w = 0; w < 4; ++w) {
        pa[w] = lds128f(base + w * 16384); pb[w] = lds128f(base + w * 16384 + 1024);
        pa[4 + w] = lds128f(base2 + w * 16384); pb[4 + w] = lds128f(base2 + w * 16384 + 1024);
      }
      SB();
    }
    const f32x4 a0 = ((pa[0] + pa[4]) + (pa[1] + pa[5])) + ((pa[2] + pa[6]) + (pa[3] + pa[7])), a1 = ((pb[0] + pb[4]) + (pb[1] + pb[5])) + ((pb[2] + pb[6]) + (pb[3] + pb[7]));
    float yv[8];
#pragma unroll
    for (int r = 0; r < 4; ++r) { auto sw = __builtin_amdgcn_permlane32_swap(__float_as_uint(a0[r]), __float_as_uint(a1[r]), false, false); yv[r] = __uint_as_float(sw[0]); yv[4 + r] = __uint_as_float(sw[1]); }
    const u32 gw[4] = {gbA[0], gbA[2], gbB[0], gbB[2]};
    u32x4 ov;
#pragma unroll
    for (int j = 0; j < 4; ++j) ov[j] = pk2(fmaf(bflo(gw[j]), yv[2 * j], bflo(x2r[j])), fmaf(bfhi(gw[j]), yv[2 * j + 1], bfhi(x2r[j])));
    *(u32x4*)(out + (size_t)(32 * (drt + dm) + l31) * 448 + dcol) = ov;
    if (b == 0 && tid == 0) *(GAS u32*)ws = tag;
  }
  STAMPL(26, "end");
}

extern "C" void kernel_launch(void* const* d_in, const int* in_sizes, int n_in, void* d_out, int out_size, void* d_ws, size_t ws_size, hipStream_t stream) {
  fused_block<<<256, 512, 0, stream>>>((const u16*)d_in[0], (const u16*)d_in[1], (const u16*)d_in[2], (const u16*)d_in[4], (const u16*)d_in[5],
                                       (const u16*)d_in[6], (const u16*)d_in[7], (const u16*)d_in[8], (u16*)d_out, (char*)d_ws);
}
```

```cpp
#define PROBE 26
#define PROBE_MUL 0
#include <hip/hip_runtime.h>
#include <stdint.h>

typedef unsigned short u16;
typedef unsigned u32;
typedef unsigned long long u64;
typedef __attribute__((ext_vector_type(2))) unsigned u32x2;
typedef __attribute__((ext_vector_type(4))) unsigned u32x4;
typedef __attribute__((ext_vector_type(2))) float f32x2;
typedef __attribute__((ext_vector_type(4))) float f32x4;
typedef __attribute__((ext_vector_type(16))) float f32x16;
typedef __attribute__((ext_vector_type(2))) __bf16 bf16x2;
typedef __attribute__((ext_vector_type(8))) __bf16 bf16x8;
typedef __attribute__((ext_vector_type(4))) short s16x4;
#define DEV __device__ __forceinline__
#define GAS __attribute__((address_space(1)))
#define LAS __attribute__((address_space(3)))
#define RLX_AGENT __ATOMIC_RELAXED, __HIP_MEMORY_SCOPE_AGENT

constexpr u32 OFF_MODG = 4096;
constexpr u32 OFF_X2G = 65536, OFF_UV = 13467648  , OFF_WQT = 524288, OFF_WOT = 1900544, OFF_WGU = 2359296, OFF_WDT = 5570560;
constexpr u32 OFF_KV = 7176192, OFF_OB = 11370496, OFF_X2 = 15302656, OFF_HB = 17137664, OFF_XH = 24477696;
constexpr u32 OFF_XF = 26312704, OFF_WQF = OFF_XF + 512 * 128, OFF_KVG = OFF_WQF + 1024 * 128, OFF_OF = OFF_KVG + 1024 * 128, OFF_HF = OFF_OF + 2048 * 128, OFF_PREPF = OFF_HF + 2048 * 128, WS_USED = OFF_PREPF + 256 * 128;
constexpr u32 OFF_STAMP = 29360128;
constexpr u32 L_STG = 90112  , L_Q = 131072, L_GAM = 139264, L_QN = 142848;
constexpr u32 L_PARK = 64512 + 1024  ;
constexpr u32 L_SP2 = 151552  , L_MODP = L_SP2, L_MX = L_SP2, L_LP = L_SP2, L_UV = 155648  , L_ST = 157696  , L_G2 = 159488  ;
constexpr u32 L_SYNC = 160000;
constexpr u32 LDS_BYTES = 163840;
constexpr int CODE_BYTES = 38000;
constexpr int SPIN_CAP = 1 << 15;
#define FIRST_POLL 280
#define KV_SLEEP 1
#define PREP_START 220
#define POLL_T 0
#define POLL_SLEEP 3

DEV float bflo(u32 w) { return __uint_as_float(w << 16); }
DEV float bfhi(u32 w) { return __uint_as_float(w & 0xffff0000u); }
DEV u32 pk2(float a, float b) { f32x2 v = {a, b}; return __builtin_bit_cast(u32, __builtin_convertvector(v, bf16x2)); }
DEV float rbf(float a) { return bflo(pk2(a, 0.f)); }
DEV float fma1(float a, float b, float c) { float r; asm("v_fma_f32 %0, %1, %2, %3" : "=v"(r) : "v"(a), "v"(b), "v"(c)); return r; }
DEV float dot2(u32 a, u32 b, float c) { return __builtin_amdgcn_fdot2_f32_bf16(__builtin_bit_cast(bf16x2, a), __builtin_bit_cast(bf16x2, b), c, false); }
DEV f32x16 mfma32(u32x4 a, u32x4 b, f32x16 c) { return __builtin_amdgcn_mfma_f32_32x32x16_bf16(__builtin_bit_cast(bf16x8, a), __builtin_bit_cast(bf16x8, b), c, 0, 0, 0); }
DEV f32x4 mfma16(u32x4 a, u32x4 b, f32x4 c) { return __builtin_amdgcn_mfma_f32_16x16x32_bf16(__builtin_bit_cast(bf16x8, a), __builtin_bit_cast(bf16x8, b), c, 0, 0, 0); }
DEV float xhalf_sum(float v) { auto r = __builtin_amdgcn_permlane32_swap(__float_as_uint(v), __float_as_uint(v), false, false); return __uint_as_float(r[0]) + __uint_as_float(r[1]); }
DEV float xhalf_max(float v) { auto r = __builtin_amdgcn_permlane32_swap(__float_as_uint(v), __float_as_uint(v), false, false); return fmaxf(__uint_as_float(r[0]), __uint_as_float(r[1])); }
#define DPPF(v, ctrl) __uint_as_float(__builtin_amdgcn_update_dpp(0u, __float_as_uint(v), ctrl, 0xf, 0xf, false))
DEV float wave_max(float v) {
  v = fmaxf(v, DPPF(v, 0x121)); v = fmaxf(v, DPPF(v, 0x122)); v = fmaxf(v, DPPF(v, 0x124)); v = fmaxf(v, DPPF(v, 0x128));
  auto r = __builtin_amdgcn_permlane16_swap(__float_as_uint(v), __float_as_uint(v), false, false);
  return xhalf_max(fmaxf(__uint_as_float(r[0]), __uint_as_float(r[1])));
}
#define WAIT_VM(n) asm volatile("s_waitcnt vmcnt(" #n ")" ::: "memory")
#define BAR() asm volatile("s_waitcnt lgkmcnt(0)\n\ts_barrier" ::: "memory")
#define SB() __builtin_amdgcn_sched_barrier(0)
#define CFENCE() asm volatile("" ::: "memory")
template <bool SC1> DEV void dma16(const void* sbase, u32 voff, u32 lds_dst) {
  u32 keep;
  if (SC1) asm volatile("s_mov_b32 %0, m0\n\ts_mov_b32 m0, %3\n\ts_nop 2\n\tglobal_load_lds_dwordx4 %1, %2 sc1\n\ts_mov_b32 m0, %0" : "=&s"(keep) : "v"(voff), "s"(sbase), "s"(lds_dst) : "memory");
  else asm volatile("s_mov_b32 %0, m0\n\ts_mov_b32 m0, %3\n\ts_nop 2\n\tglobal_load_lds_dwordx4 %1, %2\n\ts_mov_b32 m0, %0" : "=&s"(keep) : "v"(voff), "s"(sbase), "s"(lds_dst) : "memory");
}
DEV void dma16x4(const void* sbase, u32 voff, u32 lds_dst) {
  u32 keep;
  asm volatile("s_mov_b32 %0, m0\n\ts_mov_b32 m0, %3\n\ts_nop 2\n\tglobal_load_lds_dwordx4 %1, %2 sc1\n\tglobal_load_lds_dwordx4 %1, %2 offset:1024 sc1\n\t"
               "global_load_lds_dwordx4 %1, %2 offset:2048 sc1\n\tglobal_load_lds_dwordx4 %1, %2 offset:3072 sc1\n\ts_mov_b32 m0, %0" : "=&s"(keep) : "v"(voff), "s"(sbase), "s"(lds_dst) : "memory");
}
DEV void dma4_touch(const void* sbase, u32 voff, u32 lds_dst) {
  u32 keep;
  asm volatile("s_mov_b32 %0, m0\n\ts_mov_b32 m0, %3\n\ts_nop 2\n\tglobal_load_lds_dword %1, %2 sc1\n\ts_mov_b32 m0, %0" : "=&s"(keep) : "v"(voff), "s"(sbase), "s"(lds_dst) : "memory");
}
DEV u32x4 lds128(u32 a) { return *(const LAS u32x4*)a; }
DEV f32x4 lds128f(u32 a) { return *(const LAS f32x4*)a; }
DEV float lds32f(u32 a) { return *(const LAS float*)a; }
DEV u32x2 ldstr(u32 a) { return __builtin_bit_cast(u32x2, __builtin_amdgcn_ds_read_tr16_b64_v4i16((LAS s16x4*)a)); }
DEV u32x4 cat(u32x2 a, u32x2 b) { return (u32x4){a.x, a.y, b.x, b.y}; }
typedef __amdgpu_buffer_rsrc_t rsrc_t;
DEV u32x4 ld128(rsrc_t r, u32 voff, u32 soff) { return __builtin_amdgcn_raw_buffer_load_b128(r, voff, soff, 16); }
DEV u32 ld32(rsrc_t r, u32 voff, u32 soff) { return __builtin_amdgcn_raw_buffer_load_b32(r, voff, soff, 16); }
DEV void st128(rsrc_t r, u32 voff, u32 soff, u32x4 v) {
  __builtin_amdgcn_raw_buffer_store_b128(v, r, voff, soff, 16);
  asm volatile("s_nop 1" :: "v"(v) : "memory");
}
DEV void st32(rsrc_t r, u32 voff, u32 soff, u32 v) { __builtin_amdgcn_raw_buffer_store_b32(v, r, voff, soff, 16); }
DEV u32 poll32(const char* p) { return __hip_atomic_load((GAS u32*)p, RLX_AGENT); }
DEV u64 poll64(const char* p) { return __hip_atomic_load((GAS u64*)p, RLX_AGENT); }
DEV void flag32(char* p, u32 v) { __hip_atomic_store((GAS u32*)p, v, RLX_AGENT); }
DEV void flag64(char* p, u32 val, u32 tag) { __hip_atomic_store((GAS u64*)p, ((u64)tag << 32) | val, RLX_AGENT); }
DEV void wait_flags(const char* p, u32 tag) {
#pragma nounroll
  for (int i = 0; i < SPIN_CAP; ++i) { if (__all(poll32(p) == tag)) break; __builtin_amdgcn_s_sleep(POLL_SLEEP); }
  CFENCE();
}
DEV u32 wait_gran(const char* p, u32 tag) {
  u64 g = 0;
#pragma nounroll
  for (int i = 0; i < SPIN_CAP; ++i) { g = poll64(p); if (__all((u32)(g >> 32) == tag)) break; __builtin_amdgcn_s_sleep(POLL_SLEEP); }
  CFENCE();
  return (u32)g;
}
DEV u32 lds_ld(u32 a) { return __hip_atomic_load((LAS u32*)a, __ATOMIC_RELAXED, __HIP_MEMORY_SCOPE_WORKGROUP); }
DEV void lds_st(u32 a, u32 v) { __hip_atomic_store((LAS u32*)a, v, __ATOMIC_RELAXED, __HIP_MEMORY_SCOPE_WORKGROUP); }
DEV u32 lds_inc(u32 a) { return __builtin_amdgcn_readfirstlane(__hip_atomic_fetch_add((LAS u32*)a, 1u, __ATOMIC_RELAXED, __HIP_MEMORY_SCOPE_WORKGROUP)); }
DEV void lds_wait(u32 a, u32 want) {
#pragma nounroll
  for (int i = 0; i < (SPIN_CAP << 6); ++i) { if (__builtin_amdgcn_readfirstlane(lds_ld(a)) >= want) break; __builtin_amdgcn_s_sleep(1); }
  CFENCE();
}
#define WAIT_LGKM() asm volatile("s_waitcnt lgkmcnt(0)" ::: "memory")
#define STAMP(i) do {} while (0)
#undef STAMP
#define STAMP(i) do { if (((i) == 0 || (i) == PROBE) && wave == 0 && lane == 0) __hip_atomic_store((GAS u64*)(ws + OFF_STAMP) + b * 2 + ((i) != 0 || PROBE == 0) , (u64)__builtin_amdgcn_s_memrealtime(), RLX_AGENT); \
   if ((i) == 0 && PROBE == 0 && wave == 0 && lane == 0) __hip_atomic_store((GAS u64*)(ws + OFF_STAMP) + b * 2, (u64)__builtin_amdgcn_s_memrealtime(), RLX_AGENT); } while (0)
__global__ void probe_spin(char* ws) {
  __shared__ u64 mn[256], mx[256];
  const u64* p = (const u64*)(ws + OFF_STAMP);
  mn[threadIdx.x] = p[threadIdx.x * 2]; mx[threadIdx.x] = p[threadIdx.x * 2 + 1];
  __syncthreads();
  if (threadIdx.x == 0) {
    u64 a = ~0ull, c = 0; for (int i = 0; i < 256; ++i) { a = mn[i] < a ? mn[i] : a; c = mx[i] > c ? mx[i] : c; }
    u64 d = (c - a) * PROBE_MUL; if (d > 20000) d = 20000;
    const u64 t0 = __builtin_amdgcn_s_memrealtime();
    while (__builtin_amdgcn_s_memrealtime() - t0 < d) __builtin_amdgcn_s_sleep(1);
  }
}
#define STAMPL(i, s) STAMP(i)
#define GATE_MOD lds_wait(lb + L_SYNC + 36, 1)

__global__ __launch_bounds__(512, 2) void fused_block(const u16* __restrict__ x, const u16* __restrict__ wqkv, const u16* __restrict__ wo,
                                                      const u16* __restrict__ wg, const u16* __restrict__ wu, const u16* __restrict__ wd,
                                                      const u16* __restrict__ cvec, const u16* __restrict__ wmod, u16* __restrict__ out, char* ws) {
  __shared__ __attribute__((aligned(16))) char smem[LDS_BYTES];
  const int b = blockIdx.x, tid = threadIdx.x, lane = tid & 63;
  const int wave = __builtin_amdgcn_readfirstlane(tid >> 6);
  const int l31 = lane & 31, hi = lane >> 5;
  const int xg = b & 7, slot = b >> 3;
  const int ai = 4 * (xg >> 1) + (slot & 3), ah = 8 * (xg & 1) + (slot >> 2);
  const u32 lb = (u32)(uintptr_t)(LAS char*)smem;
  const rsrc_t wsr = __builtin_amdgcn_make_buffer_rsrc(ws, 0, WS_USED, 0x00020000);
  STAMP(0);
  {
    u64 pc; asm volatile("s_getpc_b64 %0" : "=s"(pc));
    if (wave == 0) {
      const u32 off = min((u32)(slot * 1280 + min(lane, 9) * 128), (u32)CODE_BYTES);
      u32 keep;
      asm volatile("s_mov_b32 %0, m0\n\ts_mov_b32 m0, %3\n\ts_nop 2\n\tglobal_load_lds_dword %1, %2\n\ts_mov_b32 m0, %0" : "=&s"(keep) : "v"(off), "s"(pc), "s"(lb + L_ST) : "memory");
    }
  }
  if (wave == 1) {
    const u32 off = min(lane, 12) << 21;
    u32 keep;
    asm volatile("s_mov_b32 %0, m0\n\ts_mov_b32 m0, %3\n\ts_nop 2\n\tglobal_load_lds_dword %1, %2\n\ts_mov_b32 m0, %0" : "=&s"(keep) : "v"(off), "s"(ws), "s"(lb + L_ST + 256) : "memory");
  }
  if (wave == 2 && lane < 16) lds_st(lb + L_SYNC + lane * 4, 0u);
  const u64 t_entry = __builtin_amdgcn_s_memrealtime();
  const u32 epoch_v = poll32(ws);
  u32 tag;

  f32x16 accQ, accK, accV;
  const int tq4 = (lane & 15) >> 2, tp4 = lane & 3, grp = lane >> 4;
  if (b < 42) {
    u32x4 wv[7];
    const u32 cv = cvec[56 * wave + min(lane, 55)];
#pragma unroll
    for (int n = 0; n < 7; ++n) wv[n] = *(const u32x4*)((const char*)wmod + (size_t)(56 * wave + 8 * n + (lane >> 3)) * 5376 + 128 * b + 16 * (lane & 7));
    BAR();
    const float cf = bflo(cv);
    const float sl = rbf(cf * __builtin_amdgcn_rcpf(1.f + __builtin_amdgcn_exp2f(-1.4426950408889634f * cf)));
    float acc[8] = {0.f, 0.f, 0.f, 0.f, 0.f, 0.f, 0.f, 0.f};
#pragma unroll
    for (int n = 0; n < 7; ++n) {
      const float sn = __uint_as_float(__builtin_amdgcn_ds_bpermute((8 * n + (lane >> 3)) * 4, __float_as_uint(sl)));
#pragma unroll
      for (int j = 0; j < 4; ++j) { acc[2 * j] = fmaf(sn, bflo(wv[n][j]), acc[2 * j]); acc[2 * j + 1] = fmaf(sn, bfhi(wv[n][j]), acc[2 * j + 1]); }
    }

#pragma unroll
    for (int j = 0; j < 8; ++j) acc[j] += DPPF(acc[j], 0x128);
#pragma unroll
    for (int j = 0; j < 4; ++j) { auto r = __builtin_amdgcn_permlane16_swap(__float_as_uint(acc[j]), __float_as_uint(acc[j + 4]), false, false); acc[j] = __uint_as_float(r[0]) + __uint_as_float(r[1]); }
#pragma unroll
    for (int j = 0; j < 2; ++j) { auto r = __builtin_amdgcn_permlane32_swap(__float_as_uint(acc[j]), __float_as_uint(acc[j + 2]), false, false); acc[j] = __uint_as_float(r[0]) + __uint_as_float(r[1]); }
    if (!(lane & 8)) *(LAS f32x2*)(lb + L_MODP + (wave * 64 + 8 * (lane & 7) + 2 * hi + 4 * (grp & 1)) * 4) = (f32x2){acc[0], acc[1]};
    WAIT_LGKM();
    u32 arrived = 0;
    if (lane == 0) arrived = __hip_atomic_fetch_add((LAS u32*)(lb + L_SYNC + 12), 1u, __ATOMIC_RELAXED, __HIP_MEMORY_SCOPE_WORKGROUP);
    if (__builtin_amdgcn_readfirstlane(arrived) == 7) {
      CFENCE();
      f32x2 t = {0.f, 0.f};
#pragma unroll
      for (int p = 0; p < 8; ++p) t += *(const LAS f32x2*)(lb + L_MODP + (p * 64 + 2 * l31) * 4);
      const u32 pv = pk2(t[0], t[1]), nv = __builtin_amdgcn_update_dpp(0u, pv, 0xB1, 0xf, 0xf, false);
      st128(wsr, (lane < 32 && !(lane & 1)) ? (u32)(32 * b + lane) * 8 : 0x80000000u, OFF_MODG, (u32x4){pv, epoch_v + 1u, nv, epoch_v + 1u});
      WAIT_VM(0);
      lds_st(lb + L_SYNC + 32, 1u);
    }
  } else if (wave < 3 && 3 * (b - 42) + wave < 512) {
    const int xgp = 3 * (b - 42) + wave, row = 4 * xgp + (lane & 3), c0i = lane >> 2;
    u32x4 xr[4];
#pragma unroll
    for (int n = 0; n < 4; ++n) xr[n] = *(const u32x4*)((const char*)x + (size_t)row * 896 + min(c0i + 16 * n, 55) * 16);
    BAR();
    if (c0i >= 8) xr[3] = (u32x4){0u, 0u, 0u, 0u};
    float s1[2] = {0.f, 0.f}, s2[2] = {0.f, 0.f};
#pragma unroll
    for (int n = 0; n < 4; ++n)
#pragma unroll
      for (int j = 0; j < 4; ++j) { s1[j & 1] = dot2(xr[n][j], 0x3F803F80u, s1[j & 1]); s2[j & 1] = dot2(xr[n][j], xr[n][j], s2[j & 1]); }
    float t1 = s1[0] + s1[1], t2 = s2[0] + s2[1];
    t1 += DPPF(t1, 0x124); t2 += DPPF(t2, 0x124); t1 += DPPF(t1, 0x128); t2 += DPPF(t2, 0x128);
    { auto r = __builtin_amdgcn_permlane16_swap(__float_as_uint(t1), __float_as_uint(t2), false, false); t1 = __uint_as_float(r[0]) + __uint_as_float(r[1]); }
    t1 = xhalf_sum(t1);
    { auto r = __builtin_amdgcn_permlane16_swap(__float_as_uint(t1), __float_as_uint(t1), false, false); t1 = __uint_as_float(r[0]); t2 = __uint_as_float(r[1]); }
    const float mean = t1 * (1.f / 448.f), ex2 = t2 * (1.f / 448.f);
    const float rstd = __builtin_amdgcn_rsqf(fmaxf(ex2 - mean * mean, 0.f) + 1e-5f), c0 = -mean * rstd;
    asm volatile("" :: "v"(c0));

#pragma unroll
    for (int n = 0; n < 4; ++n) {
      u32x4 o;
#pragma unroll
      for (int j = 0; j < 4; ++j) asm("v_cvt_pk_f16_f32 %0, %1, %2" : "=v"(o[j]) : "v"(fmaf(bflo(xr[n][j]), rstd, c0)), "v"(fmaf(bfhi(xr[n][j]), rstd, c0)));
      const u32 c = c0i + 16 * n;
      st128(wsr, c < 56 ? (c >> 1) * 1024 + ((row & 31) + 32 * (c & 1)) * 16 + (u32)(row >> 5) * 28672 : 0x80000000u, OFF_XH, o);
    }
    WAIT_VM(0);
    if (lane == 0) flag32(ws + OFF_XF + xgp * 128, epoch_v + 1u);
  } else if ((wave == 4 || wave == 5) && b < 210) {
    const int u = b - 42, part = u / 56, rem = u - part * 56, hp = rem / 7, kg = rem - hp * 7, s0 = 4 * kg + 2 * (wave - 4);
    const char* src = (const char*)wqkv + (size_t)(16 * s0) * 2688 + part * 896 + hp * 112;
    const u32 stg = lb + L_Q + (wave - 4) * 4096;
#pragma unroll
    for (int i = 0; i < 4; ++i) { const u32 c = lane & 7; dma16<false>(src + i * 8 * 2688, (lane >> 3) * 2688 + 16 * (c == 7 ? 6 : c), stg + i * 1024); }
    BAR();
    WAIT_VM(0);

#pragma unroll
    for (int e = 0; e < 4; ++e) {
      const u32 a = stg + (16 * (e >> 1) + 8 * (grp >> 1) + tq4) * 128 + (e & 1) * 56 + (grp & 1) * 32 + tp4 * 8;
      u32x4 f = cat(ldstr(a), ldstr(a + 512));
      if (l31 >= 28) f = (u32x4){0u, 0u, 0u, 0u};
      st128(wsr, lane * 16, OFF_WQT + (u32)((2 * hp + (e & 1)) * 84 + 3 * (s0 + (e >> 1)) + part) * 1024, f);
    }
    WAIT_VM(0);
    if (lane < 2) flag32(ws + OFF_WQF + ((2 * hp + lane) * 64 + 3 * (s0 >> 1) + part) * 128, epoch_v + 1u);
  } else {
    BAR();
  }
  tag = __builtin_amdgcn_readfirstlane(epoch_v) + 1u;
  STAMPL(7, "unit done");
  const int cg = b / 7, kg = b - cg * 7;
  if (wave >= 6) {
#pragma nounroll
    for (int i = 0; i < 4096 && __builtin_amdgcn_s_memrealtime() - t_entry < PREP_START; ++i) __builtin_amdgcn_s_sleep(1);
#pragma unroll
    for (int ph = 0; ph < 2; ++ph) {
      const int pw = wave & 1, tw = 2 * ph + pw, p = 4 * b + tw;
      const u32 stg = lb + L_STG + tw * 6144;
      if (b < 196) {
#pragma unroll
        for (int i = 0; i < 2; ++i) {
          const u32 vo = (u32)(64 * kg + 32 * ph + 16 * i + (lane >> 2)) * 3584 + (64 * cg + 32 * pw) * 2 + 16 * (lane & 3);
          dma16<false>(wg, vo, stg + (2 * i) * 1024); dma16<false>(wu, vo, stg + (2 * i + 1) * 1024);
        }
      }
      if (p < 784) {
        const int ds_ = p / 7, np = p - ds_ * 7;
#pragma unroll
        for (int i = 0; i < 2; ++i) dma16<false>(wd, (u32)(16 * ds_ + (lane >> 2)) * 896 + np * 128 + i * 64 + 16 * (lane & 3), stg + (4 + i) * 1024);
      } else if (p < 1008) {
        const int q = p - 784, h = q / 14, nq = q - h * 14;
#pragma unroll
        for (int i = 0; i < 2; ++i) dma16<false>(wo, (u32)min(28 * h + 16 * i + (lane >> 2), 447) * 896 + nq * 64 + 16 * (lane & 3), stg + (4 + i) * 1024);
      }
    }
    STAMPL(6, "prep issued");
    WAIT_VM(0);
    if (lane == 0) __hip_atomic_fetch_add((LAS u32*)(lb + L_SYNC + 52), 1u, __ATOMIC_RELAXED, __HIP_MEMORY_SCOPE_WORKGROUP);
    STAMPL(5, "prep landed");
  } else if (wave >= 3) {
#pragma nounroll
    for (int i = 0; i < 4096 && __builtin_amdgcn_s_memrealtime() - t_entry < FIRST_POLL; ++i) __builtin_amdgcn_s_sleep(1);
  }

  if (wave < 4) {
    if (wave == 3) { wait_flags(ws + OFF_XF + (32 * ai + l31) * 128, tag); GATE_MOD; lds_st(lb + L_SYNC, 1u); }
    else lds_wait(lb + L_SYNC, 1);

    STAMPL(3, "xhat go");
    u32x4 xf[28], pf[6]; u32 pfo[6], uvo[2]; float uvv[2];
    constexpr int XPRE = 12;
#define LDX(s) xf[s] = ld128(wsr, lane * 16 + ((s) & 3) * 1024, OFF_XH + (u32)((4 * ai + wave) * 28 + ((s) & ~3)) * 1024)
#pragma unroll
    for (int s = 0; s < XPRE; ++s) { LDX(s); SB(); }
    {
      const int pw = wave & 1, ph = (wave >> 1) & 1, p = 4 * b + wave;
      const u32 stg = lb + L_STG + wave * 6144;
      const bool isd = p < 784;
      const int ds_ = p / 7, np = p - ds_ * 7, q = p - 784, h = q / 14, nq = q - h * 14;
      lds_wait(lb + L_SYNC + 52, 2);
      const int pi = ((tp4 & 1) << 1) | (tp4 >> 1);
      float uu[2] = {0.f, 0.f}, vv[2] = {0.f, 0.f};
#pragma unroll
      for (int i = 0; i < 2; ++i) {
        const u32x4 be = lds128(lb + L_G2 + (2 * ph + i) * 32 + hi * 16), ga = lds128(lb + L_G2 + 128 + (2 * ph + i) * 32 + hi * 16);
#pragma unroll
        for (int e = 0; e < 2; ++e) {
          const u32 a = stg + (2 * i + (grp & 1)) * 1024 + (8 * (grp >> 1) + tq4) * 64 + e * 32 + pi * 8;
          u32x4 f = cat(ldstr(a), ldstr(a + 256));
#pragma unroll
          for (int j = 0; j < 4; ++j) {
            const float w0 = bflo(f[j]), w1 = bfhi(f[j]);
            vv[e] = fma1(w0, bflo(be[j]), vv[e]); vv[e] = fma1(w1, bfhi(be[j]), vv[e]);
            f[j] = pk2(w0 * bflo(ga[j]), w1 * bfhi(ga[j]));
            uu[e] += bflo(f[j]); uu[e] += bfhi(f[j]);
          }
          pf[2 * i + e] = f;
          pfo[2 * i + e] = b < 196 ? OFF_WGU + (u32)((4 * cg + 2 * pw + e) * 28 + 4 * kg + 2 * ph + i) * 1024 : 0x80000000u;
        }
      }
#pragma unroll
      for (int e = 0; e < 2; ++e) {
        const float us = xhalf_sum(uu[e]), vs = xhalf_sum(vv[e]);
        uvv[e] = hi ? vs : us; uvo[e] = b < 196 ? OFF_UV + (u32)((2 * kg + ph) * 112 + 4 * cg + 2 * pw + e) * 256 : 0x80000000u;
      }
#pragma unroll
      for (int i = 0; i < 2; ++i) {
        const u32 a = isd ? stg + (4 + i) * 1024 + (8 * (grp >> 1) + tq4) * 64 + (grp & 1) * 32 + tp4 * 8
                          : stg + 4096 + (16 * (grp >> 1) + 4 * (grp & 1) + tq4) * 64 + i * 32 + tp4 * 8;
        u32x4 f = cat(ldstr(a), ldstr(a + (isd ? 256 : 512)));
        if (!isd && grp == 3) { f[2] = 0u; f[3] = 0u; }
        pf[4 + i] = f;
        pfo[4 + i] = isd ? OFF_WDT + (u32)((2 * np + i) * 112 + ds_) * 1024 : p < 1008 ? OFF_WOT + (u32)((2 * nq + i) * 16 + h) * 1024 : 0x80000000u;
      }
      asm volatile("" : "+v"(pf[0]), "+v"(pf[1]), "+v"(pf[2]), "+v"(pf[3]), "+v"(pf[4]), "+v"(pf[5]), "+v"(uvv[0]), "+v"(uvv[1]));
      STAMPL(10, "prep tiled");
    }
    lds_wait(lb + L_SYNC + 4, 4);

    STAMPL(8, "W1 ready");
    accQ = (f32x16){}; accK = (f32x16){}; accV = (f32x16){};
    {
      u32 gb = lb + L_GAM + hi * 16; asm volatile("" : "+v"(gb));
      u32 wb = lb + lane * 16, wb2 = wb + 43008; asm volatile("" : "+v"(wb), "+v"(wb2));
      u32x4 wf[3][3], hf[2], gs[3], be[3]; float ht[8];
#define RDG(s) gs[(s) % 3] = lds128(gb + (s) * 32)
#define RDB(s) be[(s) % 3] = lds128(gb + 896 + (s) * 32)
#define RDW1(s, p) wf[(s) % 3][p] = lds128(((s) < 14 ? wb : wb2) + (3 * ((s) % 14) + (p)) * 1024)
#define RDW(s) do { RDW1(s, 0); RDW1(s, 1); RDW1(s, 2); } while (0)
#define DOTS(s, h) do { _Pragma("unroll") for (int j = 2 * (h); j < 2 * (h) + 2; ++j) { \
        asm volatile("v_fma_mix_f32 %0, %1, %2, %3 op_sel_hi:[1,1,1]" : "=v"(ht[2 * j]) : "v"(xf[s][j]), "v"(gs[(s) % 3][j]), "v"(be[(s) % 3][j])); \
        asm volatile("v_fma_mix_f32 %0, %1, %2, %3 op_sel:[1,1,1] op_sel_hi:[1,1,1]" : "=v"(ht[2 * j + 1]) : "v"(xf[s][j]), "v"(gs[(s) % 3][j]), "v"(be[(s) % 3][j])); } } while (0)
#define CVTS(s) do { hf[(s) & 1] = (u32x4){pk2(ht[0], ht[1]), pk2(ht[2], ht[3]), pk2(ht[4], ht[5]), pk2(ht[6], ht[7])}; asm volatile("" : "+v"(hf[(s) & 1])); } while (0)
      RDG(0); RDB(0); RDW(0); RDG(1); RDB(1); RDW(1); RDG(2); RDB(2); RDW(2); DOTS(0, 0); DOTS(0, 1); CVTS(0); SB();
#pragma unroll
      for (int s = 0; s < 28; ++s) {
        if (s == 1) lds_wait(lb + L_SYNC + 8, 3);
        if (s == 13) lds_wait(lb + L_SYNC + 24, 3);
        SB(); accQ = mfma32(wf[s % 3][0], hf[s & 1], accQ); SB();
        if (s < 25) RDG(s + 3);
        if (s < 25) RDW1(s + 3, 0);
        if (s < 27) DOTS(s + 1, 0);
        SB(); accK = mfma32(wf[s % 3][1], hf[s & 1], accK); SB();
        if (s < 25) RDB(s + 3);
        if (s < 25) RDW1(s + 3, 1);
        if (s < 27) DOTS(s + 1, 1);
        SB(); accV = mfma32(hf[s & 1], wf[s % 3][2], accV); SB();
        if (s < 25) RDW1(s + 3, 2);
        if (s + XPRE < 28) LDX(s + XPRE);
        else if (s < 22) st128(wsr, lane * 16 + pfo[s - 16], 0, pf[s - 16]);
        else if (s < 24) st32(wsr, lane * 4 + uvo[s - 22], 0, __float_as_uint(uvv[s - 22]));
        if (s < 27) CVTS(s + 1);
        SB();
      }
    }
    STAMPL(5, "qkv mfma done");
    const float unscale = __uint_as_float(__builtin_amdgcn_readfirstlane(lds_ld(lb + L_SYNC + 28)));
    if (unscale != 1.f) { accQ *= unscale; accK *= unscale; accV *= unscale; }
    u32x4 k0, k1, v0, v1, q0, q1;
#pragma unroll
    for (int j = 0; j < 4; ++j) {
      k0[j] = pk2(accK[2 * j], accK[2 * j + 1]); k1[j] = pk2(accK[8 + 2 * j], accK[9 + 2 * j]);
      v0[j] = pk2(accV[2 * j], accV[2 * j + 1]); v1[j] = pk2(accV[8 + 2 * j], accV[9 + 2 * j]);
    }
    if (hi) { k1[2] = 0x3F803F80u; k1[3] = 0x00003F80u; }
    if (l31 == 28) { v0 = (u32x4){0x3F803F80u, 0x3F803F80u, 0x3F803F80u, 0x3F803F80u}; v1 = v0; }
    const u32 kvo = OFF_KV + (u32)(ah * 64 + ai * 4 + wave) * 4096;
    st128(wsr, lane * 16, kvo, k0); st128(wsr, lane * 16 + 1024, kvo, k1); st128(wsr, lane * 16 + 2048, kvo, v0); st128(wsr, lane * 16 + 3072, kvo, v1);
    SB();
    float kn = 0.f, qn = 0.f;
    constexpr float QS = 0.18898223650461362f * 1.4426950408889634f;
#pragma unroll
    for (int r = 0; r < 16; ++r) { kn = fmaf(accK[r], accK[r], kn); accQ[r] *= QS; qn = fmaf(accQ[r], accQ[r], qn); }
#pragma unroll
    for (int j = 0; j < 4; ++j) { q0[j] = pk2(accQ[2 * j], accQ[2 * j + 1]); q1[j] = pk2(accQ[8 + 2 * j], accQ[9 + 2 * j]); }
    *(LAS u32x4*)(lb + L_Q + wave * 2048 + lane * 16) = q0; *(LAS u32x4*)(lb + L_Q + wave * 2048 + 1024 + lane * 16) = q1;
    qn = xhalf_sum(qn); kn = wave_max(xhalf_sum(kn));
    if (!hi) *(LAS float*)(lb + L_QN + (wave * 32 + l31) * 4) = qn;
    WAIT_VM(0);
    if (lane == 0) flag64(ws + OFF_KVG + (ah * 64 + ai * 4 + wave) * 128, __float_as_uint(kn), tag);
    if (b < 42) lds_wait(lb + L_SYNC + 32, 1);
    if (lane == 0 && __hip_atomic_fetch_add((LAS u32*)(lb + L_SYNC + 44), 1u, __ATOMIC_RELAXED, __HIP_MEMORY_SCOPE_WORKGROUP) == 3u) flag32(ws + OFF_PREPF + b * 128, tag);
    STAMPL(6, "kv flagged");
  } else {
    if (wave == 4) {
      u64 gv[8];
      __builtin_amdgcn_s_setprio(3);
#pragma nounroll
      for (int i = 0; i < SPIN_CAP; ++i) {
        bool ok = true;
#pragma unroll
        for (int n = 0; n < 7; ++n) gv[n] = poll64(ws + OFF_MODG + (64 * n + lane) * 8);
        gv[7] = poll64(ws + OFF_MODG + (672 + 224 * hi + 32 * kg + l31) * 8);
#pragma unroll
        for (int n = 0; n < 8; ++n) ok = ok && (u32)(gv[n] >> 32) == tag;
        if (__all(ok)) break;
        __builtin_amdgcn_s_sleep(POLL_SLEEP);
      }
      CFENCE();
      __builtin_amdgcn_s_setprio(0);
      *(LAS u32*)(lb + L_G2 + lane * 4) = hi ? pk2(1.f + bflo((u32)gv[7]), 1.f + bfhi((u32)gv[7])) : (u32)gv[7];
      WAIT_LGKM();
      lds_st(lb + L_SYNC + 36, 1u);

      float lo[7], hi2[7], mxv = 0.f;
#pragma unroll
      for (int n = 0; n < 7; ++n) {
        u32 v = (u32)gv[n];
        if (64 * n + lane >= 224) v = pk2(1.f + bflo(v), 1.f + bfhi(v));
        lo[n] = bflo(v); hi2[n] = bfhi(v); mxv = fmaxf(mxv, fmaxf(fabsf(lo[n]), fabsf(hi2[n])));
      }
      const u32 ex = max((int)(__float_as_uint(wave_max(mxv)) >> 23) - (127 + 14), 0);
      const float scl = __uint_as_float((127u - min(ex, 114u)) << 23);
#pragma unroll
      for (int n = 0; n < 7; ++n) {
        u32 o; asm("v_cvt_pk_f16_f32 %0, %1, %2" : "=v"(o) : "v"(lo[n] * scl), "v"(hi2[n] * scl));
        const int g = 64 * n + lane;
        *(LAS u32*)(lb + L_GAM + (g >= 224 ? g - 224 : g + 224) * 4) = o;
      }
      lds_st(lb + L_SYNC + 28, (127u + min(ex, 114u)) << 23);
      WAIT_LGKM();
      if (lane == 0) __hip_atomic_fetch_add((LAS u32*)(lb + L_SYNC + 4), 1u, __ATOMIC_RELAXED, __HIP_MEMORY_SCOPE_WORKGROUP);
    } else {
      if (wave == 5) { wait_flags(ws + OFF_WQF + (ah * 64 + min(lane, 41)) * 128, tag); GATE_MOD; lds_st(lb + L_SYNC + 48, 1u); }
      else lds_wait(lb + L_SYNC + 48, 1);

      const char* wsrc = ws + OFF_WQT + (size_t)ah * 84 * 1024 + (wave - 5) * 4096;
#pragma unroll
      for (int n = 0; n < 7; ++n) dma16x4(wsrc + n * 12288, lane * 16, lb + (wave - 5) * 4096 + n * 12288);
    }
    if (wave >= 5) {
      WAIT_VM(24);
      if (lane == 0) __hip_atomic_fetch_add((LAS u32*)(lb + L_SYNC + 4), 1u, __ATOMIC_RELAXED, __HIP_MEMORY_SCOPE_WORKGROUP);
      WAIT_VM(12);
      if (lane == 0) __hip_atomic_fetch_add((LAS u32*)(lb + L_SYNC + 8), 1u, __ATOMIC_RELAXED, __HIP_MEMORY_SCOPE_WORKGROUP);
      WAIT_VM(0);
      if (lane == 0) __hip_atomic_fetch_add((LAS u32*)(lb + L_SYNC + 24), 1u, __ATOMIC_RELAXED, __HIP_MEMORY_SCOPE_WORKGROUP);
      STAMPL(9, "W landed");
    }
  }
  BAR();
  STAMPL(11, "B3");

  const int ort = 8 * xg + (slot >> 2), ocq = slot & 3;
  const int gi = ai, gfj = ah;
  u32x4 wo_f[16], wgu[28], xres, gaA, gaB;
  {
    u32x4 qf[4][2];
#pragma unroll
    for (int t = 0; t < 4; ++t) { qf[t][0] = lds128(lb + L_Q + t * 2048 + lane * 16); qf[t][1] = lds128(lb + L_Q + t * 2048 + 1024 + lane * 16); }
    float qn2[4];
#pragma unroll
    for (int t = 0; t < 4; ++t) qn2[t] = lds32f(lb + L_QN + (t * 32 + l31) * 4);
    WAIT_LGKM();
    if (lane == 0) __hip_atomic_fetch_add((LAS u32*)(lb + L_SYNC + 40), 1u, __ATOMIC_RELAXED, __HIP_MEMORY_SCOPE_WORKGROUP);
    if (wave == 3) {
      __builtin_amdgcn_s_sleep(KV_SLEEP);
      const float km = wave_max(__uint_as_float(wait_gran(ws + OFF_KVG + (ah * 64 + lane) * 128, tag)));
      lds_st(lb + L_SYNC + 60, __float_as_uint(km));
      WAIT_LGKM();
      lds_st(lb + L_SYNC + 56, 1u);
    } else lds_wait(lb + L_SYNC + 56, 1);
    const float kmax2 = __uint_as_float(lds_ld(lb + L_SYNC + 60));
    STAMPL(12, "kv flags seen");
    const u32 kvb = OFF_KV + (u32)(ah * 64 + 8 * wave) * 4096;
    u32x4 kf[2][2], vf[2][2];
    const u32 kvl = lane * 16;
#define LDKV(D, kt, off) asm volatile("buffer_load_dwordx4 %0, %2, %3, %4 offen offset:" #off " sc1\n\tbuffer_load_dwordx4 %1, %2, %3, %4 offen offset:" #off "+1024 sc1" \
      : "=&v"(D[(kt) & 1][0]), "=&v"(D[(kt) & 1][1]) : "v"(kvl), "s"(wsr), "s"(kvb + (kt) * 4096) : "memory")
#define WAITN(n) asm volatile("s_waitcnt vmcnt(%0)" :: "i"(n) : "memory")
#define PINKV(D, kt) asm volatile("" : "+v"(D[(kt) & 1][0]), "+v"(D[(kt) & 1][1]))
#define WAITKV(D, kt, n) do { WAITN(n); SB(); PINKV(D, kt); } while (0)
#define WAITKV2(D, kt, n0, n1) do { if (wave < 4) WAITN(n0); else WAITN(n1); SB(); PINKV(D, kt); } while (0)
    asm volatile("s_nop 4");
    LDKV(kf, 0, 0); LDKV(vf, 0, 2048); LDKV(kf, 1, 0);
    float sh[4]; bool safe = true;
#pragma unroll
    for (int t = 0; t < 4; ++t) { sh[t] = 1.02f * __builtin_amdgcn_sqrtf(qn2[t] * kmax2); safe = safe && (sh[t] <= 48.f); }
    if (!__all(safe)) {
      BAR();
      float mx[4] = {-INFINITY, -INFINITY, -INFINITY, -INFINITY};
#pragma unroll 1
      for (int kt = 0; kt < 8; ++kt) {
        const u32x4 ka = ld128(wsr, lane * 16, kvb + kt * 4096), kb = ld128(wsr, lane * 16 + 1024, kvb + kt * 4096);
#pragma unroll
        for (int t = 0; t < 4; ++t) {
          f32x16 s = mfma32(ka, qf[t][0], (f32x16){}); s = mfma32(kb, qf[t][1], s);
#pragma unroll
          for (int r = 0; r < 16; ++r) mx[t] = fmaxf(mx[t], s[r]);
        }
      }
#pragma unroll
      for (int t = 0; t < 4; ++t) { const float m = xhalf_max(mx[t]); if (!hi) *(LAS float*)(lb + L_MX + (wave * 128 + t * 32 + l31) * 4) = m; }
      BAR();
#pragma unroll
      for (int t = 0; t < 4; ++t) {
        float m = -INFINITY;
#pragma unroll
        for (int w = 0; w < 8; ++w) m = fmaxf(m, lds32f(lb + L_MX + (w * 128 + t * 32 + l31) * 4));
        sh[t] = m;
      }
    }
    if (hi) {
#pragma unroll
      for (int t = 0; t < 4; ++t) {
        const float a = rbf(-sh[t]), r1 = -sh[t] - a, c = rbf(r1);
        qf[t][1][2] = pk2(a, c); qf[t][1][3] = pk2(r1 - c, 0.f);
      }
    }
    f32x16 o[4] = {(f32x16){}, (f32x16){}, (f32x16){}, (f32x16){}};
    WAITKV(kf, 0, 4);
    f32x16 sc = mfma32(kf[0][0], qf[0][0], (f32x16){}); sc = mfma32(kf[0][1], qf[0][1], sc);
    u32x4 p0, p1;
#define EXP2(v) __builtin_amdgcn_exp2f(v)
    float ec, ed;
#define GRP(r, PREV, MF, CUR) do { float ea = EXP2(sc[r]), eb = EXP2(sc[(r) + 1]); asm volatile("" : "+v"(ea), "+v"(eb)); PREV; SB(); MF; SB(); \
      ec = EXP2(sc[(r) + 2]); ed = EXP2(sc[(r) + 3]); asm volatile("" : "+v"(ec), "+v"(ed)); { u32 pa = pk2(ea, eb); asm volatile("" : "+v"(pa)); CUR = pa; } SB(); } while (0)
#define PKP(D) do { u32 pb = pk2(ec, ed); asm volatile("" : "+v"(pb)); D = pb; } while (0)
    u32 pwb = lb + wave * 8192 + lane * 16, lwb = lb + L_LP + wave * 512 + l31 * 4; asm volatile("" : "+v"(pwb), "+v"(lwb));
#define WRP(t) do { _Pragma("unroll") for (int g = 0; g < 2; ++g) *(LAS u32x4*)(pwb + ((t) * 2 + g) * 1024) = (u32x4){pk2(o[t][8 * g], o[t][8 * g + 1]), pk2(o[t][8 * g + 2], o[t][8 * g + 3]), pk2(o[t][8 * g + 4], o[t][8 * g + 5]), pk2(o[t][8 * g + 6], o[t][8 * g + 7])}; \
      if (hi) *(LAS float*)(lwb + (t) * 128) = o[t][12]; } while (0)
    const int w6 = min(wave, 6);
    const u32 wo_base = OFF_WOT + (u32)(7 * ocq + w6) * 16384, wg_base = OFF_WGU + (u32)(7 * gfj + w6) * 28672;
    auto nslot = [](int kt, int t, bool) constexpr { const int sl = 4 * (kt - 2) + t;
      return (t == 0 && kt < 7 ? 2 : 0) + (t == 2 && kt < 6 ? 2 : 0) + (kt >= 2 ? (sl == 0) + (sl < 16) + (sl >= 6 && sl < 18) : 0); };
    auto nafter = [&](int k0, int t0, int k1, int t1, bool young) constexpr { int n = -2; for (int i = 4 * k0 + t0; i <= 4 * k1 + t1; ++i) n += nslot(i >> 2, i & 3, young); return n; };
#pragma unroll
    for (int kt = 0; kt < 8; ++kt) {
      if (kt == 0) __builtin_amdgcn_s_setprio(3);
      if (kt == 2) __builtin_amdgcn_s_setprio(2);
      if (kt == 4) __builtin_amdgcn_s_setprio(1);
      if (kt == 6 && wave < 4) __builtin_amdgcn_s_setprio(0);
      if (kt == 7) __builtin_amdgcn_s_setprio(0);
#pragma unroll
      for (int t = 0; t < 4; ++t) {
        f32x16 sn;
        const bool more = t < 3 || kt < 7, first = t == 0 && kt == 0;
        const int kn = t < 3 ? (kt & 1) : ((kt + 1) & 1), tn = (t + 1) & 3;
        if (t == 3 && kt < 7) {
          if (kt == 0) WAITKV(kf, 1, nafter(0, 0, 0, 2, false) + 2);
          else if (kt == 1) WAITKV(kf, 2, nafter(0, 2, 1, 2, false));
          else WAITKV(kf, kt + 1, nafter(kt - 1, 2, kt, 2, false));
        }
        GRP(0, if (!first) PKP(p1[3]), if (more) sn = mfma32(kf[kn][0], qf[tn][0], (f32x16){}), p0[0]);
        GRP(4, PKP(p0[1]), if (more) sn = mfma32(kf[kn][1], qf[tn][1], sn), p0[2]);
        GRP(8, PKP(p0[3]), if (t > 0) o[t - 1] = mfma32(vf[kt & 1][1], p1, o[t - 1]); else if (kt > 0) o[3] = mfma32(vf[(kt + 1) & 1][1], p1, o[3]), p1[0]);
        if (t == 0 && kt < 7) LDKV(vf, kt + 1, 2048);
        if (t == 2 && kt < 6) LDKV(kf, kt + 2, 0);
        if (kt == 1 && t == 0 && wave == 3) {
#pragma nounroll
          for (int i = 0; i < SPIN_CAP; ++i) {
            const u32 a = poll32(ws + OFF_PREPF + lane * 128), c = poll32(ws + OFF_PREPF + (64 + lane) * 128), d = poll32(ws + OFF_PREPF + (128 + lane) * 128), e = poll32(ws + OFF_PREPF + (192 + lane) * 128);
            if (__all(a == tag && c == tag && d == tag && e == tag)) break;
            __builtin_amdgcn_s_sleep(8);
          }
          CFENCE();
          if (lane == 0) __hip_atomic_fetch_add((LAS u32*)(lb + L_SYNC + 40), 1u, __ATOMIC_RELAXED, __HIP_MEMORY_SCOPE_WORKGROUP);
        }
        if (t == 0) {
          if (kt == 2) STAMPL(1, "kt2");
          if (kt == 4) STAMPL(2, "kt4");
          if (kt == 6) STAMPL(4, "kt6");
        }
        if (kt >= 2) {
          const int sl = 4 * (kt - 2) + t;
          if (sl == 0) {
            lds_wait(lb + L_SYNC + 40, 9);
            dma4_touch(ws + wg_base + 12288, ((slot & 3) * 32 + l31) * 128 + hi * 64, lb + L_ST + w6 * 256);
          }
          if (sl < 10) wo_f[sl] = ld128(wsr, lane * 16, wo_base + sl * 1024);
          else if (sl < 16) wgu[sl - 10] = ld128(wsr, lane * 16, wg_base + (sl - 10) * 1024);
          if (sl >= 6 && sl < 18) dma16<true>(ws + (sl < 12 ? wo_base + (4 + sl) * 1024 : wg_base + (sl - 6) * 1024), lane * 16, lb + L_PARK + w6 * 12288 + (sl - 6) * 1024);
        }
        if (t == 0) {
          if (kt == 0) WAITKV(vf, 0, nafter(0, 0, 0, 0, false) + 4);
          else if (kt == 1) WAITKV(vf, 1, nafter(0, 0, 1, 0, false));
          else WAITKV(vf, kt, nafter(kt - 1, 0, kt, 0, false));
        }
        SB();
        GRP(12, PKP(p1[1]), o[t] = mfma32(vf[kt & 1][0], p0, o[t]), p1[2]);
        if (kt == 7 && t > 0) { WRP(t - 1); SB(); }
        if (more) sc = sn;
      }
    }
    PKP(p1[3]);
    o[3] = mfma32(vf[1][1], p1, o[3]);
    WRP(3);
    STAMPL(13, "attn done");
    BAR();
    const int qt = wave >> 1, sf = wave & 1;
    u32x4 pa[8]; float pl[8];
    {
      u32 base = lb + qt * 2048 + sf * 1024 + lane * 16, lbase = lb + L_LP + qt * 128 + l31 * 4;
      asm volatile("" : "+v"(base), "+v"(lbase));
#pragma unroll
      for (int w = 0; w < 8; ++w) { pa[w] = lds128(base + w * 8192); pl[w] = lds32f(lbase + w * 512); }
      SB();
    }
    float a8[8]; u32 sel0 = 0x00003F80u, sel1 = 0x3F800000u; asm volatile("" : "+v"(sel0), "+v"(sel1));
#pragma unroll
    for (int j = 0; j < 4; ++j) {
      a8[2 * j] = bflo(pa[0][j]); a8[2 * j + 1] = bfhi(pa[0][j]);
#pragma unroll
      for (int w = 1; w < 8; ++w) { a8[2 * j] = dot2(pa[w][j], sel0, a8[2 * j]); a8[2 * j + 1] = dot2(pa[w][j], sel1, a8[2 * j + 1]); }
    }
    const float l = ((pl[0] + pl[4]) + (pl[1] + pl[5])) + ((pl[2] + pl[6]) + (pl[3] + pl[7]));
    const float rl = __builtin_amdgcn_rcpf(l);
    const u32x4 of = {pk2(a8[0] * rl, a8[1] * rl), pk2(a8[2] * rl, a8[3] * rl), pk2(a8[4] * rl, a8[5] * rl), pk2(a8[6] * rl, a8[7] * rl)};
    st128(wsr, (l31 >> 4) * 1024 + ((l31 & 15) + 16 * (2 * sf + hi)) * 16, OFF_OB + (u32)((4 * ai + qt) * 16 + ah) * 2048, of);
    WAIT_VM(0);
    if (lane == 0) flag32(ws + OFF_OF + (((4 * ai + qt) * 16 + ah) * 2 + sf) * 128, tag);
    BAR();
    STAMPL(14, "O flagged");
  }

  u32 uvp[14];
  const int otok = 16 * ((lane >> 4) & 1) + (lane & 15), ofe = 112 * ocq + 16 * min(wave, 6) + 8 * hi;
  if (wave < 7) {
    xres = *(const u32x4*)(x + (size_t)(32 * ort + otok) * 448 + ofe);
    gaA = ld128(wsr, ofe * 4, OFF_MODG + 448 * 8); gaB = ld128(wsr, ofe * 4 + 16, OFF_MODG + 448 * 8);
#pragma unroll
    for (int k = 0; k < 14; ++k) uvp[k] = ld32(wsr, lane * 4, OFF_UV + (u32)(k * 112 + 7 * gfj + wave) * 256);
#pragma unroll
    for (int s = 12; s < 28; ++s) wgu[s] = ld128(wsr, lane * 16 + (s & 3) * 1024, OFF_WGU + (u32)((7 * gfj + wave) * 28 + (s & ~3)) * 1024);
#pragma unroll
    for (int j = 0; j < 6; ++j) wo_f[10 + j] = lds128(lb + L_PARK + wave * 12288 + j * 1024 + lane * 16);
#pragma unroll
    for (int j = 6; j < 12; ++j) wgu[j] = lds128(lb + L_PARK + wave * 12288 + j * 1024 + lane * 16);
    lds_wait(lb + L_SYNC + 16, 1);
  } else {
    wait_flags(ws + OFF_OF + (ort * 32 + l31) * 128, tag);
    lds_st(lb + L_SYNC + 16, 1u);
  }
  STAMPL(15, "O flags seen");
  {
    const char* osrc = ws + OFF_OB + (size_t)(ort * 16 + 2 * wave) * 2048;
    dma16x4(osrc, lane * 16, lb + 4 * wave * 1024);
  }
  __builtin_amdgcn_s_waitcnt(0x0F70);
  CFENCE();
  BAR();
  STAMPL(16, "O landed");
  if (wave < 7) {
    f32x4 y0 = {0.f, 0.f, 0.f, 0.f}, y1 = y0;
    u32x4 oa[8];
#pragma unroll
    for (int i = 0; i < 6; ++i) oa[i] = lds128(lb + i * 1024 + lane * 16);
#pragma unroll
    for (int h = 0; h < 16; ++h) {
      if (h < 13) { oa[(2 * h + 6) & 7] = lds128(lb + (2 * h + 6) * 1024 + lane * 16); oa[(2 * h + 7) & 7] = lds128(lb + (2 * h + 7) * 1024 + lane * 16); }
      SB(); y0 = mfma16(wo_f[h], oa[(2 * h) & 7], y0); y1 = mfma16(wo_f[h], oa[(2 * h + 1) & 7], y1); SB();
    }
    float yv[8];
#pragma unroll
    for (int r = 0; r < 4; ++r) { auto sw = __builtin_amdgcn_permlane16_swap(__float_as_uint(y0[r]), __float_as_uint(y1[r]), false, false); yv[r] = __uint_as_float(sw[0]); yv[4 + r] = __uint_as_float(sw[1]); }
    const u32 gw[4] = {gaA[0], gaA[2], gaB[0], gaB[2]};
    u32x4 x2p; float s1 = 0.f, s2 = 0.f;
#pragma unroll
    for (int j = 0; j < 4; ++j) {
      x2p[j] = pk2(fmaf(bflo(gw[j]), yv[2 * j], bflo(xres[j])), fmaf(bfhi(gw[j]), yv[2 * j + 1], bfhi(xres[j])));
      s1 = dot2(x2p[j], 0x3F803F80u, s1); s2 = dot2(x2p[j], x2p[j], s2);
    }
    const u32 fo = (u32)(ort * 28 + 7 * ocq + wave) * 1024;
    st128(wsr, (otok + 32 * hi) * 16, OFF_X2 + fo, x2p);
    s1 = xhalf_sum(s1); s2 = xhalf_sum(s2);
    *(LAS float*)(lb + L_ST + wave * 256 + (hi * 32 + otok) * 4) = hi ? s2 : s1;
    WAIT_VM(0);
  }
  BAR();
  if (wave == 7) {
    float t = 0.f;
#pragma unroll
    for (int w = 0; w < 7; ++w) t += lds32f(lb + L_ST + w * 256 + lane * 4);
    const u32 nv = __builtin_amdgcn_update_dpp(0u, __float_as_uint(t), 0xB1, 0xf, 0xf, false);
    st128(wsr, !(lane & 1) ? (u32)lane * 8 : 0x80000000u, OFF_X2G + (u32)(ort * 4 + ocq) * 512, (u32x4){__float_as_uint(t), tag, nv, tag});
  } else {
    float uvs = 0.f;
#pragma unroll
    for (int k = 0; k < 14; ++k) uvs += __uint_as_float(uvp[k]);
    *(LAS float*)(lb + L_UV + wave * 256 + lane * 4) = uvs;
  }
  STAMPL(17, "x2 flagged");

  const int dsl = slot & 15, dq = 2 * (slot >> 4) + dsl / 7, dc = dsl % 7;
  const bool dact = dsl < 14;
  u32x4 wdf[28], x2r, gbA, gbB;
  if (wave == 7) {
    u64 g[16]; const char* gp = ws + OFF_X2G + (16 * gi * 64 + lane) * 8;
#pragma nounroll
    for (int i = 0; i < SPIN_CAP; ++i) {
      bool ok = true;
#pragma unroll
      for (int p = 0; p < 16; ++p) g[p] = poll64(gp + p * 512);
#pragma unroll
      for (int p = 0; p < 16; ++p) ok = ok && (u32)(g[p] >> 32) == tag;
      if (__all(ok)) break;
      __builtin_amdgcn_s_sleep(1);
    }
    CFENCE();
#pragma unroll
    for (int p = 0; p < 16; ++p) *(LAS u32*)(lb + L_SP2 + p * 256 + lane * 4) = (u32)g[p];
  }
  BAR();
  STAMPL(18, "x2 flags seen");
  {
    const char* asrc = ws + OFF_X2 + (size_t)(4 * gi) * 28 * 1024;
#pragma unroll
    for (int n = 0; n < 4; ++n) { const int g = min(wave + 8 * n, 27); dma16x4(asrc + g * 4096, lane * 16, lb + g * 4096); }
  }
  WAIT_VM(12);
  BAR();
  STAMPL(19, "A0 landed");
  const int dm = wave >> 2, dn = (wave >> 1) & 1, dgp = wave & 1;
  const u32 wdo = OFF_WDT + (u32)(2 * dc * 112 + 14 * wave) * 1024;
  if (wave < 7) {
    u32 sb = lb + L_SP2 + l31 * 4, ub = lb + L_UV + wave * 256 + hi * 16; asm volatile("" : "+v"(sb), "+v"(ub));
    u32 ab = lb + lane * 16, ab2 = ab + 57344; asm volatile("" : "+v"(ab), "+v"(ab2));
    f32x16 acc[2]; u32x4 af[4]; f32x4 uv[4]; float st[8], rs = 0.f, nm = 0.f, hv[8];
#define EP_RD(T) do { _Pragma("unroll") for (int c = 0; c < 4; ++c) { st[c] = lds32f(sb + ((T) * 4 + c) * 256); st[4 + c] = lds32f(sb + ((T) * 4 + c) * 256 + 128); } } while (0)
#define EP_UV(g) do { uv[0] = lds128f(ub + 32 * (g)); uv[1] = lds128f(ub + 32 * (g) + 64); uv[2] = lds128f(ub + 32 * (g) + 128); uv[3] = lds128f(ub + 32 * (g) + 192); } while (0)
#define EP_ST() do { float m = ((st[0] + st[1]) + (st[2] + st[3])) * (1.f / 448.f); asm volatile("" : "+v"(m)); const float e2 = (((st[4] + st[5]) + st[6]) + st[7]) * (1.f / 448.f); \
      rs = __builtin_amdgcn_rsqf(fmaxf(e2 - m * m, 0.f) + 1e-5f); nm = -m * rs; } while (0)
    float gt, up, ez;
    constexpr float K1 = -2.f * 0.7978845608028654f * 1.4426950408889634f, K3 = K1 * 0.044715f;
#define EP_A(A, e) do { const float tg = fma1(nm, uv[0][(e) & 3], uv[2][(e) & 3]), tu = fma1(nm, uv[1][(e) & 3], uv[3][(e) & 3]); gt = fma1(A[e], rs, tg); up = fma1(A[8 + (e)], rs, tu); asm volatile("" : "+v"(gt), "+v"(up)); } while (0)
#define EP_B() do { ez = __builtin_amdgcn_exp2f(gt * fmaf(gt * gt, K3, K1)); up *= gt; asm volatile("" : "+v"(ez), "+v"(up)); } while (0)
#define EP_C(e) do { hv[e] = up * __builtin_amdgcn_rcpf(1.f + ez); asm volatile("" : "+v"(hv[e])); } while (0)
#define EP_EL(A, e) do { EP_A(A, e); EP_B(); EP_C(e); } while (0)
#define EP_OUT(T) st128(wsr, lane * 16, OFF_HB + (u32)((4 * gi + (T)) * 112 + 7 * gfj + wave) * 1024, (u32x4){pk2(hv[0], hv[1]), pk2(hv[2], hv[3]), pk2(hv[4], hv[5]), pk2(hv[6], hv[7])})
#pragma unroll
    for (int i = 0; i < 3; ++i) af[i] = lds128(ab + i * 1024);
#pragma unroll
    for (int t = 0; t < 4; ++t) {
#pragma unroll
      for (int s = 0; s < 28; ++s) {
        const int n = t * 28 + s;
        if (n == 25) { STAMPL(28, "gu n25"); WAIT_VM(0); BAR();  }
        if (n + 3 < 112) af[(n + 3) & 3] = (n + 3 < 56) ? lds128(ab + (n + 3) * 1024) : lds128(ab2 + (n + 3 - 56) * 1024);
        SB();
        if (s == 0) acc[t & 1] = mfma32(wgu[s], af[n & 3], (f32x16){}); else acc[t & 1] = mfma32(wgu[s], af[n & 3], acc[t & 1]);
        SB();
        if (t > 0) {
          if (s == 0) { EP_RD(t - 1); EP_UV(0); }
          if (s == 2) EP_ST();
          if (s >= 3 && s < 27) { if (s % 3 == 0) EP_A(acc[(t - 1) & 1], s / 3 - 1); else if (s % 3 == 1) EP_B(); else EP_C(s / 3 - 1); }
          if (s == 13) EP_UV(1);
          if (s == 27) EP_OUT(t - 1);
          if (s == 27 && t == 1) STAMPL(20, "gu tile1");
          if (s == 27 && t == 2) STAMPL(21, "gu tile2");
          if (s == 27 && t == 3) STAMPL(22, "gu tile3");
          if (s % 4 == 1 && s < 24) { const int f = 6 * (t - 1) + s / 4; wdf[f] = ld128(wsr, lane * 16, wdo + (u32)((f & 1) * 112 + (f >> 1)) * 1024); }
          SB();
        }
      }
    }
    asm volatile("s_nop 11" : "+v"(acc[1]));
    EP_RD(3); EP_UV(0); EP_ST();
    {
      f32x4 uw[4];
      uw[0] = lds128f(ub + 32); uw[1] = lds128f(ub + 96); uw[2] = lds128f(ub + 160); uw[3] = lds128f(ub + 224);
      float g8[8], u8[8];
#pragma unroll
      for (int e = 0; e < 8; ++e) {
        const f32x4* W = e < 4 ? uv : uw;
        g8[e] = fma1(acc[1][e], rs, fma1(nm, W[0][e & 3], W[2][e & 3])); u8[e] = fma1(acc[1][8 + e], rs, fma1(nm, W[1][e & 3], W[3][e & 3]));
      }
#pragma unroll
      for (int e = 0; e < 8; ++e) hv[e] = (g8[e] * u8[e]) * __builtin_amdgcn_rcpf(1.f + __builtin_amdgcn_exp2f(g8[e] * fmaf(g8[e] * g8[e], K3, K1)));
    }
    EP_OUT(3);
    WAIT_VM(0);
    if (lane == 0) flag32(ws + OFF_HF + ((gi * 16 + gfj) * 8 + wave) * 128, tag);
  } else {
    WAIT_VM(0); BAR();
#pragma unroll
    for (int f = 0; f < 18; ++f) wdf[f] = ld128(wsr, lane * 16, wdo + (u32)((f & 1) * 112 + (f >> 1)) * 1024);
  }
  STAMPL(23, "h flagged");
  if (dact) {
    BAR();
#pragma unroll
    for (int f = 18; f < 28; ++f) wdf[f] = ld128(wsr, lane * 16, wdo + (u32)((f & 1) * 112 + (f >> 1)) * 1024);
    const int drt = 8 * xg + 2 * dq, dcol = 64 * dc + 32 * dn + 16 * dgp + 8 * hi;
    gbA = ld128(wsr, dcol * 4, OFF_MODG + 1120 * 8); gbB = ld128(wsr, dcol * 4 + 16, OFF_MODG + 1120 * 8);
    wait_flags(ws + OFF_HF + (((drt >> 2) * 16 + 2 * wave + ((lane >> 3) & 1)) * 8 + min(lane & 7, 6)) * 128, tag);
    STAMPL(24, "h flags seen");
    u32x4 hf[20];
#define LDH(i) hf[(i) % 20] = ld128(wsr, lane * 16 + (((i) % 14) & 3) * 1024, OFF_HB + (u32)((drt + (i) / 14) * 112 + 14 * wave + (((i) % 14) & ~3)) * 1024)
#pragma unroll
    for (int i = 0; i < 20; ++i) { LDH(i); SB(); }
    x2r = ld128(wsr, lane * 16, OFF_X2 + (u32)((drt + dm) * 28 + 4 * dc + 2 * dn + dgp) * 1024);
    f32x16 c0, c1;
    u32 pwb = lb + wave * 16384 + lane * 16; asm volatile("" : "+v"(pwb));
#pragma unroll
    for (int i = 0; i < 28; ++i) {
      const int m = i / 14, j = i % 14;
      SB();
      if (j == 0) { c0 = mfma32(wdf[0], hf[i % 20], (f32x16){}); c1 = mfma32(wdf[1], hf[i % 20], (f32x16){}); }
      else { c0 = mfma32(wdf[2 * j], hf[i % 20], c0); c1 = mfma32(wdf[2 * j + 1], hf[i % 20], c1); }
      SB();
      if (i < 8) LDH(i + 20);
      if (j == 13) {
#pragma unroll
        for (int g = 0; g < 4; ++g) {
          *(LAS f32x4*)(pwb + ((2 * m) * 4 + g) * 1024) = (f32x4){c0[4 * g], c0[4 * g + 1], c0[4 * g + 2], c0[4 * g + 3]};
          *(LAS f32x4*)(pwb + ((2 * m + 1) * 4 + g) * 1024) = (f32x4){c1[4 * g], c1[4 * g + 1], c1[4 * g + 2], c1[4 * g + 3]};
        }
      }
    }
    STAMPL(25, "down mfma done");
    BAR();
    f32x4 pa[8], pb[8];
    {
      u32 base = lb + (2 * dm + dn) * 4096 + dgp * 2048 + lane * 16, base2 = base + 65536;
      asm volatile("" : "+v"(base), "+v"(base2));
#pragma unroll
      for (int w = 0; w < 4; ++w) {
        pa[w] = lds128f(base + w * 16384); pb[w] = lds128f(base + w * 16384 + 1024);
        pa[4 + w] = lds128f(base2 + w * 16384); pb[4 + w] = lds128f(base2 + w * 16384 + 1024);
      }
      SB();
    }
    const f32x4 a0 = ((pa[0] + pa[4]) + (pa[1] + pa[5])) + ((pa[2] + pa[6]) + (pa[3] + pa[7])), a1 = ((pb[0] + pb[4]) + (pb[1] + pb[5])) + ((pb[2] + pb[6]) + (pb[3] + pb[7]));
    float yv[8];
#pragma unroll
    for (int r = 0; r < 4; ++r) { auto sw = __builtin_amdgcn_permlane32_swap(__float_as_uint(a0[r]), __float_as_uint(a1[r]), false, false); yv[r] = __uint_as_float(sw[0]); yv[4 + r] = __uint_as_float(sw[1]); }
    const u32 gw[4] = {gbA[0], gbA[2], gbB[0], gbB[2]};
    u32x4 ov;
#pragma unroll
    for (int j = 0; j < 4; ++j) ov[j] = pk2(fmaf(bflo(gw[j]), yv[2 * j], bflo(x2r[j])), fmaf(bfhi(gw[j]), yv[2 * j + 1], bfhi(x2r[j])));
    *(u32x4*)(out + (size_t)(32 * (drt + dm) + l31) * 448 + dcol) = ov;
    if (b == 0 && tid == 0) *(GAS u32*)ws = tag;
  }
  STAMPL(26, "end");
}

extern "C" void kernel_launch(void* const* d_in, const int* in_sizes, int n_in, void* d_out, int out_size, void* d_ws, size_t ws_size, hipStream_t stream) {
  fused_block<<<256, 512, 0, stream>>>((const u16*)d_in[0], (const u16*)d_in[1], (const u16*)d_in[2], (const u16*)d_in[4], (const u16*)d_in[5],
                                       (const u16*)d_in[6], (const u16*)d_in[7], (const u16*)d_in[8], (u16*)d_out, (char*)d_ws);
  probe_spin<<<1, 256, 0, stream>>>((char*)d_ws);
}
```

```cpp
#define PROBE 0
#define PROBE_MUL 0
#define PROBE_K 0
#include <hip/hip_runtime.h>
#include <stdint.h>

typedef unsigned short u16;
typedef unsigned u32;
typedef unsigned long long u64;
typedef __attribute__((ext_vector_type(2))) unsigned u32x2;
typedef __attribute__((ext_vector_type(4))) unsigned u32x4;
typedef __attribute__((ext_vector_type(2))) float f32x2;
typedef __attribute__((ext_vector_type(4))) float f32x4;
typedef __attribute__((ext_vector_type(16))) float f32x16;
typedef __attribute__((ext_vector_type(2))) __bf16 bf16x2;
typedef __attribute__((ext_vector_type(8))) __bf16 bf16x8;
typedef __attribute__((ext_vector_type(4))) short s16x4;
#define DEV __device__ __forceinline__
#define GAS __attribute__((address_space(1)))
#define LAS __attribute__((address_space(3)))
#define RLX_AGENT __ATOMIC_RELAXED, __HIP_MEMORY_SCOPE_AGENT

constexpr u32 OFF_MODG = 4096;
constexpr u32 OFF_X2G = 65536, OFF_UV = 13467648  , OFF_WQT = 524288, OFF_WOT = 1900544, OFF_WGU = 2359296, OFF_WDT = 5570560;
constexpr u32 OFF_KV = 7176192, OFF_OB = 11370496, OFF_X2 = 15302656, OFF_HB = 17137664, OFF_XH = 24477696;
constexpr u32 OFF_XF = 26312704, OFF_WQF = OFF_XF + 512 * 128, OFF_KVG = OFF_WQF + 1024 * 128, OFF_OF = OFF_KVG + 1024 * 128, OFF_HF = OFF_OF + 2048 * 128, OFF_PREPF = OFF_HF + 2048 * 128, WS_USED = OFF_PREPF + 256 * 128;
constexpr u32 OFF_STAMP = 29360128;
constexpr u32 L_STG = 90112  , L_Q = 131072, L_GAM = 139264, L_QN = 142848;
constexpr u32 L_PARK = 64512 + 1024  ;
constexpr u32 L_SP2 = 151552  , L_MODP = L_SP2, L_MX = L_SP2, L_LP = L_SP2, L_UV = 155648  , L_ST = 157696  , L_G2 = 159488  ;
constexpr u32 L_SYNC = 160000;
constexpr u32 LDS_BYTES = 163840;
constexpr int CODE_BYTES = 38000;
constexpr int SPIN_CAP = 1 << 15;
#define FIRST_POLL 280
#define KV_SLEEP 1
#define PREP_START 220
#define POLL_T 0
#define POLL_SLEEP 3

DEV float bflo(u32 w) { return __uint_as_float(w << 16); }
DEV float bfhi(u32 w) { return __uint_as_float(w & 0xffff0000u); }
DEV u32 pk2(float a, float b) { f32x2 v = {a, b}; return __builtin_bit_cast(u32, __builtin_convertvector(v, bf16x2)); }
DEV float rbf(float a) { return bflo(pk2(a, 0.f)); }
DEV float fma1(float a, float b, float c) { float r; asm("v_fma_f32 %0, %1, %2, %3" : "=v"(r) : "v"(a), "v"(b), "v"(c)); return r; }
DEV float dot2(u32 a, u32 b, float c) { return __builtin_amdgcn_fdot2_f32_bf16(__builtin_bit_cast(bf16x2, a), __builtin_bit_cast(bf16x2, b), c, false); }
DEV f32x16 mfma32(u32x4 a, u32x4 b, f32x16 c) { return __builtin_amdgcn_mfma_f32_32x32x16_bf16(__builtin_bit_cast(bf16x8, a), __builtin_bit_cast(bf16x8, b), c, 0, 0, 0); }
DEV f32x4 mfma16(u32x4 a, u32x4 b, f32x4 c) { return __builtin_amdgcn_mfma_f32_16x16x32_bf16(__builtin_bit_cast(bf16x8, a), __builtin_bit_cast(bf16x8, b), c, 0, 0, 0); }
DEV float xhalf_sum(float v) { auto r = __builtin_amdgcn_permlane32_swap(__float_as_uint(v), __float_as_uint(v), false, false); return __uint_as_float(r[0]) + __uint_as_float(r[1]); }
DEV float xhalf_max(float v) { auto r = __builtin_amdgcn_permlane32_swap(__float_as_uint(v), __float_as_uint(v), false, false); return fmaxf(__uint_as_float(r[0]), __uint_as_float(r[1])); }
#define DPPF(v, ctrl) __uint_as_float(__builtin_amdgcn_update_dpp(0u, __float_as_uint(v), ctrl, 0xf, 0xf, false))
DEV float wave_max(float v) {
  v = fmaxf(v, DPPF(v, 0x121)); v = fmaxf(v, DPPF(v, 0x122)); v = fmaxf(v, DPPF(v, 0x124)); v = fmaxf(v, DPPF(v, 0x128));
  auto r = __builtin_amdgcn_permlane16_swap(__float_as_uint(v), __float_as_uint(v), false, false);
  return xhalf_max(fmaxf(__uint_as_float(r[0]), __uint_as_float(r[1])));
}
#define WAIT_VM(n) asm volatile("s_waitcnt vmcnt(" #n ")" ::: "memory")
#define BAR() asm volatile("s_waitcnt lgkmcnt(0)\n\ts_barrier" ::: "memory")
#define SB() __builtin_amdgcn_sched_barrier(0)
#define CFENCE() asm volatile("" ::: "memory")
template <bool SC1> DEV void dma16(const void* sbase, u32 voff, u32 lds_dst) {
  u32 keep;
  if (SC1) asm volatile("s_mov_b32 %0, m0\n\ts_mov_b32 m0, %3\n\ts_nop 2\n\tglobal_load_lds_dwordx4 %1, %2 sc1\n\ts_mov_b32 m0, %0" : "=&s"(keep) : "v"(voff), "s"(sbase), "s"(lds_dst) : "memory");
  else asm volatile("s_mov_b32 %0, m0\n\ts_mov_b32 m0, %3\n\ts_nop 2\n\tglobal_load_lds_dwordx4 %1, %2\n\ts_mov_b32 m0, %0" : "=&s"(keep) : "v"(voff), "s"(sbase), "s"(lds_dst) : "memory");
}
DEV void dma16x4(const void* sbase, u32 voff, u32 lds_dst) {
  u32 keep;
  asm volatile("s_mov_b32 %0, m0\n\ts_mov_b32 m0, %3\n\ts_nop 2\n\tglobal_load_lds_dwordx4 %1, %2 sc1\n\tglobal_load_lds_dwordx4 %1, %2 offset:1024 sc1\n\t"
               "global_load_lds_dwordx4 %1, %2 offset:2048 sc1\n\tglobal_load_lds_dwordx4 %1, %2 offset:3072 sc1\n\ts_mov_b32 m0, %0" : "=&s"(keep) : "v"(voff), "s"(sbase), "s"(lds_dst) : "memory");
}
DEV void dma4_touch(const void* sbase, u32 voff, u32 lds_dst) {
  u32 keep;
  asm volatile("s_mov_b32 %0, m0\n\ts_mov_b32 m0, %3\n\ts_nop 2\n\tglobal_load_lds_dword %1, %2 sc1\n\ts_mov_b32 m0, %0" : "=&s"(keep) : "v"(voff), "s"(sbase), "s"(lds_dst) : "memory");
}
DEV u32x4 lds128(u32 a) { return *(const LAS u32x4*)a; }
DEV f32x4 lds128f(u32 a) { return *(const LAS f32x4*)a; }
DEV float lds32f(u32 a) { return *(const LAS float*)a; }
DEV u32x2 ldstr(u32 a) { return __builtin_bit_cast(u32x2, __builtin_amdgcn_ds_read_tr16_b64_v4i16((LAS s16x4*)a)); }
DEV u32x4 cat(u32x2 a, u32x2 b) { return (u32x4){a.x, a.y, b.x, b.y}; }
typedef __amdgpu_buffer_rsrc_t rsrc_t;
DEV u32x4 ld128(rsrc_t r, u32 voff, u32 soff) { return __builtin_amdgcn_raw_buffer_load_b128(r, voff, soff, 16); }
DEV u32 ld32(rsrc_t r, u32 voff, u32 soff) { return __builtin_amdgcn_raw_buffer_load_b32(r, voff, soff, 16); }
DEV void st128(rsrc_t r, u32 voff, u32 soff, u32x4 v) {
  __builtin_amdgcn_raw_buffer_store_b128(v, r, voff, soff, 16);
  asm volatile("s_nop 1" :: "v"(v) : "memory");
}
DEV void st32(rsrc_t r, u32 voff, u32 soff, u32 v) { __builtin_amdgcn_raw_buffer_store_b32(v, r, voff, soff, 16); }
DEV u32 poll32(const char* p) { return __hip_atomic_load((GAS u32*)p, RLX_AGENT); }
DEV u64 poll64(const char* p) { return __hip_atomic_load((GAS u64*)p, RLX_AGENT); }
DEV void flag32(char* p, u32 v) { __hip_atomic_store((GAS u32*)p, v, RLX_AGENT); }
DEV void flag64(char* p, u32 val, u32 tag) { __hip_atomic_store((GAS u64*)p, ((u64)tag << 32) | val, RLX_AGENT); }
DEV void wait_flags(const char* p, u32 tag) {
#pragma nounroll
  for (int i = 0; i < SPIN_CAP; ++i) { if (__all(poll32(p) == tag)) break; __builtin_amdgcn_s_sleep(POLL_SLEEP); }
  CFENCE();
}
DEV u32 wait_gran(const char* p, u32 tag) {
  u64 g = 0;
#pragma nounroll
  for (int i = 0; i < SPIN_CAP; ++i) { g = poll64(p); if (__all((u32)(g >> 32) == tag)) break; __builtin_amdgcn_s_sleep(POLL_SLEEP); }
  CFENCE();
  return (u32)g;
}
DEV u32 lds_ld(u32 a) { return __hip_atomic_load((LAS u32*)a, __ATOMIC_RELAXED, __HIP_MEMORY_SCOPE_WORKGROUP); }
DEV void lds_st(u32 a, u32 v) { __hip_atomic_store((LAS u32*)a, v, __ATOMIC_RELAXED, __HIP_MEMORY_SCOPE_WORKGROUP); }
DEV u32 lds_inc(u32 a) { return __builtin_amdgcn_readfirstlane(__hip_atomic_fetch_add((LAS u32*)a, 1u, __ATOMIC_RELAXED, __HIP_MEMORY_SCOPE_WORKGROUP)); }
DEV void lds_wait(u32 a, u32 want) {
#pragma nounroll
  for (int i = 0; i < (SPIN_CAP << 6); ++i) { if (__builtin_amdgcn_readfirstlane(lds_ld(a)) >= want) break; __builtin_amdgcn_s_sleep(1); }
  CFENCE();
}
#define WAIT_LGKM() asm volatile("s_waitcnt lgkmcnt(0)" ::: "memory")
#define STAMP(i) do {} while (0)
#undef STAMP
#define STAMP(i) do { if (((i) == 0 || (i) == PROBE) && wave == 0 && lane == 0) __hip_atomic_store((GAS u64*)(ws + OFF_STAMP) + b * 2 + ((i) != 0 || PROBE == 0) , (u64)__builtin_amdgcn_s_memrealtime(), RLX_AGENT); \
   if ((i) == 0 && PROBE == 0 && wave == 0 && lane == 0) __hip_atomic_store((GAS u64*)(ws + OFF_STAMP) + b * 2, (u64)__builtin_amdgcn_s_memrealtime(), RLX_AGENT); } while (0)
__global__ void probe_spin(char* ws) {
  __shared__ u64 mn[256], mx[256]; __shared__ u64 sel, lo;
  const u64* p = (const u64*)(ws + OFF_STAMP);
  mn[threadIdx.x] = p[threadIdx.x * 2]; mx[threadIdx.x] = p[threadIdx.x * 2 + 1];
  __syncthreads();
  int r = 0, r0 = 0; for (int i = 0; i < 256; ++i) { r += (mx[i] < mx[threadIdx.x]) || (mx[i] == mx[threadIdx.x] && i < (int)threadIdx.x); r0 += (mn[i] < mn[threadIdx.x]) || (mn[i] == mn[threadIdx.x] && i < (int)threadIdx.x); }
  if (r == PROBE_K) sel = mx[threadIdx.x];
  if (r0 == 0) lo = mn[threadIdx.x];
  __syncthreads();
  if (threadIdx.x == 0) {
    u64 d = sel > lo ? (sel - lo) * PROBE_MUL : 0; if (d > 20000) d = 20000;
    const u64 t0 = __builtin_amdgcn_s_memrealtime();
    while (__builtin_amdgcn_s_memrealtime() - t0 < d) __builtin_amdgcn_s_sleep(1);
  }
}
#define STAMPL(i, s) STAMP(i)
#define GATE_MOD lds_wait(lb + L_SYNC + 36, 1)

__global__ __launch_bounds__(512, 2) void fused_block(const u16* __restrict__ x, const u16* __restrict__ wqkv, const u16* __restrict__ wo,
                                                      const u16* __restrict__ wg, const u16* __restrict__ wu, const u16* __restrict__ wd,
                                                      const u16* __restrict__ cvec, const u16* __restrict__ wmod, u16* __restrict__ out, char* ws) {
  __shared__ __attribute__((aligned(16))) char smem[LDS_BYTES];
  const int b = blockIdx.x, tid = threadIdx.x, lane = tid & 63;
  const int wave = __builtin_amdgcn_readfirstlane(tid >> 6);
  const int l31 = lane & 31, hi = lane >> 5;
  const int xg = b & 7, slot = b >> 3;
  const int ai = 4 * (xg >> 1) + (slot & 3), ah = 8 * (xg & 1) + (slot >> 2);
  const u32 lb = (u32)(uintptr_t)(LAS char*)smem;
  const rsrc_t wsr = __builtin_amdgcn_make_buffer_rsrc(ws, 0, WS_USED, 0x00020000);
  STAMP(0);
  {
    u64 pc; asm volatile("s_getpc_b64 %0" : "=s"(pc));
    if (wave == 0) {
      const u32 off = min((u32)(slot * 1280 + min(lane, 9) * 128), (u32)CODE_BYTES);
      u32 keep;
      asm volatile("s_mov_b32 %0, m0\n\ts_mov_b32 m0, %3\n\ts_nop 2\n\tglobal_load_lds_dword %1, %2\n\ts_mov_b32 m0, %0" : "=&s"(keep) : "v"(off), "s"(pc), "s"(lb + L_ST) : "memory");
    }
  }
  if (wave == 1) {
    const u32 off = min(lane, 12) << 21;
    u32 keep;
    asm volatile("s_mov_b32 %0, m0\n\ts_mov_b32 m0, %3\n\ts_nop 2\n\tglobal_load_lds_dword %1, %2\n\ts_mov_b32 m0, %0" : "=&s"(keep) : "v"(off), "s"(ws), "s"(lb + L_ST + 256) : "memory");
  }
  if (wave == 2 && lane < 16) lds_st(lb + L_SYNC + lane * 4, 0u);
  const u64 t_entry = __builtin_amdgcn_s_memrealtime();
  const u32 epoch_v = poll32(ws);
  u32 tag;

  f32x16 accQ, accK, accV;
  const int tq4 = (lane & 15) >> 2, tp4 = lane & 3, grp = lane >> 4;
  if (b < 42) {
    u32x4 wv[7];
    const u32 cv = cvec[56 * wave + min(lane, 55)];
#pragma unroll
    for (int n = 0; n < 7; ++n) wv[n] = *(const u32x4*)((const char*)wmod + (size_t)(56 * wave + 8 * n + (lane >> 3)) * 5376 + 128 * b + 16 * (lane & 7));
    BAR();
    const float cf = bflo(cv);
    const float sl = rbf(cf * __builtin_amdgcn_rcpf(1.f + __builtin_amdgcn_exp2f(-1.4426950408889634f * cf)));
    float acc[8] = {0.f, 0.f, 0.f, 0.f, 0.f, 0.f, 0.f, 0.f};
#pragma unroll
    for (int n = 0; n < 7; ++n) {
      const float sn = __uint_as_float(__builtin_amdgcn_ds_bpermute((8 * n + (lane >> 3)) * 4, __float_as_uint(sl)));
#pragma unroll
      for (int j = 0; j < 4; ++j) { acc[2 * j] = fmaf(sn, bflo(wv[n][j]), acc[2 * j]); acc[2 * j + 1] = fmaf(sn, bfhi(wv[n][j]), acc[2 * j + 1]); }
    }

#pragma unroll
    for (int j = 0; j < 8; ++j) acc[j] += DPPF(acc[j], 0x128);
#pragma unroll
    for (int j = 0; j < 4; ++j) { auto r = __builtin_amdgcn_permlane16_swap(__float_as_uint(acc[j]), __float_as_uint(acc[j + 4]), false, false); acc[j] = __uint_as_float(r[0]) + __uint_as_float(r[1]); }
#pragma unroll
    for (int j = 0; j < 2; ++j) { auto r = __builtin_amdgcn_permlane32_swap(__float_as_uint(acc[j]), __float_as_uint(acc[j + 2]), false, false); acc[j] = __uint_as_float(r[0]) + __uint_as_float(r[1]); }
    if (!(lane & 8)) *(LAS f32x2*)(lb + L_MODP + (wave * 64 + 8 * (lane & 7) + 2 * hi + 4 * (grp & 1)) * 4) = (f32x2){acc[0], acc[1]};
    WAIT_LGKM();
    u32 arrived = 0;
    if (lane == 0) arrived = __hip_atomic_fetch_add((LAS u32*)(lb + L_SYNC + 12), 1u, __ATOMIC_RELAXED, __HIP_MEMORY_SCOPE_WORKGROUP);
    if (__builtin_amdgcn_readfirstlane(arrived) == 7) {
      CFENCE();
      f32x2 t = {0.f, 0.f};
#pragma unroll
      for (int p = 0; p < 8; ++p) t += *(const LAS f32x2*)(lb + L_MODP + (p * 64 + 2 * l31) * 4);
      const u32 pv = pk2(t[0], t[1]), nv = __builtin_amdgcn_update_dpp(0u, pv, 0xB1, 0xf, 0xf, false);
      st128(wsr, (lane < 32 && !(lane & 1)) ? (u32)(32 * b + lane) * 8 : 0x80000000u, OFF_MODG, (u32x4){pv, epoch_v + 1u, nv, epoch_v + 1u});
      WAIT_VM(0);
      lds_st(lb + L_SYNC + 32, 1u);
    }
  } else if (wave < 3 && 3 * (b - 42) + wave < 512) {
    const int xgp = 3 * (b - 42) + wave, row = 4 * xgp + (lane & 3), c0i = lane >> 2;
    u32x4 xr[4];
#pragma unroll
    for (int n = 0; n < 4; ++n) xr[n] = *(const u32x4*)((const char*)x + (size_t)row * 896 + min(c0i + 16 * n, 55) * 16);
    BAR();
    if (c0i >= 8) xr[3] = (u32x4){0u, 0u, 0u, 0u};
    float s1[2] = {0.f, 0.f}, s2[2] = {0.f, 0.f};
#pragma unroll
    for (int n = 0; n < 4; ++n)
#pragma unroll
      for (int j = 0; j < 4; ++j) { s1[j & 1] = dot2(xr[n][j], 0x3F803F80u, s1[j & 1]); s2[j & 1] = dot2(xr[n][j], xr[n][j], s2[j & 1]); }
    float t1 = s1[0] + s1[1], t2 = s2[0] + s2[1];
    t1 += DPPF(t1, 0x124); t2 += DPPF(t2, 0x124); t1 += DPPF(t1, 0x128); t2 += DPPF(t2, 0x128);
    { auto r = __builtin_amdgcn_permlane16_swap(__float_as_uint(t1), __float_as_uint(t2), false, false); t1 = __uint_as_float(r[0]) + __uint_as_float(r[1]); }
    t1 = xhalf_sum(t1);
    { auto r = __builtin_amdgcn_permlane16_swap(__float_as_uint(t1), __float_as_uint(t1), false, false); t1 = __uint_as_float(r[0]); t2 = __uint_as_float(r[1]); }
    const float mean = t1 * (1.f / 448.f), ex2 = t2 * (1.f / 448.f);
    const float rstd = __builtin_amdgcn_rsqf(fmaxf(ex2 - mean * mean, 0.f) + 1e-5f), c0 = -mean * rstd;
    asm volatile("" :: "v"(c0));

#pragma unroll
    for (int n = 0; n < 4; ++n) {
      u32x4 o;
#pragma unroll
      for (int j = 0; j < 4; ++j) asm("v_cvt_pk_f16_f32 %0, %1, %2" : "=v"(o[j]) : "v"(fmaf(bflo(xr[n][j]), rstd, c0)), "v"(fmaf(bfhi(xr[n][j]), rstd, c0)));
      const u32 c = c0i + 16 * n;
      st128(wsr, c < 56 ? (c >> 1) * 1024 + ((row & 31) + 32 * (c & 1)) * 16 + (u32)(row >> 5) * 28672 : 0x80000000u, OFF_XH, o);
    }
    WAIT_VM(0);
    if (lane == 0) flag32(ws + OFF_XF + xgp * 128, epoch_v + 1u);
  } else if ((wave == 4 || wave == 5) && b < 210) {
    const int u = b - 42, part = u / 56, rem = u - part * 56, hp = rem / 7, kg = rem - hp * 7, s0 = 4 * kg + 2 * (wave - 4);
    const char* src = (const char*)wqkv + (size_t)(16 * s0) * 2688 + part * 896 + hp * 112;
    const u32 stg = lb + L_Q + (wave - 4) * 4096;
#pragma unroll
    for (int i = 0; i < 4; ++i) { const u32 c = lane & 7; dma16<false>(src + i * 8 * 2688, (lane >> 3) * 2688 + 16 * (c == 7 ? 6 : c), stg + i * 1024); }
    BAR();
    WAIT_VM(0);

#pragma unroll
    for (int e = 0; e < 4; ++e) {
      const u32 a = stg + (16 * (e >> 1) + 8 * (grp >> 1) + tq4) * 128 + (e & 1) * 56 + (grp & 1) * 32 + tp4 * 8;
      u32x4 f = cat(ldstr(a), ldstr(a + 512));
      if (l31 >= 28) f = (u32x4){0u, 0u, 0u, 0u};
      st128(wsr, lane * 16, OFF_WQT + (u32)((2 * hp + (e & 1)) * 84 + 3 * (s0 + (e >> 1)) + part) * 1024, f);
    }
    WAIT_VM(0);
    if (lane < 2) flag32(ws + OFF_WQF + ((2 * hp + lane) * 64 + 3 * (s0 >> 1) + part) * 128, epoch_v + 1u);
  } else {
    BAR();
  }
  tag = __builtin_amdgcn_readfirstlane(epoch_v) + 1u;
  STAMPL(7, "unit done");
  const int cg = b / 7, kg = b - cg * 7;
  if (wave >= 6) {
#pragma nounroll
    for (int i = 0; i < 4096 && __builtin_amdgcn_s_memrealtime() - t_entry < PREP_START; ++i) __builtin_amdgcn_s_sleep(1);
#pragma unroll
    for (int ph = 0; ph < 2; ++ph) {
      const int pw = wave & 1, tw = 2 * ph + pw, p = 4 * b + tw;
      const u32 stg = lb + L_STG + tw * 6144;
      if (b < 196) {
#pragma unroll
        for (int i = 0; i < 2; ++i) {
          const u32 vo = (u32)(64 * kg + 32 * ph + 16 * i + (lane >> 2)) * 3584 + (64 * cg + 32 * pw) * 2 + 16 * (lane & 3);
          dma16<false>(wg, vo, stg + (2 * i) * 1024); dma16<false>(wu, vo, stg + (2 * i + 1) * 1024);
        }
      }
      if (p < 784) {
        const int ds_ = p / 7, np = p - ds_ * 7;
#pragma unroll
        for (int i = 0; i < 2; ++i) dma16<false>(wd, (u32)(16 * ds_ + (lane >> 2)) * 896 + np * 128 + i * 64 + 16 * (lane & 3), stg + (4 + i) * 1024);
      } else if (p < 1008) {
        const int q = p - 784, h = q / 14, nq = q - h * 14;
#pragma unroll
        for (int i = 0; i < 2; ++i) dma16<false>(wo, (u32)min(28 * h + 16 * i + (lane >> 2), 447) * 896 + nq * 64 + 16 * (lane & 3), stg + (4 + i) * 1024);
      }
    }
    STAMPL(6, "prep issued");
    WAIT_VM(0);
    if (lane == 0) __hip_atomic_fetch_add((LAS u32*)(lb + L_SYNC + 52), 1u, __ATOMIC_RELAXED, __HIP_MEMORY_SCOPE_WORKGROUP);
    STAMPL(5, "prep landed");
  } else if (wave >= 3) {
#pragma nounroll
    for (int i = 0; i < 4096 && __builtin_amdgcn_s_memrealtime() - t_entry < FIRST_POLL; ++i) __builtin_amdgcn_s_sleep(1);
  }

  if (wave < 4) {
    if (wave == 3) { wait_flags(ws + OFF_XF + (32 * ai + l31) * 128, tag); GATE_MOD; lds_st(lb + L_SYNC, 1u); }
    else lds_wait(lb + L_SYNC, 1);

    STAMPL(3, "xhat go");
    u32x4 xf[28], pf[6]; u32 pfo[6], uvo[2]; float uvv[2];
    constexpr int XPRE = 12;
#define LDX(s) xf[s] = ld128(wsr, lane * 16 + ((s) & 3) * 1024, OFF_XH + (u32)((4 * ai + wave) * 28 + ((s) & ~3)) * 1024)
#pragma unroll
    for (int s = 0; s < XPRE; ++s) { LDX(s); SB(); }
    {
      const int pw = wave & 1, ph = (wave >> 1) & 1, p = 4 * b + wave;
      const u32 stg = lb + L_STG + wave * 6144;
      const bool isd = p < 784;
      const int ds_ = p / 7, np = p - ds_ * 7, q = p - 784, h = q / 14, nq = q - h * 14;
      lds_wait(lb + L_SYNC + 52, 2);
      const int pi = ((tp4 & 1) << 1) | (tp4 >> 1);
      float uu[2] = {0.f, 0.f}, vv[2] = {0.f, 0.f};
#pragma unroll
      for (int i = 0; i < 2; ++i) {
        const u32x4 be = lds128(lb + L_G2 + (2 * ph + i) * 32 + hi * 16), ga = lds128(lb + L_G2 + 128 + (2 * ph + i) * 32 + hi * 16);
#pragma unroll
        for (int e = 0; e < 2; ++e) {
          const u32 a = stg + (2 * i + (grp & 1)) * 1024 + (8 * (grp >> 1) + tq4) * 64 + e * 32 + pi * 8;
          u32x4 f = cat(ldstr(a), ldstr(a + 256));
#pragma unroll
          for (int j = 0; j < 4; ++j) {
            const float w0 = bflo(f[j]), w1 = bfhi(f[j]);
            vv[e] = fma1(w0, bflo(be[j]), vv[e]); vv[e] = fma1(w1, bfhi(be[j]), vv[e]);
            f[j] = pk2(w0 * bflo(ga[j]), w1 * bfhi(ga[j]));
            uu[e] += bflo(f[j]); uu[e] += bfhi(f[j]);
          }
          pf[2 * i + e] = f;
          pfo[2 * i + e] = b < 196 ? OFF_WGU + (u32)((4 * cg + 2 * pw + e) * 28 + 4 * kg + 2 * ph + i) * 1024 : 0x80000000u;
        }
      }
#pragma unroll
      for (int e = 0; e < 2; ++e) {
        const float us = xhalf_sum(uu[e]), vs = xhalf_sum(vv[e]);
        uvv[e] = hi ? vs : us; uvo[e] = b < 196 ? OFF_UV + (u32)((2 * kg + ph) * 112 + 4 * cg + 2 * pw + e) * 256 : 0x80000000u;
      }
#pragma unroll
      for (int i = 0; i < 2; ++i) {
        const u32 a = isd ? stg + (4 + i) * 1024 + (8 * (grp >> 1) + tq4) * 64 + (grp & 1) * 32 + tp4 * 8
                          : stg + 4096 + (16 * (grp >> 1) + 4 * (grp & 1) + tq4) * 64 + i * 32 + tp4 * 8;
        u32x4 f = cat(ldstr(a), ldstr(a + (isd ? 256 : 512)));
        if (!isd && grp == 3) { f[2] = 0u; f[3] = 0u; }
        pf[4 + i] = f;
        pfo[4 + i] = isd ? OFF_WDT + (u32)((2 * np + i) * 112 + ds_) * 1024 : p < 1008 ? OFF_WOT + (u32)((2 * nq + i) * 16 + h) * 1024 : 0x80000000u;
      }
      asm volatile("" : "+v"(pf[0]), "+v"(pf[1]), "+v"(pf[2]), "+v"(pf[3]), "+v"(pf[4]), "+v"(pf[5]), "+v"(uvv[0]), "+v"(uvv[1]));
      STAMPL(10, "prep tiled");
    }
    lds_wait(lb + L_SYNC + 4, 4);

    STAMPL(8, "W1 ready");
    accQ = (f32x16){}; accK = (f32x16){}; accV = (f32x16){};
    {
      u32 gb = lb + L_GAM + hi * 16; asm volatile("" : "+v"(gb));
      u32 wb = lb + lane * 16, wb2 = wb + 43008; asm volatile("" : "+v"(wb), "+v"(wb2));
      u32x4 wf[3][3], hf[2], gs[3], be[3]; float ht[8];
#define RDG(s) gs[(s) % 3] = lds128(gb + (s) * 32)
#define RDB(s) be[(s) % 3] = lds128(gb + 896 + (s) * 32)
#define RDW1(s, p) wf[(s) % 3][p] = lds128(((s) < 14 ? wb : wb2) + (3 * ((s) % 14) + (p)) * 1024)
#define RDW(s) do { RDW1(s, 0); RDW1(s, 1); RDW1(s, 2); } while (0)
#define DOTS(s, h) do { _Pragma("unroll") for (int j = 2 * (h); j < 2 * (h) + 2; ++j) { \
        asm volatile("v_fma_mix_f32 %0, %1, %2, %3 op_sel_hi:[1,1,1]" : "=v"(ht[2 * j]) : "v"(xf[s][j]), "v"(gs[(s) % 3][j]), "v"(be[(s) % 3][j])); \
        asm volatile("v_fma_mix_f32 %0, %1, %2, %3 op_sel:[1,1,1] op_sel_hi:[1,1,1]" : "=v"(ht[2 * j + 1]) : "v"(xf[s][j]), "v"(gs[(s) % 3][j]), "v"(be[(s) % 3][j])); } } while (0)
#define CVTS(s) do { hf[(s) & 1] = (u32x4){pk2(ht[0], ht[1]), pk2(ht[2], ht[3]), pk2(ht[4], ht[5]), pk2(ht[6], ht[7])}; asm volatile("" : "+v"(hf[(s) & 1])); } while (0)
      RDG(0); RDB(0); RDW(0); RDG(1); RDB(1); RDW(1); RDG(2); RDB(2); RDW(2); DOTS(0, 0); DOTS(0, 1); CVTS(0); SB();
#pragma unroll
      for (int s = 0; s < 28; ++s) {
        if (s == 1) lds_wait(lb + L_SYNC + 8, 3);
        if (s == 13) lds_wait(lb + L_SYNC + 24, 3);
        SB(); accQ = mfma32(wf[s % 3][0], hf[s & 1], accQ); SB();
        if (s < 25) RDG(s + 3);
        if (s < 25) RDW1(s + 3, 0);
        if (s < 27) DOTS(s + 1, 0);
        SB(); accK = mfma32(wf[s % 3][1], hf[s & 1], accK); SB();
        if (s < 25) RDB(s + 3);
        if (s < 25) RDW1(s + 3, 1);
        if (s < 27) DOTS(s + 1, 1);
        SB(); accV = mfma32(hf[s & 1], wf[s % 3][2], accV); SB();
        if (s < 25) RDW1(s + 3, 2);
        if (s + XPRE < 28) LDX(s + XPRE);
        else if (s < 22) st128(wsr, lane * 16 + pfo[s - 16], 0, pf[s - 16]);
        else if (s < 24) st32(wsr, lane * 4 + uvo[s - 22], 0, __float_as_uint(uvv[s - 22]));
        if (s < 27) CVTS(s + 1);
        SB();
      }
    }
    STAMPL(5, "qkv mfma done");
    const float unscale = __uint_as_float(__builtin_amdgcn_readfirstlane(lds_ld(lb + L_SYNC + 28)));
    if (unscale != 1.f) { accQ *= unscale; accK *= unscale; accV *= unscale; }
    u32x4 k0, k1, v0, v1, q0, q1;
#pragma unroll
    for (int j = 0; j < 4; ++j) {
      k0[j] = pk2(accK[2 * j], accK[2 * j + 1]); k1[j] = pk2(accK[8 + 2 * j], accK[9 + 2 * j]);
      v0[j] = pk2(accV[2 * j], accV[2 * j + 1]); v1[j] = pk2(accV[8 + 2 * j], accV[9 + 2 * j]);
    }
    if (hi) { k1[2] = 0x3F803F80u; k1[3] = 0x00003F80u; }
    if (l31 == 28) { v0 = (u32x4){0x3F803F80u, 0x3F803F80u, 0x3F803F80u, 0x3F803F80u}; v1 = v0; }
    const u32 kvo = OFF_KV + (u32)(ah * 64 + ai * 4 + wave) * 4096;
    st128(wsr, lane * 16, kvo, k0); st128(wsr, lane * 16 + 1024, kvo, k1); st128(wsr, lane * 16 + 2048, kvo, v0); st128(wsr, lane * 16 + 3072, kvo, v1);
    SB();
    float kn = 0.f, qn = 0.f;
    constexpr float QS = 0.18898223650461362f * 1.4426950408889634f;
#pragma unroll
    for (int r = 0; r < 16; ++r) { kn = fmaf(accK[r], accK[r], kn); accQ[r] *= QS; qn = fmaf(accQ[r], accQ[r], qn); }
#pragma unroll
    for (int j = 0; j < 4; ++j) { q0[j] = pk2(accQ[2 * j], accQ[2 * j + 1]); q1[j] = pk2(accQ[8 + 2 * j], accQ[9 + 2 * j]); }
    *(LAS u32x4*)(lb + L_Q + wave * 2048 + lane * 16) = q0; *(LAS u32x4*)(lb + L_Q + wave * 2048 + 1024 + lane * 16) = q1;
    qn = xhalf_sum(qn); kn = wave_max(xhalf_sum(kn));
    if (!hi) *(LAS float*)(lb + L_QN + (wave * 32 + l31) * 4) = qn;
    WAIT_VM(0);
    if (lane == 0) flag64(ws + OFF_KVG + (ah * 64 + ai * 4 + wave) * 128, __float_as_uint(kn), tag);
    if (b < 42) lds_wait(lb + L_SYNC + 32, 1);
    if (lane == 0 && __hip_atomic_fetch_add((LAS u32*)(lb + L_SYNC + 44), 1u, __ATOMIC_RELAXED, __HIP_MEMORY_SCOPE_WORKGROUP) == 3u) flag32(ws + OFF_PREPF + b * 128, tag);
    STAMPL(6, "kv flagged");
  } else {
    if (wave == 4) {
      u64 gv[8];
      __builtin_amdgcn_s_setprio(3);
#pragma nounroll
      for (int i = 0; i < SPIN_CAP; ++i) {
        bool ok = true;
#pragma unroll
        for (int n = 0; n < 7; ++n) gv[n] = poll64(ws + OFF_MODG + (64 * n + lane) * 8);
        gv[7] = poll64(ws + OFF_MODG + (672 + 224 * hi + 32 * kg + l31) * 8);
#pragma unroll
        for (int n = 0; n < 8; ++n) ok = ok && (u32)(gv[n] >> 32) == tag;
        if (__all(ok)) break;
        __builtin_amdgcn_s_sleep(POLL_SLEEP);
      }
      CFENCE();
      __builtin_amdgcn_s_setprio(0);
      *(LAS u32*)(lb + L_G2 + lane * 4) = hi ? pk2(1.f + bflo((u32)gv[7]), 1.f + bfhi((u32)gv[7])) : (u32)gv[7];
      WAIT_LGKM();
      lds_st(lb + L_SYNC + 36, 1u);

      float lo[7], hi2[7], mxv = 0.f;
#pragma unroll
      for (int n = 0; n < 7; ++n) {
        u32 v = (u32)gv[n];
        if (64 * n + lane >= 224) v = pk2(1.f + bflo(v), 1.f + bfhi(v));
        lo[n] = bflo(v); hi2[n] = bfhi(v); mxv = fmaxf(mxv, fmaxf(fabsf(lo[n]), fabsf(hi2[n])));
      }
      const u32 ex = max((int)(__float_as_uint(wave_max(mxv)) >> 23) - (127 + 14), 0);
      const float scl = __uint_as_float((127u - min(ex, 114u)) << 23);
#pragma unroll
      for (int n = 0; n < 7; ++n) {
        u32 o; asm("v_cvt_pk_f16_f32 %0, %1, %2" : "=v"(o) : "v"(lo[n] * scl), "v"(hi2[n] * scl));
        const int g = 64 * n + lane;
        *(LAS u32*)(lb + L_GAM + (g >= 224 ? g - 224 : g + 224) * 4) = o;
      }
      lds_st(lb + L_SYNC + 28, (127u + min(ex, 114u)) << 23);
      WAIT_LGKM();
      if (lane == 0) __hip_atomic_fetch_add((LAS u32*)(lb + L_SYNC + 4), 1u, __ATOMIC_RELAXED, __HIP_MEMORY_SCOPE_WORKGROUP);
    } else {
      if (wave == 5) { wait_flags(ws + OFF_WQF + (ah * 64 + min(lane, 41)) * 128, tag); GATE_MOD; lds_st(lb + L_SYNC + 48, 1u); }
      else lds_wait(lb + L_SYNC + 48, 1);

      const char* wsrc = ws + OFF_WQT + (size_t)ah * 84 * 1024 + (wave - 5) * 4096;
#pragma unroll
      for (int n = 0; n < 7; ++n) dma16x4(wsrc + n * 12288, lane * 16, lb + (wave - 5) * 4096 + n * 12288);
    }
    if (wave >= 5) {
      WAIT_VM(24);
      if (lane == 0) __hip_atomic_fetch_add((LAS u32*)(lb + L_SYNC + 4), 1u, __ATOMIC_RELAXED, __HIP_MEMORY_SCOPE_WORKGROUP);
      WAIT_VM(12);
      if (lane == 0) __hip_atomic_fetch_add((LAS u32*)(lb + L_SYNC + 8), 1u, __ATOMIC_RELAXED, __HIP_MEMORY_SCOPE_WORKGROUP);
      WAIT_VM(0);
      if (lane == 0) __hip_atomic_fetch_add((LAS u32*)(lb + L_SYNC + 24), 1u, __ATOMIC_RELAXED, __HIP_MEMORY_SCOPE_WORKGROUP);
      STAMPL(9, "W landed");
    }
  }
  BAR();
  STAMPL(11, "B3");

  const int ort = 8 * xg + (slot >> 2), ocq = slot & 3;
  const int gi = ai, gfj = ah;
  u32x4 wo_f[16], wgu[28], xres, gaA, gaB;
  {
    u32x4 qf[4][2];
#pragma unroll
    for (int t = 0; t < 4; ++t) { qf[t][0] = lds128(lb + L_Q + t * 2048 + lane * 16); qf[t][1] = lds128(lb + L_Q + t * 2048 + 1024 + lane * 16); }
    float qn2[4];
#pragma unroll
    for (int t = 0; t < 4; ++t) qn2[t] = lds32f(lb + L_QN + (t * 32 + l31) * 4);
    WAIT_LGKM();
    if (lane == 0) __hip_atomic_fetch_add((LAS u32*)(lb + L_SYNC + 40), 1u, __ATOMIC_RELAXED, __HIP_MEMORY_SCOPE_WORKGROUP);
    if (wave == 3) {
      __builtin_amdgcn_s_sleep(KV_SLEEP);
      const float km = wave_max(__uint_as_float(wait_gran(ws + OFF_KVG + (ah * 64 + lane) * 128, tag)));
      lds_st(lb + L_SYNC + 60, __float_as_uint(km));
      WAIT_LGKM();
      lds_st(lb + L_SYNC + 56, 1u);
    } else lds_wait(lb + L_SYNC + 56, 1);
    const float kmax2 = __uint_as_float(lds_ld(lb + L_SYNC + 60));
    STAMPL(12, "kv flags seen");
    const u32 kvb = OFF_KV + (u32)(ah * 64 + 8 * wave) * 4096;
    u32x4 kf[2][2], vf[2][2];
    const u32 kvl = lane * 16;
#define LDKV(D, kt, off) asm volatile("buffer_load_dwordx4 %0, %2, %3, %4 offen offset:" #off " sc1\n\tbuffer_load_dwordx4 %1, %2, %3, %4 offen offset:" #off "+1024 sc1" \
      : "=&v"(D[(kt) & 1][0]), "=&v"(D[(kt) & 1][1]) : "v"(kvl), "s"(wsr), "s"(kvb + (kt) * 4096) : "memory")
#define WAITN(n) asm volatile("s_waitcnt vmcnt(%0)" :: "i"(n) : "memory")
#define PINKV(D, kt) asm volatile("" : "+v"(D[(kt) & 1][0]), "+v"(D[(kt) & 1][1]))
#define WAITKV(D, kt, n) do { WAITN(n); SB(); PINKV(D, kt); } while (0)
#define WAITKV2(D, kt, n0, n1) do { if (wave < 4) WAITN(n0); else WAITN(n1); SB(); PINKV(D, kt); } while (0)
    asm volatile("s_nop 4");
    LDKV(kf, 0, 0); LDKV(vf, 0, 2048); LDKV(kf, 1, 0);
    float sh[4]; bool safe = true;
#pragma unroll
    for (int t = 0; t < 4; ++t) { sh[t] = 1.02f * __builtin_amdgcn_sqrtf(qn2[t] * kmax2); safe = safe && (sh[t] <= 48.f); }
    if (!__all(safe)) {
      BAR();
      float mx[4] = {-INFINITY, -INFINITY, -INFINITY, -INFINITY};
#pragma unroll 1
      for (int kt = 0; kt < 8; ++kt) {
        const u32x4 ka = ld128(wsr, lane * 16, kvb + kt * 4096), kb = ld128(wsr, lane * 16 + 1024, kvb + kt * 4096);
#pragma unroll
        for (int t = 0; t < 4; ++t) {
          f32x16 s = mfma32(ka, qf[t][0], (f32x16){}); s = mfma32(kb, qf[t][1], s);
#pragma unroll
          for (int r = 0; r < 16; ++r) mx[t] = fmaxf(mx[t], s[r]);
        }
      }
#pragma unroll
      for (int t = 0; t < 4; ++t) { const float m = xhalf_max(mx[t]); if (!hi) *(LAS float*)(lb + L_MX + (wave * 128 + t * 32 + l31) * 4) = m; }
      BAR();
#pragma unroll
      for (int t = 0; t < 4; ++t) {
        float m = -INFINITY;
#pragma unroll
        for (int w = 0; w < 8; ++w) m = fmaxf(m, lds32f(lb + L_MX + (w * 128 + t * 32 + l31) * 4));
        sh[t] = m;
      }
    }
    if (hi) {
#pragma unroll
      for (int t = 0; t < 4; ++t) {
        const float a = rbf(-sh[t]), r1 = -sh[t] - a, c = rbf(r1);
        qf[t][1][2] = pk2(a, c); qf[t][1][3] = pk2(r1 - c, 0.f);
      }
    }
    f32x16 o[4] = {(f32x16){}, (f32x16){}, (f32x16){}, (f32x16){}};
    WAITKV(kf, 0, 4);
    f32x16 sc = mfma32(kf[0][0], qf[0][0], (f32x16){}); sc = mfma32(kf[0][1], qf[0][1], sc);
    u32x4 p0, p1;
#define EXP2(v) __builtin_amdgcn_exp2f(v)
    float ec, ed;
#define GRP(r, PREV, MF, CUR) do { float ea = EXP2(sc[r]), eb = EXP2(sc[(r) + 1]); asm volatile("" : "+v"(ea), "+v"(eb)); PREV; SB(); MF; SB(); \
      ec = EXP2(sc[(r) + 2]); ed = EXP2(sc[(r) + 3]); asm volatile("" : "+v"(ec), "+v"(ed)); { u32 pa = pk2(ea, eb); asm volatile("" : "+v"(pa)); CUR = pa; } SB(); } while (0)
#define PKP(D) do { u32 pb = pk2(ec, ed); asm volatile("" : "+v"(pb)); D = pb; } while (0)
    u32 pwb = lb + wave * 8192 + lane * 16, lwb = lb + L_LP + wave * 512 + l31 * 4; asm volatile("" : "+v"(pwb), "+v"(lwb));
#define WRP(t) do { _Pragma("unroll") for (int g = 0; g < 2; ++g) *(LAS u32x4*)(pwb + ((t) * 2 + g) * 1024) = (u32x4){pk2(o[t][8 * g], o[t][8 * g + 1]), pk2(o[t][8 * g + 2], o[t][8 * g + 3]), pk2(o[t][8 * g + 4], o[t][8 * g + 5]), pk2(o[t][8 * g + 6], o[t][8 * g + 7])}; \
      if (hi) *(LAS float*)(lwb + (t) * 128) = o[t][12]; } while (0)
    const int w6 = min(wave, 6);
    const u32 wo_base = OFF_WOT + (u32)(7 * ocq + w6) * 16384, wg_base = OFF_WGU + (u32)(7 * gfj + w6) * 28672;
    auto nslot = [](int kt, int t, bool) constexpr { const int sl = 4 * (kt - 2) + t;
      return (t == 0 && kt < 7 ? 2 : 0) + (t == 2 && kt < 6 ? 2 : 0) + (kt >= 2 ? (sl == 0) + (sl < 16) + (sl >= 6 && sl < 18) : 0); };
    auto nafter = [&](int k0, int t0, int k1, int t1, bool young) constexpr { int n = -2; for (int i = 4 * k0 + t0; i <= 4 * k1 + t1; ++i) n += nslot(i >> 2, i & 3, young); return n; };
#pragma unroll
    for (int kt = 0; kt < 8; ++kt) {
      if (kt == 0) __builtin_amdgcn_s_setprio(3);
      if (kt == 2) __builtin_amdgcn_s_setprio(2);
      if (kt == 4) __builtin_amdgcn_s_setprio(1);
      if (kt == 6 && wave < 4) __builtin_amdgcn_s_setprio(0);
      if (kt == 7) __builtin_amdgcn_s_setprio(0);
#pragma unroll
      for (int t = 0; t < 4; ++t) {
        f32x16 sn;
        const bool more = t < 3 || kt < 7, first = t == 0 && kt == 0;
        const int kn = t < 3 ? (kt & 1) : ((kt + 1) & 1), tn = (t + 1) & 3;
        if (t == 3 && kt < 7) {
          if (kt == 0) WAITKV(kf, 1, nafter(0, 0, 0, 2, false) + 2);
          else if (kt == 1) WAITKV(kf, 2, nafter(0, 2, 1, 2, false));
          else WAITKV(kf, kt + 1, nafter(kt - 1, 2, kt, 2, false));
        }
        GRP(0, if (!first) PKP(p1[3]), if (more) sn = mfma32(kf[kn][0], qf[tn][0], (f32x16){}), p0[0]);
        GRP(4, PKP(p0[1]), if (more) sn = mfma32(kf[kn][1], qf[tn][1], sn), p0[2]);
        GRP(8, PKP(p0[3]), if (t > 0) o[t - 1] = mfma32(vf[kt & 1][1], p1, o[t - 1]); else if (kt > 0) o[3] = mfma32(vf[(kt + 1) & 1][1], p1, o[3]), p1[0]);
        if (t == 0 && kt < 7) LDKV(vf, kt + 1, 2048);
        if (t == 2 && kt < 6) LDKV(kf, kt + 2, 0);
        if (kt == 1 && t == 0 && wave == 3) {
#pragma nounroll
          for (int i = 0; i < SPIN_CAP; ++i) {
            const u32 a = poll32(ws + OFF_PREPF + lane * 128), c = poll32(ws + OFF_PREPF + (64 + lane) * 128), d = poll32(ws + OFF_PREPF + (128 + lane) * 128), e = poll32(ws + OFF_PREPF + (192 + lane) * 128);
            if (__all(a == tag && c == tag && d == tag && e == tag)) break;
            __builtin_amdgcn_s_sleep(8);
          }
          CFENCE();
          if (lane == 0) __hip_atomic_fetch_add((LAS u32*)(lb + L_SYNC + 40), 1u, __ATOMIC_RELAXED, __HIP_MEMORY_SCOPE_WORKGROUP);
        }
        if (t == 0) {
          if (kt == 2) STAMPL(1, "kt2");
          if (kt == 4) STAMPL(2, "kt4");
          if (kt == 6) STAMPL(4, "kt6");
        }
        if (kt >= 2) {
          const int sl = 4 * (kt - 2) + t;
          if (sl == 0) {
            lds_wait(lb + L_SYNC + 40, 9);
            dma4_touch(ws + wg_base + 12288, ((slot & 3) * 32 + l31) * 128 + hi * 64, lb + L_ST + w6 * 256);
          }
          if (sl < 10) wo_f[sl] = ld128(wsr, lane * 16, wo_base + sl * 1024);
          else if (sl < 16) wgu[sl - 10] = ld128(wsr, lane * 16, wg_base + (sl - 10) * 1024);
          if (sl >= 6 && sl < 18) dma16<true>(ws + (sl < 12 ? wo_base + (4 + sl) * 1024 : wg_base + (sl - 6) * 1024), lane * 16, lb + L_PARK + w6 * 12288 + (sl - 6) * 1024);
        }
        if (t == 0) {
          if (kt == 0) WAITKV(vf, 0, nafter(0, 0, 0, 0, false) + 4);
          else if (kt == 1) WAITKV(vf, 1, nafter(0, 0, 1, 0, false));
          else WAITKV(vf, kt, nafter(kt - 1, 0, kt, 0, false));
        }
        SB();
        GRP(12, PKP(p1[1]), o[t] = mfma32(vf[kt & 1][0], p0, o[t]), p1[2]);
        if (kt == 7 && t > 0) { WRP(t - 1); SB(); }
        if (more) sc = sn;
      }
    }
    PKP(p1[3]);
    o[3] = mfma32(vf[1][1], p1, o[3]);
    WRP(3);
    STAMPL(13, "attn done");
    BAR();
    const int qt = wave >> 1, sf = wave & 1;
    u32x4 pa[8]; float pl[8];
    {
      u32 base = lb + qt * 2048 + sf * 1024 + lane * 16, lbase = lb + L_LP + qt * 128 + l31 * 4;
      asm volatile("" : "+v"(base), "+v"(lbase));
#pragma unroll
      for (int w = 0; w < 8; ++w) { pa[w] = lds128(base + w * 8192); pl[w] = lds32f(lbase + w * 512); }
      SB();
    }
    float a8[8]; u32 sel0 = 0x00003F80u, sel1 = 0x3F800000u; asm volatile("" : "+v"(sel0), "+v"(sel1));
#pragma unroll
    for (int j = 0; j < 4; ++j) {
      a8[2 * j] = bflo(pa[0][j]); a8[2 * j + 1] = bfhi(pa[0][j]);
#pragma unroll
      for (int w = 1; w < 8; ++w) { a8[2 * j] = dot2(pa[w][j], sel0, a8[2 * j]); a8[2 * j + 1] = dot2(pa[w][j], sel1, a8[2 * j + 1]); }
    }
    const float l = ((pl[0] + pl[4]) + (pl[1] + pl[5])) + ((pl[2] + pl[6]) + (pl[3] + pl[7]));
    const float rl = __builtin_amdgcn_rcpf(l);
    const u32x4 of = {pk2(a8[0] * rl, a8[1] * rl), pk2(a8[2] * rl, a8[3] * rl), pk2(a8[4] * rl, a8[5] * rl), pk2(a8[6] * rl, a8[7] * rl)};
    st128(wsr, (l31 >> 4) * 1024 + ((l31 & 15) + 16 * (2 * sf + hi)) * 16, OFF_OB + (u32)((4 * ai + qt) * 16 + ah) * 2048, of);
    WAIT_VM(0);
    if (lane == 0) flag32(ws + OFF_OF + (((4 * ai + qt) * 16 + ah) * 2 + sf) * 128, tag);
    BAR();
    STAMPL(14, "O flagged");
  }

  u32 uvp[14];
  const int otok = 16 * ((lane >> 4) & 1) + (lane & 15), ofe = 112 * ocq + 16 * min(wave, 6) + 8 * hi;
  if (wave < 7) {
    xres = *(const u32x4*)(x + (size_t)(32 * ort + otok) * 448 + ofe);
    gaA = ld128(wsr, ofe * 4, OFF_MODG + 448 * 8); gaB = ld128(wsr, ofe * 4 + 16, OFF_MODG + 448 * 8);
#pragma unroll
    for (int k = 0; k < 14; ++k) uvp[k] = ld32(wsr, lane * 4, OFF_UV + (u32)(k * 112 + 7 * gfj + wave) * 256);
#pragma unroll
    for (int s = 12; s < 28; ++s) wgu[s] = ld128(wsr, lane * 16 + (s & 3) * 1024, OFF_WGU + (u32)((7 * gfj + wave) * 28 + (s & ~3)) * 1024);
#pragma unroll
    for (int j = 0; j < 6; ++j) wo_f[10 + j] = lds128(lb + L_PARK + wave * 12288 + j * 1024 + lane * 16);
#pragma unroll
    for (int j = 6; j < 12; ++j) wgu[j] = lds128(lb + L_PARK + wave * 12288 + j * 1024 + lane * 16);
    lds_wait(lb + L_SYNC + 16, 1);
  } else {
    wait_flags(ws + OFF_OF + (ort * 32 + l31) * 128, tag);
    lds_st(lb + L_SYNC + 16, 1u);
  }
  STAMPL(15, "O flags seen");
  {
    const char* osrc = ws + OFF_OB + (size_t)(ort * 16 + 2 * wave) * 2048;
    dma16x4(osrc, lane * 16, lb + 4 * wave * 1024);
  }
  __builtin_amdgcn_s_waitcnt(0x0F70);
  CFENCE();
  BAR();
  STAMPL(16, "O landed");
  if (wave < 7) {
    f32x4 y0 = {0.f, 0.f, 0.f, 0.f}, y1 = y0;
    u32x4 oa[8];
#pragma unroll
    for (int i = 0; i < 6; ++i) oa[i] = lds128(lb + i * 1024 + lane * 16);
#pragma unroll
    for (int h = 0; h < 16; ++h) {
      if (h < 13) { oa[(2 * h + 6) & 7] = lds128(lb + (2 * h + 6) * 1024 + lane * 16); oa[(2 * h + 7) & 7] = lds128(lb + (2 * h + 7) * 1024 + lane * 16); }
      SB(); y0 = mfma16(wo_f[h], oa[(2 * h) & 7], y0); y1 = mfma16(wo_f[h], oa[(2 * h + 1) & 7], y1); SB();
    }
    float yv[8];
#pragma unroll
    for (int r = 0; r < 4; ++r) { auto sw = __builtin_amdgcn_permlane16_swap(__float_as_uint(y0[r]), __float_as_uint(y1[r]), false, false); yv[r] = __uint_as_float(sw[0]); yv[4 + r] = __uint_as_float(sw[1]); }
    const u32 gw[4] = {gaA[0], gaA[2], gaB[0], gaB[2]};
    u32x4 x2p; float s1 = 0.f, s2 = 0.f;
#pragma unroll
    for (int j = 0; j < 4; ++j) {
      x2p[j] = pk2(fmaf(bflo(gw[j]), yv[2 * j], bflo(xres[j])), fmaf(bfhi(gw[j]), yv[2 * j + 1], bfhi(xres[j])));
      s1 = dot2(x2p[j], 0x3F803F80u, s1); s2 = dot2(x2p[j], x2p[j], s2);
    }
    const u32 fo = (u32)(ort * 28 + 7 * ocq + wave) * 1024;
    st128(wsr, (otok + 32 * hi) * 16, OFF_X2 + fo, x2p);
    s1 = xhalf_sum(s1); s2 = xhalf_sum(s2);
    *(LAS float*)(lb + L_ST + wave * 256 + (hi * 32 + otok) * 4) = hi ? s2 : s1;
    WAIT_VM(0);
  }
  BAR();
  if (wave == 7) {
    float t = 0.f;
#pragma unroll
    for (int w = 0; w < 7; ++w) t += lds32f(lb + L_ST + w * 256 + lane * 4);
    const u32 nv = __builtin_amdgcn_update_dpp(0u, __float_as_uint(t), 0xB1, 0xf, 0xf, false);
    st128(wsr, !(lane & 1) ? (u32)lane * 8 : 0x80000000u, OFF_X2G + (u32)(ort * 4 + ocq) * 512, (u32x4){__float_as_uint(t), tag, nv, tag});
  } else {
    float uvs = 0.f;
#pragma unroll
    for (int k = 0; k < 14; ++k) uvs += __uint_as_float(uvp[k]);
    *(LAS float*)(lb + L_UV + wave * 256 + lane * 4) = uvs;
  }
  STAMPL(17, "x2 flagged");

  const int dsl = slot & 15, dq = 2 * (slot >> 4) + dsl / 7, dc = dsl % 7;
  const bool dact = dsl < 14;
  u32x4 wdf[28], x2r, gbA, gbB;
  if (wave == 7) {
    u64 g[16]; const char* gp = ws + OFF_X2G + (16 * gi * 64 + lane) * 8;
#pragma nounroll
    for (int i = 0; i < SPIN_CAP; ++i) {
      bool ok = true;
#pragma unroll
      for (int p = 0; p < 16; ++p) g[p] = poll64(gp + p * 512);
#pragma unroll
      for (int p = 0; p < 16; ++p) ok = ok && (u32)(g[p] >> 32) == tag;
      if (__all(ok)) break;
      __builtin_amdgcn_s_sleep(1);
    }
    CFENCE();
#pragma unroll
    for (int p = 0; p < 16; ++p) *(LAS u32*)(lb + L_SP2 + p * 256 + lane * 4) = (u32)g[p];
  }
  BAR();
  STAMPL(18, "x2 flags seen");
  {
    const char* asrc = ws + OFF_X2 + (size_t)(4 * gi) * 28 * 1024;
#pragma unroll
    for (int n = 0; n < 4; ++n) { const int g = min(wave + 8 * n, 27); dma16x4(asrc + g * 4096, lane * 16, lb + g * 4096); }
  }
  WAIT_VM(12);
  BAR();
  STAMPL(19, "A0 landed");
  const int dm = wave >> 2, dn = (wave >> 1) & 1, dgp = wave & 1;
  const u32 wdo = OFF_WDT + (u32)(2 * dc * 112 + 14 * wave) * 1024;
  if (wave < 7) {
    u32 sb = lb + L_SP2 + l31 * 4, ub = lb + L_UV + wave * 256 + hi * 16; asm volatile("" : "+v"(sb), "+v"(ub));
    u32 ab = lb + lane * 16, ab2 = ab + 57344; asm volatile("" : "+v"(ab), "+v"(ab2));
    f32x16 acc[2]; u32x4 af[4]; f32x4 uv[4]; float st[8], rs = 0.f, nm = 0.f, hv[8];
#define EP_RD(T) do { _Pragma("unroll") for (int c = 0; c < 4; ++c) { st[c] = lds32f(sb + ((T) * 4 + c) * 256); st[4 + c] = lds32f(sb + ((T) * 4 + c) * 256 + 128); } } while (0)
#define EP_UV(g) do { uv[0] = lds128f(ub + 32 * (g)); uv[1] = lds128f(ub + 32 * (g) + 64); uv[2] = lds128f(ub + 32 * (g) + 128); uv[3] = lds128f(ub + 32 * (g) + 192); } while (0)
#define EP_ST() do { float m = ((st[0] + st[1]) + (st[2] + st[3])) * (1.f / 448.f); asm volatile("" : "+v"(m)); const float e2 = (((st[4] + st[5]) + st[6]) + st[7]) * (1.f / 448.f); \
      rs = __builtin_amdgcn_rsqf(fmaxf(e2 - m * m, 0.f) + 1e-5f); nm = -m * rs; } while (0)
    float gt, up, ez;
    constexpr float K1 = -2.f * 0.7978845608028654f * 1.4426950408889634f, K3 = K1 * 0.044715f;
#define EP_A(A, e) do { const float tg = fma1(nm, uv[0][(e) & 3], uv[2][(e) & 3]), tu = fma1(nm, uv[1][(e) & 3], uv[3][(e) & 3]); gt = fma1(A[e], rs, tg); up = fma1(A[8 + (e)], rs, tu); asm volatile("" : "+v"(gt), "+v"(up)); } while (0)
#define EP_B() do { ez = __builtin_amdgcn_exp2f(gt * fmaf(gt * gt, K3, K1)); up *= gt; asm volatile("" : "+v"(ez), "+v"(up)); } while (0)
#define EP_C(e) do { hv[e] = up * __builtin_amdgcn_rcpf(1.f + ez); asm volatile("" : "+v"(hv[e])); } while (0)
#define EP_EL(A, e) do { EP_A(A, e); EP_B(); EP_C(e); } while (0)
#define EP_OUT(T) st128(wsr, lane * 16, OFF_HB + (u32)((4 * gi + (T)) * 112 + 7 * gfj + wave) * 1024, (u32x4){pk2(hv[0], hv[1]), pk2(hv[2], hv[3]), pk2(hv[4], hv[5]), pk2(hv[6], hv[7])})
#pragma unroll
    for (int i = 0; i < 3; ++i) af[i] = lds128(ab + i * 1024);
#pragma unroll
    for (int t = 0; t < 4; ++t) {
#pragma unroll
      for (int s = 0; s < 28; ++s) {
        const int n = t * 28 + s;
        if (n == 25) { STAMPL(28, "gu n25"); WAIT_VM(0); BAR();  }
        if (n + 3 < 112) af[(n + 3) & 3] = (n + 3 < 56) ? lds128(ab + (n + 3) * 1024) : lds128(ab2 + (n + 3 - 56) * 1024);
        SB();
        if (s == 0) acc[t & 1] = mfma32(wgu[s], af[n & 3], (f32x16){}); else acc[t & 1] = mfma32(wgu[s], af[n & 3], acc[t & 1]);
        SB();
        if (t > 0) {
          if (s == 0) { EP_RD(t - 1); EP_UV(0); }
          if (s == 2) EP_ST();
          if (s >= 3 && s < 27) { if (s % 3 == 0) EP_A(acc[(t - 1) & 1], s / 3 - 1); else if (s % 3 == 1) EP_B(); else EP_C(s / 3 - 1); }
          if (s == 13) EP_UV(1);
          if (s == 27) EP_OUT(t - 1);
          if (s == 27 && t == 1) STAMPL(20, "gu tile1");
          if (s == 27 && t == 2) STAMPL(21, "gu tile2");
          if (s == 27 && t == 3) STAMPL(22, "gu tile3");
          if (s % 4 == 1 && s < 24) { const int f = 6 * (t - 1) + s / 4; wdf[f] = ld128(wsr, lane * 16, wdo + (u32)((f & 1) * 112 + (f >> 1)) * 1024); }
          SB();
        }
      }
    }
    asm volatile("s_nop 11" : "+v"(acc[1]));
    EP_RD(3); EP_UV(0); EP_ST();
    {
      f32x4 uw[4];
      uw[0] = lds128f(ub + 32); uw[1] = lds128f(ub + 96); uw[2] = lds128f(ub + 160); uw[3] = lds128f(ub + 224);
      float g8[8], u8[8];
#pragma unroll
      for (int e = 0; e < 8; ++e) {
        const f32x4* W = e < 4 ? uv : uw;
        g8[e] = fma1(acc[1][e], rs, fma1(nm, W[0][e & 3], W[2][e & 3])); u8[e] = fma1(acc[1][8 + e], rs, fma1(nm, W[1][e & 3], W[3][e & 3]));
      }
#pragma unroll
      for (int e = 0; e < 8; ++e) hv[e] = (g8[e] * u8[e]) * __builtin_amdgcn_rcpf(1.f + __builtin_amdgcn_exp2f(g8[e] * fmaf(g8[e] * g8[e], K3, K1)));
    }
    EP_OUT(3);
    WAIT_VM(0);
    if (lane == 0) flag32(ws + OFF_HF + ((gi * 16 + gfj) * 8 + wave) * 128, tag);
  } else {
    WAIT_VM(0); BAR();
#pragma unroll
    for (int f = 0; f < 18; ++f) wdf[f] = ld128(wsr, lane * 16, wdo + (u32)((f & 1) * 112 + (f >> 1)) * 1024);
  }
  STAMPL(23, "h flagged");
  if (dact) {
    BAR();
#pragma unroll
    for (int f = 18; f < 28; ++f) wdf[f] = ld128(wsr, lane * 16, wdo + (u32)((f & 1) * 112 + (f >> 1)) * 1024);
    const int drt = 8 * xg + 2 * dq, dcol = 64 * dc + 32 * dn + 16 * dgp + 8 * hi;
    gbA = ld128(wsr, dcol * 4, OFF_MODG + 1120 * 8); gbB = ld128(wsr, dcol * 4 + 16, OFF_MODG + 1120 * 8);
    wait_flags(ws + OFF_HF + (((drt >> 2) * 16 + 2 * wave + ((lane >> 3) & 1)) * 8 + min(lane & 7, 6)) * 128, tag);
    STAMPL(24, "h flags seen");
    u32x4 hf[20];
#define LDH(i) hf[(i) % 20] = ld128(wsr, lane * 16 + (((i) % 14) & 3) * 1024, OFF_HB + (u32)((drt + (i) / 14) * 112 + 14 * wave + (((i) % 14) & ~3)) * 1024)
#pragma unroll
    for (int i = 0; i < 20; ++i) { LDH(i); SB(); }
    x2r = ld128(wsr, lane * 16, OFF_X2 + (u32)((drt + dm) * 28 + 4 * dc + 2 * dn + dgp) * 1024);
    f32x16 c0, c1;
    u32 pwb = lb + wave * 16384 + lane * 16; asm volatile("" : "+v"(pwb));
#pragma unroll
    for (int i = 0; i < 28; ++i) {
      const int m = i / 14, j = i % 14;
      SB();
      if (j == 0) { c0 = mfma32(wdf[0], hf[i % 20], (f32x16){}); c1 = mfma32(wdf[1], hf[i % 20], (f32x16){}); }
      else { c0 = mfma32(wdf[2 * j], hf[i % 20], c0); c1 = mfma32(wdf[2 * j + 1], hf[i % 20], c1); }
      SB();
      if (i < 8) LDH(i + 20);
      if (j == 13) {
#pragma unroll
        for (int g = 0; g < 4; ++g) {
          *(LAS f32x4*)(pwb + ((2 * m) * 4 + g) * 1024) = (f32x4){c0[4 * g], c0[4 * g + 1], c0[4 * g + 2], c0[4 * g + 3]};
          *(LAS f32x4*)(pwb + ((2 * m + 1) * 4 + g) * 1024) = (f32x4){c1[4 * g], c1[4 * g + 1], c1[4 * g + 2], c1[4 * g + 3]};
        }
      }
    }
    STAMPL(25, "down mfma done");
    BAR();
    f32x4 pa[8], pb[8];
    {
      u32 base = lb + (2 * dm + dn) * 4096 + dgp * 2048 + lane * 16, base2 = base + 65536;
      asm volatile("" : "+v"(base), "+v"(base2));
#pragma unroll
      for (int w = 0; w < 4; ++w) {
        pa[w] = lds128f(base + w * 16384); pb[w] = lds128f(base + w * 16384 + 1024);
        pa[4 + w] = lds128f(base2 + w * 16384); pb[4 + w] = lds128f(base2 + w * 16384 + 1024);
      }
      SB();
    }
    const f32x4 a0 = ((pa[0] + pa[4]) + (pa[1] + pa[5])) + ((pa[2] + pa[6]) + (pa[3] + pa[7])), a1 = ((pb[0] + pb[4]) + (pb[1] + pb[5])) + ((pb[2] + pb[6]) + (pb[3] + pb[7]));
    float yv[8];
#pragma unroll
    for (int r = 0; r < 4; ++r) { auto sw = __builtin_amdgcn_permlane32_swap(__float_as_uint(a0[r]), __float_as_uint(a1[r]), false, false); yv[r] = __uint_as_float(sw[0]); yv[4 + r] = __uint_as_float(sw[1]); }
    const u32 gw[4] = {gbA[0], gbA[2], gbB[0], gbB[2]};
    u32x4 ov;
#pragma unroll
    for (int j = 0; j < 4; ++j) ov[j] = pk2(fmaf(bflo(gw[j]), yv[2 * j], bflo(x2r[j])), fmaf(bfhi(gw[j]), yv[2 * j + 1], bfhi(x2r[j])));
    *(u32x4*)(out + (size_t)(32 * (drt + dm) + l31) * 448 + dcol) = ov;
    if (b == 0 && tid == 0) *(GAS u32*)ws = tag;
  }
  STAMPL(26, "end");
}

extern "C" void kernel_launch(void* const* d_in, const int* in_sizes, int n_in, void* d_out, int out_size, void* d_ws, size_t ws_size, hipStream_t stream) {
  fused_block<<<256, 512, 0, stream>>>((const u16*)d_in[0], (const u16*)d_in[1], (const u16*)d_in[2], (const u16*)d_in[4], (const u16*)d_in[5],
                                       (const u16*)d_in[6], (const u16*)d_in[7], (const u16*)d_in[8], (u16*)d_out, (char*)d_ws);
  probe_spin<<<1, 256, 0, stream>>>((char*)d_ws);
}
```

```cpp
#define PROBE 26
#define PROBE_MUL 0
#define PROBE_MODE 0
#define PROBE_PRE 0
#include <hip/hip_runtime.h>
#include <stdint.h>

typedef unsigned short u16;
typedef unsigned u32;
typedef unsigned long long u64;
typedef __attribute__((ext_vector_type(2))) unsigned u32x2;
typedef __attribute__((ext_vector_type(4))) unsigned u32x4;
typedef __attribute__((ext_vector_type(2))) float f32x2;
typedef __attribute__((ext_vector_type(4))) float f32x4;
typedef __attribute__((ext_vector_type(16))) float f32x16;
typedef __attribute__((ext_vector_type(2))) __bf16 bf16x2;
typedef __attribute__((ext_vector_type(8))) __bf16 bf16x8;
typedef __attribute__((ext_vector_type(4))) short s16x4;
#define DEV __device__ __forceinline__
#define GAS __attribute__((address_space(1)))
#define LAS __attribute__((address_space(3)))
#define RLX_AGENT __ATOMIC_RELAXED, __HIP_MEMORY_SCOPE_AGENT

constexpr u32 OFF_MODG = 4096;
constexpr u32 OFF_X2G = 65536, OFF_UV = 13467648  , OFF_WQT = 524288, OFF_WOT = 1900544, OFF_WGU = 2359296, OFF_WDT = 5570560;
constexpr u32 OFF_KV = 7176192, OFF_OB = 11370496, OFF_X2 = 15302656, OFF_HB = 17137664, OFF_XH = 24477696;
constexpr u32 OFF_XF = 26312704, OFF_WQF = OFF_XF + 512 * 128, OFF_KVG = OFF_WQF + 1024 * 128, OFF_OF = OFF_KVG + 1024 * 128, OFF_HF = OFF_OF + 2048 * 128, OFF_PREPF = OFF_HF + 2048 * 128, WS_USED = OFF_PREPF + 256 * 128;
constexpr u32 OFF_STAMP = 29360128;
constexpr u32 L_STG = 90112  , L_Q = 131072, L_GAM = 139264, L_QN = 142848;
constexpr u32 L_PARK = 64512 + 1024  ;
constexpr u32 L_SP2 = 151552  , L_MODP = L_SP2, L_MX = L_SP2, L_LP = L_SP2, L_UV = 155648  , L_ST = 157696  , L_G2 = 159488  ;
constexpr u32 L_SYNC = 160000;
constexpr u32 LDS_BYTES = 163840;
constexpr int CODE_BYTES = 38000;
constexpr int SPIN_CAP = 1 << 15;
#define FIRST_POLL 280
#define KV_SLEEP 1
#define PREP_START 220
#define POLL_T 0
#define POLL_SLEEP 3

DEV float bflo(u32 w) { return __uint_as_float(w << 16); }
DEV float bfhi(u32 w) { return __uint_as_float(w & 0xffff0000u); }
DEV u32 pk2(float a, float b) { f32x2 v = {a, b}; return __builtin_bit_cast(u32, __builtin_convertvector(v, bf16x2)); }
DEV float rbf(float a) { return bflo(pk2(a, 0.f)); }
DEV float fma1(float a, float b, float c) { float r; asm("v_fma_f32 %0, %1, %2, %3" : "=v"(r) : "v"(a), "v"(b), "v"(c)); return r; }
DEV float dot2(u32 a, u32 b, float c) { return __builtin_amdgcn_fdot2_f32_bf16(__builtin_bit_cast(bf16x2, a), __builtin_bit_cast(bf16x2, b), c, false); }
DEV f32x16 mfma32(u32x4 a, u32x4 b, f32x16 c) { return __builtin_amdgcn_mfma_f32_32x32x16_bf16(__builtin_bit_cast(bf16x8, a), __builtin_bit_cast(bf16x8, b), c, 0, 0, 0); }
DEV f32x4 mfma16(u32x4 a, u32x4 b, f32x4 c) { return __builtin_amdgcn_mfma_f32_16x16x32_bf16(__builtin_bit_cast(bf16x8, a), __builtin_bit_cast(bf16x8, b), c, 0, 0, 0); }
DEV float xhalf_sum(float v) { auto r = __builtin_amdgcn_permlane32_swap(__float_as_uint(v), __float_as_uint(v), false, false); return __uint_as_float(r[0]) + __uint_as_float(r[1]); }
DEV float xhalf_max(float v) { auto r = __builtin_amdgcn_permlane32_swap(__float_as_uint(v), __float_as_uint(v), false, false); return fmaxf(__uint_as_float(r[0]), __uint_as_float(r[1])); }
#define DPPF(v, ctrl) __uint_as_float(__builtin_amdgcn_update_dpp(0u, __float_as_uint(v), ctrl, 0xf, 0xf, false))
DEV float wave_max(float v) {
  v = fmaxf(v, DPPF(v, 0x121)); v = fmaxf(v, DPPF(v, 0x122)); v = fmaxf(v, DPPF(v, 0x124)); v = fmaxf(v, DPPF(v, 0x128));
  auto r = __builtin_amdgcn_permlane16_swap(__float_as_uint(v), __float_as_uint(v), false, false);
  return xhalf_max(fmaxf(__uint_as_float(r[0]), __uint_as_float(r[1])));
}
#define WAIT_VM(n) asm volatile("s_waitcnt vmcnt(" #n ")" ::: "memory")
#define BAR() asm volatile("s_waitcnt lgkmcnt(0)\n\ts_barrier" ::: "memory")
#define SB() __builtin_amdgcn_sched_barrier(0)
#define CFENCE() asm volatile("" ::: "memory")
template <bool SC1> DEV void dma16(const void* sbase, u32 voff, u32 lds_dst) {
  u32 keep;
  if (SC1) asm volatile("s_mov_b32 %0, m0\n\ts_mov_b32 m0, %3\n\ts_nop 2\n\tglobal_load_lds_dwordx4 %1, %2 sc1\n\ts_mov_b32 m0, %0" : "=&s"(keep) : "v"(voff), "s"(sbase), "s"(lds_dst) : "memory");
  else asm volatile("s_mov_b32 %0, m0\n\ts_mov_b32 m0, %3\n\ts_nop 2\n\tglobal_load_lds_dwordx4 %1, %2\n\ts_mov_b32 m0, %0" : "=&s"(keep) : "v"(voff), "s"(sbase), "s"(lds_dst) : "memory");
}
DEV void dma16x4(const void* sbase, u32 voff, u32 lds_dst) {
  u32 keep;
  asm volatile("s_mov_b32 %0, m0\n\ts_mov_b32 m0, %3\n\ts_nop 2\n\tglobal_load_lds_dwordx4 %1, %2 sc1\n\tglobal_load_lds_dwordx4 %1, %2 offset:1024 sc1\n\t"
               "global_load_lds_dwordx4 %1, %2 offset:2048 sc1\n\tglobal_load_lds_dwordx4 %1, %2 offset:3072 sc1\n\ts_mov_b32 m0, %0" : "=&s"(keep) : "v"(voff), "s"(sbase), "s"(lds_dst) : "memory");
}
DEV void dma4_touch(const void* sbase, u32 voff, u32 lds_dst) {
  u32 keep;
  asm volatile("s_mov_b32 %0, m0\n\ts_mov_b32 m0, %3\n\ts_nop 2\n\tglobal_load_lds_dword %1, %2 sc1\n\ts_mov_b32 m0, %0" : "=&s"(keep) : "v"(voff), "s"(sbase), "s"(lds_dst) : "memory");
}
DEV u32x4 lds128(u32 a) { return *(const LAS u32x4*)a; }
DEV f32x4 lds128f(u32 a) { return *(const LAS f32x4*)a; }
DEV float lds32f(u32 a) { return *(const LAS float*)a; }
DEV u32x2 ldstr(u32 a) { return __builtin_bit_cast(u32x2, __builtin_amdgcn_ds_read_tr16_b64_v4i16((LAS s16x4*)a)); }
DEV u32x4 cat(u32x2 a, u32x2 b) { return (u32x4){a.x, a.y, b.x, b.y}; }
typedef __amdgpu_buffer_rsrc_t rsrc_t;
DEV u32x4 ld128(rsrc_t r, u32 voff, u32 soff) { return __builtin_amdgcn_raw_buffer_load_b128(r, voff, soff, 16); }
DEV u32 ld32(rsrc_t r, u32 voff, u32 soff) { return __builtin_amdgcn_raw_buffer_load_b32(r, voff, soff, 16); }
DEV void st128(rsrc_t r, u32 voff, u32 soff, u32x4 v) {
  __builtin_amdgcn_raw_buffer_store_b128(v, r, voff, soff, 16);
  asm volatile("s_nop 1" :: "v"(v) : "memory");
}
DEV void st32(rsrc_t r, u32 voff, u32 soff, u32 v) { __builtin_amdgcn_raw_buffer_store_b32(v, r, voff, soff, 16); }
DEV u32 poll32(const char* p) { return __hip_atomic_load((GAS u32*)p, RLX_AGENT); }
DEV u64 poll64(const char* p) { return __hip_atomic_load((GAS u64*)p, RLX_AGENT); }
DEV void flag32(char* p, u32 v) { __hip_atomic_store((GAS u32*)p, v, RLX_AGENT); }
DEV void flag64(char* p, u32 val, u32 tag) { __hip_atomic_store((GAS u64*)p, ((u64)tag << 32) | val, RLX_AGENT); }
DEV void wait_flags(const char* p, u32 tag) {
#pragma nounroll
  for (int i = 0; i < SPIN_CAP; ++i) { if (__all(poll32(p) == tag)) break; __builtin_amdgcn_s_sleep(POLL_SLEEP); }
  CFENCE();
}
DEV u32 wait_gran(const char* p, u32 tag) {
  u64 g = 0;
#pragma nounroll
  for (int i = 0; i < SPIN_CAP; ++i) { g = poll64(p); if (__all((u32)(g >> 32) == tag)) break; __builtin_amdgcn_s_sleep(POLL_SLEEP); }
  CFENCE();
  return (u32)g;
}
DEV u32 lds_ld(u32 a) { return __hip_atomic_load((LAS u32*)a, __ATOMIC_RELAXED, __HIP_MEMORY_SCOPE_WORKGROUP); }
DEV void lds_st(u32 a, u32 v) { __hip_atomic_store((LAS u32*)a, v, __ATOMIC_RELAXED, __HIP_MEMORY_SCOPE_WORKGROUP); }
DEV u32 lds_inc(u32 a) { return __builtin_amdgcn_readfirstlane(__hip_atomic_fetch_add((LAS u32*)a, 1u, __ATOMIC_RELAXED, __HIP_MEMORY_SCOPE_WORKGROUP)); }
DEV void lds_wait(u32 a, u32 want) {
#pragma nounroll
  for (int i = 0; i < (SPIN_CAP << 6); ++i) { if (__builtin_amdgcn_readfirstlane(lds_ld(a)) >= want) break; __builtin_amdgcn_s_sleep(1); }
  CFENCE();
}
#define WAIT_LGKM() asm volatile("s_waitcnt lgkmcnt(0)" ::: "memory")
#define STAMP(i) do {} while (0)
#undef STAMP
#define STAMP(i) do { if (((i) == 0 || (i) == PROBE) && wave == 0 && lane == 0) __hip_atomic_store((GAS u64*)(ws + OFF_STAMP) + b * 2 + ((i) != 0 || PROBE == 0) , (u64)__builtin_amdgcn_s_memrealtime(), RLX_AGENT); \
   if ((i) == 0 && PROBE == 0 && wave == 0 && lane == 0) __hip_atomic_store((GAS u64*)(ws + OFF_STAMP) + b * 2, (u64)__builtin_amdgcn_s_memrealtime(), RLX_AGENT); } while (0)
__global__ void probe_pre(char* ws) { if (threadIdx.x == 0) __hip_atomic_store((GAS u64*)(ws + OFF_STAMP + 8192), (u64)__builtin_amdgcn_s_memrealtime(), RLX_AGENT); }
__global__ void probe_spin(char* ws, int mode, int mul) {
  const u64 tin = __builtin_amdgcn_s_memrealtime();
  const u64* p = (const u64*)(ws + OFF_STAMP);
  u64 a = p[threadIdx.x * 2], c = p[threadIdx.x * 2 + 1];
  for (int i = 1; i < 4; ++i) { const u64 a2 = p[(threadIdx.x + 64 * i) * 2], c2 = p[(threadIdx.x + 64 * i) * 2 + 1]; a = a2 < a ? a2 : a; c = c2 > c ? c2 : c; }
  for (int o = 32; o; o >>= 1) { const u64 a2 = __shfl_xor(a, o), c2 = __shfl_xor(c, o); a = a2 < a ? a2 : a; c = c2 > c ? c2 : c; }
  const u64 pre = p[1024];
  u64 d = mode == 0 ? c - a : mode == 1 ? a - pre : tin - c;
  d *= mul; if (d > 20000) d = 20000;
  const u64 t0 = __builtin_amdgcn_s_memrealtime();
  while (__builtin_amdgcn_s_memrealtime() - t0 < d) __builtin_amdgcn_s_sleep(1);
}
#define STAMPL(i, s) STAMP(i)
#define GATE_MOD lds_wait(lb + L_SYNC + 36, 1)

__global__ __launch_bounds__(512, 2) void fused_block(const u16* __restrict__ x, const u16* __restrict__ wqkv, const u16* __restrict__ wo,
                                                      const u16* __restrict__ wg, const u16* __restrict__ wu, const u16* __restrict__ wd,
                                                      const u16* __restrict__ cvec, const u16* __restrict__ wmod, u16* __restrict__ out, char* ws) {
  __shared__ __attribute__((aligned(16))) char smem[LDS_BYTES];
  const int b = blockIdx.x, tid = threadIdx.x, lane = tid & 63;
  const int wave = __builtin_amdgcn_readfirstlane(tid >> 6);
  const int l31 = lane & 31, hi = lane >> 5;
  const int xg = b & 7, slot = b >> 3;
  const int ai = 4 * (xg >> 1) + (slot & 3), ah = 8 * (xg & 1) + (slot >> 2);
  const u32 lb = (u32)(uintptr_t)(LAS char*)smem;
  const rsrc_t wsr = __builtin_amdgcn_make_buffer_rsrc(ws, 0, WS_USED, 0x00020000);
  STAMP(0);
  {
    u64 pc; asm volatile("s_getpc_b64 %0" : "=s"(pc));
    if (wave == 0) {
      const u32 off = min((u32)(slot * 1280 + min(lane, 9) * 128), (u32)CODE_BYTES);
      u32 keep;
      asm volatile("s_mov_b32 %0, m0\n\ts_mov_b32 m0, %3\n\ts_nop 2\n\tglobal_load_lds_dword %1, %2\n\ts_mov_b32 m0, %0" : "=&s"(keep) : "v"(off), "s"(pc), "s"(lb + L_ST) : "memory");
    }
  }
  if (wave == 1) {
    const u32 off = min(lane, 12) << 21;
    u32 keep;
    asm volatile("s_mov_b32 %0, m0\n\ts_mov_b32 m0, %3\n\ts_nop 2\n\tglobal_load_lds_dword %1, %2\n\ts_mov_b32 m0, %0" : "=&s"(keep) : "v"(off), "s"(ws), "s"(lb + L_ST + 256) : "memory");
  }
  if (wave == 2 && lane < 16) lds_st(lb + L_SYNC + lane * 4, 0u);
  const u64 t_entry = __builtin_amdgcn_s_memrealtime();
  const u32 epoch_v = poll32(ws);
  u32 tag;

  f32x16 accQ, accK, accV;
  const int tq4 = (lane & 15) >> 2, tp4 = lane & 3, grp = lane >> 4;
  if (b < 42) {
    u32x4 wv[7];
    const u32 cv = cvec[56 * wave + min(lane, 55)];
#pragma unroll
    for (int n = 0; n < 7; ++n) wv[n] = *(const u32x4*)((const char*)wmod + (size_t)(56 * wave + 8 * n + (lane >> 3)) * 5376 + 128 * b + 16 * (lane & 7));
    BAR();
    const float cf = bflo(cv);
    const float sl = rbf(cf * __builtin_amdgcn_rcpf(1.f + __builtin_amdgcn_exp2f(-1.4426950408889634f * cf)));
    float acc[8] = {0.f, 0.f, 0.f, 0.f, 0.f, 0.f, 0.f, 0.f};
#pragma unroll
    for (int n = 0; n < 7; ++n) {
      const float sn = __uint_as_float(__builtin_amdgcn_ds_bpermute((8 * n + (lane >> 3)) * 4, __float_as_uint(sl)));
#pragma unroll
      for (int j = 0; j < 4; ++j) { acc[2 * j] = fmaf(sn, bflo(wv[n][j]), acc[2 * j]); acc[2 * j + 1] = fmaf(sn, bfhi(wv[n][j]), acc[2 * j + 1]); }
    }

#pragma unroll
    for (int j = 0; j < 8; ++j) acc[j] += DPPF(acc[j], 0x128);
#pragma unroll
    for (int j = 0; j < 4; ++j) { auto r = __builtin_amdgcn_permlane16_swap(__float_as_uint(acc[j]), __float_as_uint(acc[j + 4]), false, false); acc[j] = __uint_as_float(r[0]) + __uint_as_float(r[1]); }
#pragma unroll
    for (int j = 0; j < 2; ++j) { auto r = __builtin_amdgcn_permlane32_swap(__float_as_uint(acc[j]), __float_as_uint(acc[j + 2]), false, false); acc[j] = __uint_as_float(r[0]) + __uint_as_float(r[1]); }
    if (!(lane & 8)) *(LAS f32x2*)(lb + L_MODP + (wave * 64 + 8 * (lane & 7) + 2 * hi + 4 * (grp & 1)) * 4) = (f32x2){acc[0], acc[1]};
    WAIT_LGKM();
    u32 arrived = 0;
    if (lane == 0) arrived = __hip_atomic_fetch_add((LAS u32*)(lb + L_SYNC + 12), 1u, __ATOMIC_RELAXED, __HIP_MEMORY_SCOPE_WORKGROUP);
    if (__builtin_amdgcn_readfirstlane(arrived) == 7) {
      CFENCE();
      f32x2 t = {0.f, 0.f};
#pragma unroll
      for (int p = 0; p < 8; ++p) t += *(const LAS f32x2*)(lb + L_MODP + (p * 64 + 2 * l31) * 4);
      const u32 pv = pk2(t[0], t[1]), nv = __builtin_amdgcn_update_dpp(0u, pv, 0xB1, 0xf, 0xf, false);
      st128(wsr, (lane < 32 && !(lane & 1)) ? (u32)(32 * b + lane) * 8 : 0x80000000u, OFF_MODG, (u32x4){pv, epoch_v + 1u, nv, epoch_v + 1u});
      WAIT_VM(0);
      lds_st(lb + L_SYNC + 32, 1u);
    }
  } else if (wave < 3 && 3 * (b - 42) + wave < 512) {
    const int xgp = 3 * (b - 42) + wave, row = 4 * xgp + (lane & 3), c0i = lane >> 2;
    u32x4 xr[4];
#pragma unroll
    for (int n = 0; n < 4; ++n) xr[n] = *(const u32x4*)((const char*)x + (size_t)row * 896 + min(c0i + 16 * n, 55) * 16);
    BAR();
    if (c0i >= 8) xr[3] = (u32x4){0u, 0u, 0u, 0u};
    float s1[2] = {0.f, 0.f}, s2[2] = {0.f, 0.f};
#pragma unroll
    for (int n = 0; n < 4; ++n)
#pragma unroll
      for (int j = 0; j < 4; ++j) { s1[j & 1] = dot2(xr[n][j], 0x3F803F80u, s1[j & 1]); s2[j & 1] = dot2(xr[n][j], xr[n][j], s2[j & 1]); }
    float t1 = s1[0] + s1[1], t2 = s2[0] + s2[1];
    t1 += DPPF(t1, 0x124); t2 += DPPF(t2, 0x124); t1 += DPPF(t1, 0x128); t2 += DPPF(t2, 0x128);
    { auto r = __builtin_amdgcn_permlane16_swap(__float_as_uint(t1), __float_as_uint(t2), false, false); t1 = __uint_as_float(r[0]) + __uint_as_float(r[1]); }
    t1 = xhalf_sum(t1);
    { auto r = __builtin_amdgcn_permlane16_swap(__float_as_uint(t1), __float_as_uint(t1), false, false); t1 = __uint_as_float(r[0]); t2 = __uint_as_float(r[1]); }
    const float mean = t1 * (1.f / 448.f), ex2 = t2 * (1.f / 448.f);
    const float rstd = __builtin_amdgcn_rsqf(fmaxf(ex2 - mean * mean, 0.f) + 1e-5f), c0 = -mean * rstd;
    asm volatile("" :: "v"(c0));

#pragma unroll
    for (int n = 0; n < 4; ++n) {
      u32x4 o;
#pragma unroll
      for (int j = 0; j < 4; ++j) asm("v_cvt_pk_f16_f32 %0, %1, %2" : "=v"(o[j]) : "v"(fmaf(bflo(xr[n][j]), rstd, c0)), "v"(fmaf(bfhi(xr[n][j]), rstd, c0)));
      const u32 c = c0i + 16 * n;
      st128(wsr, c < 56 ? (c >> 1) * 1024 + ((row & 31) + 32 * (c & 1)) * 16 + (u32)(row >> 5) * 28672 : 0x80000000u, OFF_XH, o);
    }
    WAIT_VM(0);
    if (lane == 0) flag32(ws + OFF_XF + xgp * 128, epoch_v + 1u);
  } else if ((wave == 4 || wave == 5) && b < 210) {
    const int u = b - 42, part = u / 56, rem = u - part * 56, hp = rem / 7, kg = rem - hp * 7, s0 = 4 * kg + 2 * (wave - 4);
    const char* src = (const char*)wqkv + (size_t)(16 * s0) * 2688 + part * 896 + hp * 112;
    const u32 stg = lb + L_Q + (wave - 4) * 4096;
#pragma unroll
    for (int i = 0; i < 4; ++i) { const u32 c = lane & 7; dma16<false>(src + i * 8 * 2688, (lane >> 3) * 2688 + 16 * (c == 7 ? 6 : c), stg + i * 1024); }
    BAR();
    WAIT_VM(0);

#pragma unroll
    for (int e = 0; e < 4; ++e) {
      const u32 a = stg + (16 * (e >> 1) + 8 * (grp >> 1) + tq4) * 128 + (e & 1) * 56 + (grp & 1) * 32 + tp4 * 8;
      u32x4 f = cat(ldstr(a), ldstr(a + 512));
      if (l31 >= 28) f = (u32x4){0u, 0u, 0u, 0u};
      st128(wsr, lane * 16, OFF_WQT + (u32)((2 * hp + (e & 1)) * 84 + 3 * (s0 + (e >> 1)) + part) * 1024, f);
    }
    WAIT_VM(0);
    if (lane < 2) flag32(ws + OFF_WQF + ((2 * hp + lane) * 64 + 3 * (s0 >> 1) + part) * 128, epoch_v + 1u);
  } else {
    BAR();
  }
  tag = __builtin_amdgcn_readfirstlane(epoch_v) + 1u;
  STAMPL(7, "unit done");
  const int cg = b / 7, kg = b - cg * 7;
  if (wave >= 6) {
#pragma nounroll
    for (int i = 0; i < 4096 && __builtin_amdgcn_s_memrealtime() - t_entry < PREP_START; ++i) __builtin_amdgcn_s_sleep(1);
#pragma unroll
    for (int ph = 0; ph < 2; ++ph) {
      const int pw = wave & 1, tw = 2 * ph + pw, p = 4 * b + tw;
      const u32 stg = lb + L_STG + tw * 6144;
      if (b < 196) {
#pragma unroll
        for (int i = 0; i < 2; ++i) {
          const u32 vo = (u32)(64 * kg + 32 * ph + 16 * i + (lane >> 2)) * 3584 + (64 * cg + 32 * pw) * 2 + 16 * (lane & 3);
          dma16<false>(wg, vo, stg + (2 * i) * 1024); dma16<false>(wu, vo, stg + (2 * i + 1) * 1024);
        }
      }
      if (p < 784) {
        const int ds_ = p / 7, np = p - ds_ * 7;
#pragma unroll
        for (int i = 0; i < 2; ++i) dma16<false>(wd, (u32)(16 * ds_ + (lane >> 2)) * 896 + np * 128 + i * 64 + 16 * (lane & 3), stg + (4 + i) * 1024);
      } else if (p < 1008) {
        const int q = p - 784, h = q / 14, nq = q - h * 14;
#pragma unroll
        for (int i = 0; i < 2; ++i) dma16<false>(wo, (u32)min(28 * h + 16 * i + (lane >> 2), 447) * 896 + nq * 64 + 16 * (lane & 3), stg + (4 + i) * 1024);
      }
    }
    STAMPL(6, "prep issued");
    WAIT_VM(0);
    if (lane == 0) __hip_atomic_fetch_add((LAS u32*)(lb + L_SYNC + 52), 1u, __ATOMIC_RELAXED, __HIP_MEMORY_SCOPE_WORKGROUP);
    STAMPL(5, "prep landed");
  } else if (wave >= 3) {
#pragma nounroll
    for (int i = 0; i < 4096 && __builtin_amdgcn_s_memrealtime() - t_entry < FIRST_POLL; ++i) __builtin_amdgcn_s_sleep(1);
  }

  if (wave < 4) {
    if (wave == 3) { wait_flags(ws + OFF_XF + (32 * ai + l31) * 128, tag); GATE_MOD; lds_st(lb + L_SYNC, 1u); }
    else lds_wait(lb + L_SYNC, 1);

    STAMPL(3, "xhat go");
    u32x4 xf[28], pf[6]; u32 pfo[6], uvo[2]; float uvv[2];
    constexpr int XPRE = 12;
#define LDX(s) xf[s] = ld128(wsr, lane * 16 + ((s) & 3) * 1024, OFF_XH + (u32)((4 * ai + wave) * 28 + ((s) & ~3)) * 1024)
#pragma unroll
    for (int s = 0; s < XPRE; ++s) { LDX(s); SB(); }
    {
      const int pw = wave & 1, ph = (wave >> 1) & 1, p = 4 * b + wave;
      const u32 stg = lb + L_STG + wave * 6144;
      const bool isd = p < 784;
      const int ds_ = p / 7, np = p - ds_ * 7, q = p - 784, h = q / 14, nq = q - h * 14;
      lds_wait(lb + L_SYNC + 52, 2);
      const int pi = ((tp4 & 1) << 1) | (tp4 >> 1);
      float uu[2] = {0.f, 0.f}, vv[2] = {0.f, 0.f};
#pragma unroll
      for (int i = 0; i < 2; ++i) {
        const u32x4 be = lds128(lb + L_G2 + (2 * ph + i) * 32 + hi * 16), ga = lds128(lb + L_G2 + 128 + (2 * ph + i) * 32 + hi * 16);
#pragma unroll
        for (int e = 0; e < 2; ++e) {
          const u32 a = stg + (2 * i + (grp & 1)) * 1024 + (8 * (grp >> 1) + tq4) * 64 + e * 32 + pi * 8;
          u32x4 f = cat(ldstr(a), ldstr(a + 256));
#pragma unroll
          for (int j = 0; j < 4; ++j) {
            const float w0 = bflo(f[j]), w1 = bfhi(f[j]);
            vv[e] = fma1(w0, bflo(be[j]), vv[e]); vv[e] = fma1(w1, bfhi(be[j]), vv[e]);
            f[j] = pk2(w0 * bflo(ga[j]), w1 * bfhi(ga[j]));
            uu[e] += bflo(f[j]); uu[e] += bfhi(f[j]);
          }
          pf[2 * i + e] = f;
          pfo[2 * i + e] = b < 196 ? OFF_WGU + (u32)((4 * cg + 2 * pw + e) * 28 + 4 * kg + 2 * ph + i) * 1024 : 0x80000000u;
        }
      }
#pragma unroll
      for (int e = 0; e < 2; ++e) {
        const float us = xhalf_sum(uu[e]), vs = xhalf_sum(vv[e]);
        uvv[e] = hi ? vs : us; uvo[e] = b < 196 ? OFF_UV + (u32)((2 * kg + ph) * 112 + 4 * cg + 2 * pw + e) * 256 : 0x80000000u;
      }
#pragma unroll
      for (int i = 0; i < 2; ++i) {
        const u32 a = isd ? stg + (4 + i) * 1024 + (8 * (grp >> 1) + tq4) * 64 + (grp & 1) * 32 + tp4 * 8
                          : stg + 4096 + (16 * (grp >> 1) + 4 * (grp & 1) + tq4) * 64 + i * 32 + tp4 * 8;
        u32x4 f = cat(ldstr(a), ldstr(a + (isd ? 256 : 512)));
        if (!isd && grp == 3) { f[2] = 0u; f[3] = 0u; }
        pf[4 + i] = f;
        pfo[4 + i] = isd ? OFF_WDT + (u32)((2 * np + i) * 112 + ds_) * 1024 : p < 1008 ? OFF_WOT + (u32)((2 * nq + i) * 16 + h) * 1024 : 0x80000000u;
      }
      asm volatile("" : "+v"(pf[0]), "+v"(pf[1]), "+v"(pf[2]), "+v"(pf[3]), "+v"(pf[4]), "+v"(pf[5]), "+v"(uvv[0]), "+v"(uvv[1]));
      STAMPL(10, "prep tiled");
    }
    lds_wait(lb + L_SYNC + 4, 4);

    STAMPL(8, "W1 ready");
    accQ = (f32x16){}; accK = (f32x16){}; accV = (f32x16){};
    {
      u32 gb = lb + L_GAM + hi * 16; asm volatile("" : "+v"(gb));
      u32 wb = lb + lane * 16, wb2 = wb + 43008; asm volatile("" : "+v"(wb), "+v"(wb2));
      u32x4 wf[3][3], hf[2], gs[3], be[3]; float ht[8];
#define RDG(s) gs[(s) % 3] = lds128(gb + (s) * 32)
#define RDB(s) be[(s) % 3] = lds128(gb + 896 + (s) * 32)
#define RDW1(s, p) wf[(s) % 3][p] = lds128(((s) < 14 ? wb : wb2) + (3 * ((s) % 14) + (p)) * 1024)
#define RDW(s) do { RDW1(s, 0); RDW1(s, 1); RDW1(s, 2); } while (0)
#define DOTS(s, h) do { _Pragma("unroll") for (int j = 2 * (h); j < 2 * (h) + 2; ++j) { \
        asm volatile("v_fma_mix_f32 %0, %1, %2, %3 op_sel_hi:[1,1,1]" : "=v"(ht[2 * j]) : "v"(xf[s][j]), "v"(gs[(s) % 3][j]), "v"(be[(s) % 3][j])); \
        asm volatile("v_fma_mix_f32 %0, %1, %2, %3 op_sel:[1,1,1] op_sel_hi:[1,1,1]" : "=v"(ht[2 * j + 1]) : "v"(xf[s][j]), "v"(gs[(s) % 3][j]), "v"(be[(s) % 3][j])); } } while (0)
#define CVTS(s) do { hf[(s) & 1] = (u32x4){pk2(ht[0], ht[1]), pk2(ht[2], ht[3]), pk2(ht[4], ht[5]), pk2(ht[6], ht[7])}; asm volatile("" : "+v"(hf[(s) & 1])); } while (0)
      RDG(0); RDB(0); RDW(0); RDG(1); RDB(1); RDW(1); RDG(2); RDB(2); RDW(2); DOTS(0, 0); DOTS(0, 1); CVTS(0); SB();
#pragma unroll
      for (int s = 0; s < 28; ++s) {
        if (s == 1) lds_wait(lb + L_SYNC + 8, 3);
        if (s == 13) lds_wait(lb + L_SYNC + 24, 3);
        SB(); accQ = mfma32(wf[s % 3][0], hf[s & 1], accQ); SB();
        if (s < 25) RDG(s + 3);
        if (s < 25) RDW1(s + 3, 0);
        if (s < 27) DOTS(s + 1, 0);
        SB(); accK = mfma32(wf[s % 3][1], hf[s & 1], accK); SB();
        if (s < 25) RDB(s + 3);
        if (s < 25) RDW1(s + 3, 1);
        if (s < 27) DOTS(s + 1, 1);
        SB(); accV = mfma32(hf[s & 1], wf[s % 3][2], accV); SB();
        if (s < 25) RDW1(s + 3, 2);
        if (s + XPRE < 28) LDX(s + XPRE);
        else if (s < 22) st128(wsr, lane * 16 + pfo[s - 16], 0, pf[s - 16]);
        else if (s < 24) st32(wsr, lane * 4 + uvo[s - 22], 0, __float_as_uint(uvv[s - 22]));
        if (s < 27) CVTS(s + 1);
        SB();
      }
    }
    STAMPL(5, "qkv mfma done");
    const float unscale = __uint_as_float(__builtin_amdgcn_readfirstlane(lds_ld(lb + L_SYNC + 28)));
    if (unscale != 1.f) { accQ *= unscale; accK *= unscale; accV *= unscale; }
    u32x4 k0, k1, v0, v1, q0, q1;
#pragma unroll
    for (int j = 0; j < 4; ++j) {
      k0[j] = pk2(accK[2 * j], accK[2 * j + 1]); k1[j] = pk2(accK[8 + 2 * j], accK[9 + 2 * j]);
      v0[j] = pk2(accV[2 * j], accV[2 * j + 1]); v1[j] = pk2(accV[8 + 2 * j], accV[9 + 2 * j]);
    }
    if (hi) { k1[2] = 0x3F803F80u; k1[3] = 0x00003F80u; }
    if (l31 == 28) { v0 = (u32x4){0x3F803F80u, 0x3F803F80u, 0x3F803F80u, 0x3F803F80u}; v1 = v0; }
    const u32 kvo = OFF_KV + (u32)(ah * 64 + ai * 4 + wave) * 4096;
    st128(wsr, lane * 16, kvo, k0); st128(wsr, lane * 16 + 1024, kvo, k1); st128(wsr, lane * 16 + 2048, kvo, v0); st128(wsr, lane * 16 + 3072, kvo, v1);
    SB();
    float kn = 0.f, qn = 0.f;
    constexpr float QS = 0.18898223650461362f * 1.4426950408889634f;
#pragma unroll
    for (int r = 0; r < 16; ++r) { kn = fmaf(accK[r], accK[r], kn); accQ[r] *= QS; qn = fmaf(accQ[r], accQ[r], qn); }
#pragma unroll
    for (int j = 0; j < 4; ++j) { q0[j] = pk2(accQ[2 * j], accQ[2 * j + 1]); q1[j] = pk2(accQ[8 + 2 * j], accQ[9 + 2 * j]); }
    *(LAS u32x4*)(lb + L_Q + wave * 2048 + lane * 16) = q0; *(LAS u32x4*)(lb + L_Q + wave * 2048 + 1024 + lane * 16) = q1;
    qn = xhalf_sum(qn); kn = wave_max(xhalf_sum(kn));
    if (!hi) *(LAS float*)(lb + L_QN + (wave * 32 + l31) * 4) = qn;
    WAIT_VM(0);
    if (lane == 0) flag64(ws + OFF_KVG + (ah * 64 + ai * 4 + wave) * 128, __float_as_uint(kn), tag);
    if (b < 42) lds_wait(lb + L_SYNC + 32, 1);
    if (lane == 0 && __hip_atomic_fetch_add((LAS u32*)(lb + L_SYNC + 44), 1u, __ATOMIC_RELAXED, __HIP_MEMORY_SCOPE_WORKGROUP) == 3u) flag32(ws + OFF_PREPF + b * 128, tag);
    STAMPL(6, "kv flagged");
  } else {
    if (wave == 4) {
      u64 gv[8];
      __builtin_amdgcn_s_setprio(3);
#pragma nounroll
      for (int i = 0; i < SPIN_CAP; ++i) {
        bool ok = true;
#pragma unroll
        for (int n = 0; n < 7; ++n) gv[n] = poll64(ws + OFF_MODG + (64 * n + lane) * 8);
        gv[7] = poll64(ws + OFF_MODG + (672 + 224 * hi + 32 * kg + l31) * 8);
#pragma unroll
        for (int n = 0; n < 8; ++n) ok = ok && (u32)(gv[n] >> 32) == tag;
        if (__all(ok)) break;
        __builtin_amdgcn_s_sleep(POLL_SLEEP);
      }
      CFENCE();
      __builtin_amdgcn_s_setprio(0);
      *(LAS u32*)(lb + L_G2 + lane * 4) = hi ? pk2(1.f + bflo((u32)gv[7]), 1.f + bfhi((u32)gv[7])) : (u32)gv[7];
      WAIT_LGKM();
      lds_st(lb + L_SYNC + 36, 1u);

      float lo[7], hi2[7], mxv = 0.f;
#pragma unroll
      for (int n = 0; n < 7; ++n) {
        u32 v = (u32)gv[n];
        if (64 * n + lane >= 224) v = pk2(1.f + bflo(v), 1.f + bfhi(v));
        lo[n] = bflo(v); hi2[n] = bfhi(v); mxv = fmaxf(mxv, fmaxf(fabsf(lo[n]), fabsf(hi2[n])));
      }
      const u32 ex = max((int)(__float_as_uint(wave_max(mxv)) >> 23) - (127 + 14), 0);
      const float scl = __uint_as_float((127u - min(ex, 114u)) << 23);
#pragma unroll
      for (int n = 0; n < 7; ++n) {
        u32 o; asm("v_cvt_pk_f16_f32 %0, %1, %2" : "=v"(o) : "v"(lo[n] * scl), "v"(hi2[n] * scl));
        const int g = 64 * n + lane;
        *(LAS u32*)(lb + L_GAM + (g >= 224 ? g - 224 : g + 224) * 4) = o;
      }
      lds_st(lb + L_SYNC + 28, (127u + min(ex, 114u)) << 23);
      WAIT_LGKM();
      if (lane == 0) __hip_atomic_fetch_add((LAS u32*)(lb + L_SYNC + 4), 1u, __ATOMIC_RELAXED, __HIP_MEMORY_SCOPE_WORKGROUP);
    } else {
      if (wave == 5) { wait_flags(ws + OFF_WQF + (ah * 64 + min(lane, 41)) * 128, tag); GATE_MOD; lds_st(lb + L_SYNC + 48, 1u); }
      else lds_wait(lb + L_SYNC + 48, 1);

      const char* wsrc = ws + OFF_WQT + (size_t)ah * 84 * 1024 + (wave - 5) * 4096;
#pragma unroll
      for (int n = 0; n < 7; ++n) dma16x4(wsrc + n * 12288, lane * 16, lb + (wave - 5) * 4096 + n * 12288);
    }
    if (wave >= 5) {
      WAIT_VM(24);
      if (lane == 0) __hip_atomic_fetch_add((LAS u32*)(lb + L_SYNC + 4), 1u, __ATOMIC_RELAXED, __HIP_MEMORY_SCOPE_WORKGROUP);
      WAIT_VM(12);
      if (lane == 0) __hip_atomic_fetch_add((LAS u32*)(lb + L_SYNC + 8), 1u, __ATOMIC_RELAXED, __HIP_MEMORY_SCOPE_WORKGROUP);
      WAIT_VM(0);
      if (lane == 0) __hip_atomic_fetch_add((LAS u32*)(lb + L_SYNC + 24), 1u, __ATOMIC_RELAXED, __HIP_MEMORY_SCOPE_WORKGROUP);
      STAMPL(9, "W landed");
    }
  }
  BAR();
  STAMPL(11, "B3");

  const int ort = 8 * xg + (slot >> 2), ocq = slot & 3;
  const int gi = ai, gfj = ah;
  u32x4 wo_f[16], wgu[28], xres, gaA, gaB;
  {
    u32x4 qf[4][2];
#pragma unroll
    for (int t = 0; t < 4; ++t) { qf[t][0] = lds128(lb + L_Q + t * 2048 + lane * 16); qf[t][1] = lds128(lb + L_Q + t * 2048 + 1024 + lane * 16); }
    float qn2[4];
#pragma unroll
    for (int t = 0; t < 4; ++t) qn2[t] = lds32f(lb + L_QN + (t * 32 + l31) * 4);
    WAIT_LGKM();
    if (lane == 0) __hip_atomic_fetch_add((LAS u32*)(lb + L_SYNC + 40), 1u, __ATOMIC_RELAXED, __HIP_MEMORY_SCOPE_WORKGROUP);
    if (wave == 3) {
      __builtin_amdgcn_s_sleep(KV_SLEEP);
      const float km = wave_max(__uint_as_float(wait_gran(ws + OFF_KVG + (ah * 64 + lane) * 128, tag)));
      lds_st(lb + L_SYNC + 60, __float_as_uint(km));
      WAIT_LGKM();
      lds_st(lb + L_SYNC + 56, 1u);
    } else lds_wait(lb + L_SYNC + 56, 1);
    const float kmax2 = __uint_as_float(lds_ld(lb + L_SYNC + 60));
    STAMPL(12, "kv flags seen");
    const u32 kvb = OFF_KV + (u32)(ah * 64 + 8 * wave) * 4096;
    u32x4 kf[2][2], vf[2][2];
    const u32 kvl = lane * 16;
#define LDKV(D, kt, off) asm volatile("buffer_load_dwordx4 %0, %2, %3, %4 offen offset:" #off " sc1\n\tbuffer_load_dwordx4 %1, %2, %3, %4 offen offset:" #off "+1024 sc1" \
      : "=&v"(D[(kt) & 1][0]), "=&v"(D[(kt) & 1][1]) : "v"(kvl), "s"(wsr), "s"(kvb + (kt) * 4096) : "memory")
#define WAITN(n) asm volatile("s_waitcnt vmcnt(%0)" :: "i"(n) : "memory")
#define PINKV(D, kt) asm volatile("" : "+v"(D[(kt) & 1][0]), "+v"(D[(kt) & 1][1]))
#define WAITKV(D, kt, n) do { WAITN(n); SB(); PINKV(D, kt); } while (0)
#define WAITKV2(D, kt, n0, n1) do { if (wave < 4) WAITN(n0); else WAITN(n1); SB(); PINKV(D, kt); } while (0)
    asm volatile("s_nop 4");
    LDKV(kf, 0, 0); LDKV(vf, 0, 2048); LDKV(kf, 1, 0);
    float sh[4]; bool safe = true;
#pragma unroll
    for (int t = 0; t < 4; ++t) { sh[t] = 1.02f * __builtin_amdgcn_sqrtf(qn2[t] * kmax2); safe = safe && (sh[t] <= 48.f); }
    if (!__all(safe)) {
      BAR();
      float mx[4] = {-INFINITY, -INFINITY, -INFINITY, -INFINITY};
#pragma unroll 1
      for (int kt = 0; kt < 8; ++kt) {
        const u32x4 ka = ld128(wsr, lane * 16, kvb + kt * 4096), kb = ld128(wsr, lane * 16 + 1024, kvb + kt * 4096);
#pragma unroll
        for (int t = 0; t < 4; ++t) {
          f32x16 s = mfma32(ka, qf[t][0], (f32x16){}); s = mfma32(kb, qf[t][1], s);
#pragma unroll
          for (int r = 0; r < 16; ++r) mx[t] = fmaxf(mx[t], s[r]);
        }
      }
#pragma unroll
      for (int t = 0; t < 4; ++t) { const float m = xhalf_max(mx[t]); if (!hi) *(LAS float*)(lb + L_MX + (wave * 128 + t * 32 + l31) * 4) = m; }
      BAR();
#pragma unroll
      for (int t = 0; t < 4; ++t) {
        float m = -INFINITY;
#pragma unroll
        for (int w = 0; w < 8; ++w) m = fmaxf(m, lds32f(lb + L_MX + (w * 128 + t * 32 + l31) * 4));
        sh[t] = m;
      }
    }
    if (hi) {
#pragma unroll
      for (int t = 0; t < 4; ++t) {
        const float a = rbf(-sh[t]), r1 = -sh[t] - a, c = rbf(r1);
        qf[t][1][2] = pk2(a, c); qf[t][1][3] = pk2(r1 - c, 0.f);
      }
    }
    f32x16 o[4] = {(f32x16){}, (f32x16){}, (f32x16){}, (f32x16){}};
    WAITKV(kf, 0, 4);
    f32x16 sc = mfma32(kf[0][0], qf[0][0], (f32x16){}); sc = mfma32(kf[0][1], qf[0][1], sc);
    u32x4 p0, p1;
#define EXP2(v) __builtin_amdgcn_exp2f(v)
    float ec, ed;
#define GRP(r, PREV, MF, CUR) do { float ea = EXP2(sc[r]), eb = EXP2(sc[(r) + 1]); asm volatile("" : "+v"(ea), "+v"(eb)); PREV; SB(); MF; SB(); \
      ec = EXP2(sc[(r) + 2]); ed = EXP2(sc[(r) + 3]); asm volatile("" : "+v"(ec), "+v"(ed)); { u32 pa = pk2(ea, eb); asm volatile("" : "+v"(pa)); CUR = pa; } SB(); } while (0)
#define PKP(D) do { u32 pb = pk2(ec, ed); asm volatile("" : "+v"(pb)); D = pb; } while (0)
    u32 pwb = lb + wave * 8192 + lane * 16, lwb = lb + L_LP + wave * 512 + l31 * 4; asm volatile("" : "+v"(pwb), "+v"(lwb));
#define WRP(t) do { _Pragma("unroll") for (int g = 0; g < 2; ++g) *(LAS u32x4*)(pwb + ((t) * 2 + g) * 1024) = (u32x4){pk2(o[t][8 * g], o[t][8 * g + 1]), pk2(o[t][8 * g + 2], o[t][8 * g + 3]), pk2(o[t][8 * g + 4], o[t][8 * g + 5]), pk2(o[t][8 * g + 6], o[t][8 * g + 7])}; \
      if (hi) *(LAS float*)(lwb + (t) * 128) = o[t][12]; } while (0)
    const int w6 = min(wave, 6);
    const u32 wo_base = OFF_WOT + (u32)(7 * ocq + w6) * 16384, wg_base = OFF_WGU + (u32)(7 * gfj + w6) * 28672;
    auto nslot = [](int kt, int t, bool) constexpr { const int sl = 4 * (kt - 2) + t;
      return (t == 0 && kt < 7 ? 2 : 0) + (t == 2 && kt < 6 ? 2 : 0) + (kt >= 2 ? (sl == 0) + (sl < 16) + (sl >= 6 && sl < 18) : 0); };
    auto nafter = [&](int k0, int t0, int k1, int t1, bool young) constexpr { int n = -2; for (int i = 4 * k0 + t0; i <= 4 * k1 + t1; ++i) n += nslot(i >> 2, i & 3, young); return n; };
#pragma unroll
    for (int kt = 0; kt < 8; ++kt) {
      if (kt == 0) __builtin_amdgcn_s_setprio(3);
      if (kt == 2) __builtin_amdgcn_s_setprio(2);
      if (kt == 4) __builtin_amdgcn_s_setprio(1);
      if (kt == 6 && wave < 4) __builtin_amdgcn_s_setprio(0);
      if (kt == 7) __builtin_amdgcn_s_setprio(0);
#pragma unroll
      for (int t = 0; t < 4; ++t) {
        f32x16 sn;
        const bool more = t < 3 || kt < 7, first = t == 0 && kt == 0;
        const int kn = t < 3 ? (kt & 1) : ((kt + 1) & 1), tn = (t + 1) & 3;
        if (t == 3 && kt < 7) {
          if (kt == 0) WAITKV(kf, 1, nafter(0, 0, 0, 2, false) + 2);
          else if (kt == 1) WAITKV(kf, 2, nafter(0, 2, 1, 2, false));
          else WAITKV(kf, kt + 1, nafter(kt - 1, 2, kt, 2, false));
        }
        GRP(0, if (!first) PKP(p1[3]), if (more) sn = mfma32(kf[kn][0], qf[tn][0], (f32x16){}), p0[0]);
        GRP(4, PKP(p0[1]), if (more) sn = mfma32(kf[kn][1], qf[tn][1], sn), p0[2]);
        GRP(8, PKP(p0[3]), if (t > 0) o[t - 1] = mfma32(vf[kt & 1][1], p1, o[t - 1]); else if (kt > 0) o[3] = mfma32(vf[(kt + 1) & 1][1], p1, o[3]), p1[0]);
        if (t == 0 && kt < 7) LDKV(vf, kt + 1, 2048);
        if (t == 2 && kt < 6) LDKV(kf, kt + 2, 0);
        if (kt == 1 && t == 0 && wave == 3) {
#pragma nounroll
          for (int i = 0; i < SPIN_CAP; ++i) {
            const u32 a = poll32(ws + OFF_PREPF + lane * 128), c = poll32(ws + OFF_PREPF + (64 + lane) * 128), d = poll32(ws + OFF_PREPF + (128 + lane) * 128), e = poll32(ws + OFF_PREPF + (192 + lane) * 128);
            if (__all(a == tag && c == tag && d == tag && e == tag)) break;
            __builtin_amdgcn_s_sleep(8);
          }
          CFENCE();
          if (lane == 0) __hip_atomic_fetch_add((LAS u32*)(lb + L_SYNC + 40), 1u, __ATOMIC_RELAXED, __HIP_MEMORY_SCOPE_WORKGROUP);
        }
        if (t == 0) {
          if (kt == 2) STAMPL(1, "kt2");
          if (kt == 4) STAMPL(2, "kt4");
          if (kt == 6) STAMPL(4, "kt6");
        }
        if (kt >= 2) {
          const int sl = 4 * (kt - 2) + t;
          if (sl == 0) {
            lds_wait(lb + L_SYNC + 40, 9);
            dma4_touch(ws + wg_base + 12288, ((slot & 3) * 32 + l31) * 128 + hi * 64, lb + L_ST + w6 * 256);
          }
          if (sl < 10) wo_f[sl] = ld128(wsr, lane * 16, wo_base + sl * 1024);
          else if (sl < 16) wgu[sl - 10] = ld128(wsr, lane * 16, wg_base + (sl - 10) * 1024);
          if (sl >= 6 && sl < 18) dma16<true>(ws + (sl < 12 ? wo_base + (4 + sl) * 1024 : wg_base + (sl - 6) * 1024), lane * 16, lb + L_PARK + w6 * 12288 + (sl - 6) * 1024);
        }
        if (t == 0) {
          if (kt == 0) WAITKV(vf, 0, nafter(0, 0, 0, 0, false) + 4);
          else if (kt == 1) WAITKV(vf, 1, nafter(0, 0, 1, 0, false));
          else WAITKV(vf, kt, nafter(kt - 1, 0, kt, 0, false));
        }
        SB();
        GRP(12, PKP(p1[1]), o[t] = mfma32(vf[kt & 1][0], p0, o[t]), p1[2]);
        if (kt == 7 && t > 0) { WRP(t - 1); SB(); }
        if (more) sc = sn;
      }
    }
    PKP(p1[3]);
    o[3] = mfma32(vf[1][1], p1, o[3]);
    WRP(3);
    STAMPL(13, "attn done");
    BAR();
    const int qt = wave >> 1, sf = wave & 1;
    u32x4 pa[8]; float pl[8];
    {
      u32 base = lb + qt * 2048 + sf * 1024 + lane * 16, lbase = lb + L_LP + qt * 128 + l31 * 4;
      asm volatile("" : "+v"(base), "+v"(lbase));
#pragma unroll
      for (int w = 0; w < 8; ++w) { pa[w] = lds128(base + w * 8192); pl[w] = lds32f(lbase + w * 512); }
      SB();
    }
    float a8[8]; u32 sel0 = 0x00003F80u, sel1 = 0x3F800000u; asm volatile("" : "+v"(sel0), "+v"(sel1));
#pragma unroll
    for (int j = 0; j < 4; ++j) {
      a8[2 * j] = bflo(pa[0][j]); a8[2 * j + 1] = bfhi(pa[0][j]);
#pragma unroll
      for (int w = 1; w < 8; ++w) { a8[2 * j] = dot2(pa[w][j], sel0, a8[2 * j]); a8[2 * j + 1] = dot2(pa[w][j], sel1, a8[2 * j + 1]); }
    }
    const float l = ((pl[0] + pl[4]) + (pl[1] + pl[5])) + ((pl[2] + pl[6]) + (pl[3] + pl[7]));
    const float rl = __builtin_amdgcn_rcpf(l);
    const u32x4 of = {pk2(a8[0] * rl, a8[1] * rl), pk2(a8[2] * rl, a8[3] * rl), pk2(a8[4] * rl, a8[5] * rl), pk2(a8[6] * rl, a8[7] * rl)};
    st128(wsr, (l31 >> 4) * 1024 + ((l31 & 15) + 16 * (2 * sf + hi)) * 16, OFF_OB + (u32)((4 * ai + qt) * 16 + ah) * 2048, of);
    WAIT_VM(0);
    if (lane == 0) flag32(ws + OFF_OF + (((4 * ai + qt) * 16 + ah) * 2 + sf) * 128, tag);
    BAR();
    STAMPL(14, "O flagged");
  }

  u32 uvp[14];
  const int otok = 16 * ((lane >> 4) & 1) + (lane & 15), ofe = 112 * ocq + 16 * min(wave, 6) + 8 * hi;
  if (wave < 7) {
    xres = *(const u32x4*)(x + (size_t)(32 * ort + otok) * 448 + ofe);
    gaA = ld128(wsr, ofe * 4, OFF_MODG + 448 * 8); gaB = ld128(wsr, ofe * 4 + 16, OFF_MODG + 448 * 8);
#pragma unroll
    for (int k = 0; k < 14; ++k) uvp[k] = ld32(wsr, lane * 4, OFF_UV + (u32)(k * 112 + 7 * gfj + wave) * 256);
#pragma unroll
    for (int s = 12; s < 28; ++s) wgu[s] = ld128(wsr, lane * 16 + (s & 3) * 1024, OFF_WGU + (u32)((7 * gfj + wave) * 28 + (s & ~3)) * 1024);
#pragma unroll
    for (int j = 0; j < 6; ++j) wo_f[10 + j] = lds128(lb + L_PARK + wave * 12288 + j * 1024 + lane * 16);
#pragma unroll
    for (int j = 6; j < 12; ++j) wgu[j] = lds128(lb + L_PARK + wave * 12288 + j * 1024 + lane * 16);
    lds_wait(lb + L_SYNC + 16, 1);
  } else {
    wait_flags(ws + OFF_OF + (ort * 32 + l31) * 128, tag);
    lds_st(lb + L_SYNC + 16, 1u);
  }
  STAMPL(15, "O flags seen");
  {
    const char* osrc = ws + OFF_OB + (size_t)(ort * 16 + 2 * wave) * 2048;
    dma16x4(osrc, lane * 16, lb + 4 * wave * 1024);
  }
  __builtin_amdgcn_s_waitcnt(0x0F70);
  CFENCE();
  BAR();
  STAMPL(16, "O landed");
  if (wave < 7) {
    f32x4 y0 = {0.f, 0.f, 0.f, 0.f}, y1 = y0;
    u32x4 oa[8];
#pragma unroll
    for (int i = 0; i < 6; ++i) oa[i] = lds128(lb + i * 1024 + lane * 16);
#pragma unroll
    for (int h = 0; h < 16; ++h) {
      if (h < 13) { oa[(2 * h + 6) & 7] = lds128(lb + (2 * h + 6) * 1024 + lane * 16); oa[(2 * h + 7) & 7] = lds128(lb + (2 * h + 7) * 1024 + lane * 16); }
      SB(); y0 = mfma16(wo_f[h], oa[(2 * h) & 7], y0); y1 = mfma16(wo_f[h], oa[(2 * h + 1) & 7], y1); SB();
    }
    float yv[8];
#pragma unroll
    for (int r = 0; r < 4; ++r) { auto sw = __builtin_amdgcn_permlane16_swap(__float_as_uint(y0[r]), __float_as_uint(y1[r]), false, false); yv[r] = __uint_as_float(sw[0]); yv[4 + r] = __uint_as_float(sw[1]); }
    const u32 gw[4] = {gaA[0], gaA[2], gaB[0], gaB[2]};
    u32x4 x2p; float s1 = 0.f, s2 = 0.f;
#pragma unroll
    for (int j = 0; j < 4; ++j) {
      x2p[j] = pk2(fmaf(bflo(gw[j]), yv[2 * j], bflo(xres[j])), fmaf(bfhi(gw[j]), yv[2 * j + 1], bfhi(xres[j])));
      s1 = dot2(x2p[j], 0x3F803F80u, s1); s2 = dot2(x2p[j], x2p[j], s2);
    }
    const u32 fo = (u32)(ort * 28 + 7 * ocq + wave) * 1024;
    st128(wsr, (otok + 32 * hi) * 16, OFF_X2 + fo, x2p);
    s1 = xhalf_sum(s1); s2 = xhalf_sum(s2);
    *(LAS float*)(lb + L_ST + wave * 256 + (hi * 32 + otok) * 4) = hi ? s2 : s1;
    WAIT_VM(0);
  }
  BAR();
  if (wave == 7) {
    float t = 0.f;
#pragma unroll
    for (int w = 0; w < 7; ++w) t += lds32f(lb + L_ST + w * 256 + lane * 4);
    const u32 nv = __builtin_amdgcn_update_dpp(0u, __float_as_uint(t), 0xB1, 0xf, 0xf, false);
    st128(wsr, !(lane & 1) ? (u32)lane * 8 : 0x80000000u, OFF_X2G + (u32)(ort * 4 + ocq) * 512, (u32x4){__float_as_uint(t), tag, nv, tag});
  } else {
    float uvs = 0.f;
#pragma unroll
    for (int k = 0; k < 14; ++k) uvs += __uint_as_float(uvp[k]);
    *(LAS float*)(lb + L_UV + wave * 256 + lane * 4) = uvs;
  }
  STAMPL(17, "x2 flagged");

  const int dsl = slot & 15, dq = 2 * (slot >> 4) + dsl / 7, dc = dsl % 7;
  const bool dact = dsl < 14;
  u32x4 wdf[28], x2r, gbA, gbB;
  if (wave == 7) {
    u64 g[16]; const char* gp = ws + OFF_X2G + (16 * gi * 64 + lane) * 8;
#pragma nounroll
    for (int i = 0; i < SPIN_CAP; ++i) {
      bool ok = true;
#pragma unroll
      for (int p = 0; p < 16; ++p) g[p] = poll64(gp + p * 512);
#pragma unroll
      for (int p = 0; p < 16; ++p) ok = ok && (u32)(g[p] >> 32) == tag;
      if (__all(ok)) break;
      __builtin_amdgcn_s_sleep(1);
    }
    CFENCE();
#pragma unroll
    for (int p = 0; p < 16; ++p) *(LAS u32*)(lb + L_SP2 + p * 256 + lane * 4) = (u32)g[p];
  }
  BAR();
  STAMPL(18, "x2 flags seen");
  {
    const char* asrc = ws + OFF_X2 + (size_t)(4 * gi) * 28 * 1024;
#pragma unroll
    for (int n = 0; n < 4; ++n) { const int g = min(wave + 8 * n, 27); dma16x4(asrc + g * 4096, lane * 16, lb + g * 4096); }
  }
  WAIT_VM(12);
  BAR();
  STAMPL(19, "A0 landed");
  const int dm = wave >> 2, dn = (wave >> 1) & 1, dgp = wave & 1;
  const u32 wdo = OFF_WDT + (u32)(2 * dc * 112 + 14 * wave) * 1024;
  if (wave < 7) {
    u32 sb = lb + L_SP2 + l31 * 4, ub = lb + L_UV + wave * 256 + hi * 16; asm volatile("" : "+v"(sb), "+v"(ub));
    u32 ab = lb + lane * 16, ab2 = ab + 57344; asm volatile("" : "+v"(ab), "+v"(ab2));
    f32x16 acc[2]; u32x4 af[4]; f32x4 uv[4]; float st[8], rs = 0.f, nm = 0.f, hv[8];
#define EP_RD(T) do { _Pragma("unroll") for (int c = 0; c < 4; ++c) { st[c] = lds32f(sb + ((T) * 4 + c) * 256); st[4 + c] = lds32f(sb + ((T) * 4 + c) * 256 + 128); } } while (0)
#define EP_UV(g) do { uv[0] = lds128f(ub + 32 * (g)); uv[1] = lds128f(ub + 32 * (g) + 64); uv[2] = lds128f(ub + 32 * (g) + 128); uv[3] = lds128f(ub + 32 * (g) + 192); } while (0)
#define EP_ST() do { float m = ((st[0] + st[1]) + (st[2] + st[3])) * (1.f / 448.f); asm volatile("" : "+v"(m)); const float e2 = (((st[4] + st[5]) + st[6]) + st[7]) * (1.f / 448.f); \
      rs = __builtin_amdgcn_rsqf(fmaxf(e2 - m * m, 0.f) + 1e-5f); nm = -m * rs; } while (0)
    float gt, up, ez;
    constexpr float K1 = -2.f * 0.7978845608028654f * 1.4426950408889634f, K3 = K1 * 0.044715f;
#define EP_A(A, e) do { const float tg = fma1(nm, uv[0][(e) & 3], uv[2][(e) & 3]), tu = fma1(nm, uv[1][(e) & 3], uv[3][(e) & 3]); gt = fma1(A[e], rs, tg); up = fma1(A[8 + (e)], rs, tu); asm volatile("" : "+v"(gt), "+v"(up)); } while (0)
#define EP_B() do { ez = __builtin_amdgcn_exp2f(gt * fmaf(gt * gt, K3, K1)); up *= gt; asm volatile("" : "+v"(ez), "+v"(up)); } while (0)
#define EP_C(e) do { hv[e] = up * __builtin_amdgcn_rcpf(1.f + ez); asm volatile("" : "+v"(hv[e])); } while (0)
#define EP_EL(A, e) do { EP_A(A, e); EP_B(); EP_C(e); } while (0)
#define EP_OUT(T) st128(wsr, lane * 16, OFF_HB + (u32)((4 * gi + (T)) * 112 + 7 * gfj + wave) * 1024, (u32x4){pk2(hv[0], hv[1]), pk2(hv[2], hv[3]), pk2(hv[4], hv[5]), pk2(hv[6], hv[7])})
#pragma unroll
    for (int i = 0; i < 3; ++i) af[i] = lds128(ab + i * 1024);
#pragma unroll
    for (int t = 0; t < 4; ++t) {
#pragma unroll
      for (int s = 0; s < 28; ++s) {
        const int n = t * 28 + s;
        if (n == 25) { STAMPL(28, "gu n25"); WAIT_VM(0); BAR();  }
        if (n + 3 < 112) af[(n + 3) & 3] = (n + 3 < 56) ? lds128(ab + (n + 3) * 1024) : lds128(ab2 + (n + 3 - 56) * 1024);
        SB();
        if (s == 0) acc[t & 1] = mfma32(wgu[s], af[n & 3], (f32x16){}); else acc[t & 1] = mfma32(wgu[s], af[n & 3], acc[t & 1]);
        SB();
        if (t > 0) {
          if (s == 0) { EP_RD(t - 1); EP_UV(0); }
          if (s == 2) EP_ST();
          if (s >= 3 && s < 27) { if (s % 3 == 0) EP_A(acc[(t - 1) & 1], s / 3 - 1); else if (s % 3 == 1) EP_B(); else EP_C(s / 3 - 1); }
          if (s == 13) EP_UV(1);
          if (s == 27) EP_OUT(t - 1);
          if (s == 27 && t == 1) STAMPL(20, "gu tile1");
          if (s == 27 && t == 2) STAMPL(21, "gu tile2");
          if (s == 27 && t == 3) STAMPL(22, "gu tile3");
          if (s % 4 == 1 && s < 24) { const int f = 6 * (t - 1) + s / 4; wdf[f] = ld128(wsr, lane * 16, wdo + (u32)((f & 1) * 112 + (f >> 1)) * 1024); }
          SB();
        }
      }
    }
    asm volatile("s_nop 11" : "+v"(acc[1]));
    EP_RD(3); EP_UV(0); EP_ST();
    {
      f32x4 uw[4];
      uw[0] = lds128f(ub + 32); uw[1] = lds128f(ub + 96); uw[2] = lds128f(ub + 160); uw[3] = lds128f(ub + 224);
      float g8[8], u8[8];
#pragma unroll
      for (int e = 0; e < 8; ++e) {
        const f32x4* W = e < 4 ? uv : uw;
        g8[e] = fma1(acc[1][e], rs, fma1(nm, W[0][e & 3], W[2][e & 3])); u8[e] = fma1(acc[1][8 + e], rs, fma1(nm, W[1][e & 3], W[3][e & 3]));
      }
#pragma unroll
      for (int e = 0; e < 8; ++e) hv[e] = (g8[e] * u8[e]) * __builtin_amdgcn_rcpf(1.f + __builtin_amdgcn_exp2f(g8[e] * fmaf(g8[e] * g8[e], K3, K1)));
    }
    EP_OUT(3);
    WAIT_VM(0);
    if (lane == 0) flag32(ws + OFF_HF + ((gi * 16 + gfj) * 8 + wave) * 128, tag);
  } else {
    WAIT_VM(0); BAR();
#pragma unroll
    for (int f = 0; f < 18; ++f) wdf[f] = ld128(wsr, lane * 16, wdo + (u32)((f & 1) * 112 + (f >> 1)) * 1024);
  }
  STAMPL(23, "h flagged");
  if (dact) {
    BAR();
#pragma unroll
    for (int f = 18; f < 28; ++f) wdf[f] = ld128(wsr, lane * 16, wdo + (u32)((f & 1) * 112 + (f >> 1)) * 1024);
    const int drt = 8 * xg + 2 * dq, dcol = 64 * dc + 32 * dn + 16 * dgp + 8 * hi;
    gbA = ld128(wsr, dcol * 4, OFF_MODG + 1120 * 8); gbB = ld128(wsr, dcol * 4 + 16, OFF_MODG + 1120 * 8);
    wait_flags(ws + OFF_HF + (((drt >> 2) * 16 + 2 * wave + ((lane >> 3) & 1)) * 8 + min(lane & 7, 6)) * 128, tag);
    STAMPL(24, "h flags seen");
    u32x4 hf[20];
#define LDH(i) hf[(i) % 20] = ld128(wsr, lane * 16 + (((i) % 14) & 3) * 1024, OFF_HB + (u32)((drt + (i) / 14) * 112 + 14 * wave + (((i) % 14) & ~3)) * 1024)
#pragma unroll
    for (int i = 0; i < 20; ++i) { LDH(i); SB(); }
    x2r = ld128(wsr, lane * 16, OFF_X2 + (u32)((drt + dm) * 28 + 4 * dc + 2 * dn + dgp) * 1024);
    f32x16 c0, c1;
    u32 pwb = lb + wave * 16384 + lane * 16; asm volatile("" : "+v"(pwb));
#pragma unroll
    for (int i = 0; i < 28; ++i) {
      const int m = i / 14, j = i % 14;
      SB();
      if (j == 0) { c0 = mfma32(wdf[0], hf[i % 20], (f32x16){}); c1 = mfma32(wdf[1], hf[i % 20], (f32x16){}); }
      else { c0 = mfma32(wdf[2 * j], hf[i % 20], c0); c1 = mfma32(wdf[2 * j + 1], hf[i % 20], c1); }
      SB();
      if (i < 8) LDH(i + 20);
      if (j == 13) {
#pragma unroll
        for (int g = 0; g < 4; ++g) {
          *(LAS f32x4*)(pwb + ((2 * m) * 4 + g) * 1024) = (f32x4){c0[4 * g], c0[4 * g + 1], c0[4 * g + 2], c0[4 * g + 3]};
          *(LAS f32x4*)(pwb + ((2 * m + 1) * 4 + g) * 1024) = (f32x4){c1[4 * g], c1[4 * g + 1], c1[4 * g + 2], c1[4 * g + 3]};
        }
      }
    }
    STAMPL(25, "down mfma done");
    BAR();
    f32x4 pa[8], pb[8];
    {
      u32 base = lb + (2 * dm + dn) * 4096 + dgp * 2048 + lane * 16, base2 = base + 65536;
      asm volatile("" : "+v"(base), "+v"(base2));
#pragma unroll
      for (int w = 0; w < 4; ++w) {
        pa[w] = lds128f(base + w * 16384); pb[w] = lds128f(base + w * 16384 + 1024);
        pa[4 + w] = lds128f(base2 + w * 16384); pb[4 + w] = lds128f(base2 + w * 16384 + 1024);
      }
      SB();
    }
    const f32x4 a0 = ((pa[0] + pa[4]) + (pa[1] + pa[5])) + ((pa[2] + pa[6]) + (pa[3] + pa[7])), a1 = ((pb[0] + pb[4]) + (pb[1] + pb[5])) + ((pb[2] + pb[6]) + (pb[3] + pb[7]));
    float yv[8];
#pragma unroll
    for (int r = 0; r < 4; ++r) { auto sw = __builtin_amdgcn_permlane32_swap(__float_as_uint(a0[r]), __float_as_uint(a1[r]), false, false); yv[r] = __uint_as_float(sw[0]); yv[4 + r] = __uint_as_float(sw[1]); }
    const u32 gw[4] = {gbA[0], gbA[2], gbB[0], gbB[2]};
    u32x4 ov;
#pragma unroll
    for (int j = 0; j < 4; ++j) ov[j] = pk2(fmaf(bflo(gw[j]), yv[2 * j], bflo(x2r[j])), fmaf(bfhi(gw[j]), yv[2 * j + 1], bfhi(x2r[j])));
    *(u32x4*)(out + (size_t)(32 * (drt + dm) + l31) * 448 + dcol) = ov;
    if (b == 0 && tid == 0) *(GAS u32*)ws = tag;
  }
  STAMPL(26, "end");
}

extern "C" void kernel_launch(void* const* d_in, const int* in_sizes, int n_in, void* d_out, int out_size, void* d_ws, size_t ws_size, hipStream_t stream) {
  fused_block<<<256, 512, 0, stream>>>((const u16*)d_in[0], (const u16*)d_in[1], (const u16*)d_in[2], (const u16*)d_in[4], (const u16*)d_in[5],
                                       (const u16*)d_in[6], (const u16*)d_in[7], (const u16*)d_in[8], (u16*)d_out, (char*)d_ws);
  probe_spin<<<1, 64, 0, stream>>>((char*)d_ws, PROBE_MODE, PROBE_MUL);
}
```

```cpp
#define FIRST_POLL 180
#define PREP_START 120
#include <hip/hip_runtime.h>
#include <stdint.h>

typedef unsigned short u16;
typedef unsigned u32;
typedef unsigned long long u64;
typedef __attribute__((ext_vector_type(2))) unsigned u32x2;
typedef __attribute__((ext_vector_type(4))) unsigned u32x4;
typedef __attribute__((ext_vector_type(2))) float f32x2;
typedef __attribute__((ext_vector_type(4))) float f32x4;
typedef __attribute__((ext_vector_type(16))) float f32x16;
typedef __attribute__((ext_vector_type(2))) __bf16 bf16x2;
typedef __attribute__((ext_vector_type(8))) __bf16 bf16x8;
typedef __attribute__((ext_vector_type(4))) short s16x4;
#define DEV __device__ __forceinline__
#define GAS __attribute__((address_space(1)))
#define LAS __attribute__((address_space(3)))
#define RLX_AGENT __ATOMIC_RELAXED, __HIP_MEMORY_SCOPE_AGENT

constexpr u32 OFF_MODG = 4096;
constexpr u32 OFF_X2G = 65536, OFF_UV = 13467648  , OFF_WQT = 524288, OFF_WOT = 1900544, OFF_WGU = 2359296, OFF_WDT = 5570560;
constexpr u32 OFF_KV = 7176192, OFF_OB = 11370496, OFF_X2 = 15302656, OFF_HB = 17137664, OFF_XH = 24477696;
constexpr u32 OFF_XF = 26312704, OFF_WQF = OFF_XF + 512 * 128, OFF_KVG = OFF_WQF + 1024 * 128, OFF_OF = OFF_KVG + 1024 * 128, OFF_HF = OFF_OF + 2048 * 128, OFF_PREPF = OFF_HF + 2048 * 128, WS_USED = OFF_PREPF + 256 * 128;
constexpr u32 OFF_STAMP = 29360128;
constexpr u32 L_STG = 90112  , L_Q = 131072, L_GAM = 139264, L_QN = 142848;
constexpr u32 L_PARK = 64512 + 1024  ;
constexpr u32 L_SP2 = 151552  , L_MODP = L_SP2, L_MX = L_SP2, L_LP = L_SP2, L_UV = 155648  , L_ST = 157696  , L_G2 = 159488  ;
constexpr u32 L_SYNC = 160000;
constexpr u32 LDS_BYTES = 163840;
constexpr int CODE_BYTES = 40000;
constexpr int SPIN_CAP = 1 << 15;
#define KV_SLEEP 1
#define POLL_T 0
#define POLL_SLEEP 3

DEV float bflo(u32 w) { return __uint_as_float(w << 16); }
DEV float bfhi(u32 w) { return __uint_as_float(w & 0xffff0000u); }
DEV u32 pk2(float a, float b) { f32x2 v = {a, b}; return __builtin_bit_cast(u32, __builtin_convertvector(v, bf16x2)); }
DEV float rbf(float a) { return bflo(pk2(a, 0.f)); }
DEV float fma1(float a, float b, float c) { float r; asm("v_fma_f32 %0, %1, %2, %3" : "=v"(r) : "v"(a), "v"(b), "v"(c)); return r; }
DEV float dot2(u32 a, u32 b, float c) { return __builtin_amdgcn_fdot2_f32_bf16(__builtin_bit_cast(bf16x2, a), __builtin_bit_cast(bf16x2, b), c, false); }
DEV f32x16 mfma32(u32x4 a, u32x4 b, f32x16 c) { return __builtin_amdgcn_mfma_f32_32x32x16_bf16(__builtin_bit_cast(bf16x8, a), __builtin_bit_cast(bf16x8, b), c, 0, 0, 0); }
DEV f32x4 mfma16(u32x4 a, u32x4 b, f32x4 c) { return __builtin_amdgcn_mfma_f32_16x16x32_bf16(__builtin_bit_cast(bf16x8, a), __builtin_bit_cast(bf16x8, b), c, 0, 0, 0); }
DEV float xhalf_sum(float v) { auto r = __builtin_amdgcn_permlane32_swap(__float_as_uint(v), __float_as_uint(v), false, false); return __uint_as_float(r[0]) + __uint_as_float(r[1]); }
DEV float xhalf_max(float v) { auto r = __builtin_amdgcn_permlane32_swap(__float_as_uint(v), __float_as_uint(v), false, false); return fmaxf(__uint_as_float(r[0]), __uint_as_float(r[1])); }
#define DPPF(v, ctrl) __uint_as_float(__builtin_amdgcn_update_dpp(0u, __float_as_uint(v), ctrl, 0xf, 0xf, false))
DEV float wave_max(float v) {
  v = fmaxf(v, DPPF(v, 0x121)); v = fmaxf(v, DPPF(v, 0x122)); v = fmaxf(v, DPPF(v, 0x124)); v = fmaxf(v, DPPF(v, 0x128));
  auto r = __builtin_amdgcn_permlane16_swap(__float_as_uint(v), __float_as_uint(v), false, false);
  return xhalf_max(fmaxf(__uint_as_float(r[0]), __uint_as_float(r[1])));
}
#define WAIT_VM(n) asm volatile("s_waitcnt vmcnt(" #n ")" ::: "memory")
#define BAR() asm volatile("s_waitcnt lgkmcnt(0)\n\ts_barrier" ::: "memory")
#define SB() __builtin_amdgcn_sched_barrier(0)
#define CFENCE() asm volatile("" ::: "memory")
template <bool SC1> DEV void dma16(const void* sbase, u32 voff, u32 lds_dst) {
  u32 keep;
  if (SC1) asm volatile("s_mov_b32 %0, m0\n\ts_mov_b32 m0, %3\n\ts_nop 2\n\tglobal_load_lds_dwordx4 %1, %2 sc1\n\ts_mov_b32 m0, %0" : "=&s"(keep) : "v"(voff), "s"(sbase), "s"(lds_dst) : "memory");
  else asm volatile("s_mov_b32 %0, m0\n\ts_mov_b32 m0, %3\n\ts_nop 2\n\tglobal_load_lds_dwordx4 %1, %2\n\ts_mov_b32 m0, %0" : "=&s"(keep) : "v"(voff), "s"(sbase), "s"(lds_dst) : "memory");
}
DEV void dma16x4(const void* sbase, u32 voff, u32 lds_dst) {
  u32 keep;
  asm volatile("s_mov_b32 %0, m0\n\ts_mov_b32 m0, %3\n\ts_nop 2\n\tglobal_load_lds_dwordx4 %1, %2 sc1\n\tglobal_load_lds_dwordx4 %1, %2 offset:1024 sc1\n\t"
               "global_load_lds_dwordx4 %1, %2 offset:2048 sc1\n\tglobal_load_lds_dwordx4 %1, %2 offset:3072 sc1\n\ts_mov_b32 m0, %0" : "=&s"(keep) : "v"(voff), "s"(sbase), "s"(lds_dst) : "memory");
}
DEV void dma4_touch(const void* sbase, u32 voff, u32 lds_dst) {
  u32 keep;
  asm volatile("s_mov_b32 %0, m0\n\ts_mov_b32 m0, %3\n\ts_nop 2\n\tglobal_load_lds_dword %1, %2 sc1\n\ts_mov_b32 m0, %0" : "=&s"(keep) : "v"(voff), "s"(sbase), "s"(lds_dst) : "memory");
}
DEV u32x4 lds128(u32 a) { return *(const LAS u32x4*)a; }
DEV f32x4 lds128f(u32 a) { return *(const LAS f32x4*)a; }
DEV float lds32f(u32 a) { return *(const LAS float*)a; }
DEV u32x2 ldstr(u32 a) { return __builtin_bit_cast(u32x2, __builtin_amdgcn_ds_read_tr16_b64_v4i16((LAS s16x4*)a)); }
DEV u32x4 cat(u32x2 a, u32x2 b) { return (u32x4){a.x, a.y, b.x, b.y}; }
typedef __amdgpu_buffer_rsrc_t rsrc_t;
DEV u32x4 ld128(rsrc_t r, u32 voff, u32 soff) { return __builtin_amdgcn_raw_buffer_load_b128(r, voff, soff, 16); }
DEV u32 ld32(rsrc_t r, u32 voff, u32 soff) { return __builtin_amdgcn_raw_buffer_load_b32(r, voff, soff, 16); }
DEV void st128(rsrc_t r, u32 voff, u32 soff, u32x4 v) {
  __builtin_amdgcn_raw_buffer_store_b128(v, r, voff, soff, 16);
  asm volatile("s_nop 1" :: "v"(v) : "memory");
}
DEV void st32(rsrc_t r, u32 voff, u32 soff, u32 v) { __builtin_amdgcn_raw_buffer_store_b32(v, r, voff, soff, 16); }
DEV u32 poll32(const char* p) { return __hip_atomic_load((GAS u32*)p, RLX_AGENT); }
DEV u64 poll64(const char* p) { return __hip_atomic_load((GAS u64*)p, RLX_AGENT); }
DEV void flag32(char* p, u32 v) { __hip_atomic_store((GAS u32*)p, v, RLX_AGENT); }
DEV void flag64(char* p, u32 val, u32 tag) { __hip_atomic_store((GAS u64*)p, ((u64)tag << 32) | val, RLX_AGENT); }
DEV void wait_flags(const char* p, u32 tag) {
#pragma nounroll
  for (int i = 0; i < SPIN_CAP; ++i) { if (__all(poll32(p) == tag)) break; __builtin_amdgcn_s_sleep(POLL_SLEEP); }
  CFENCE();
}
DEV u32 wait_gran(const char* p, u32 tag) {
  u64 g = 0;
#pragma nounroll
  for (int i = 0; i < SPIN_CAP; ++i) { g = poll64(p); if (__all((u32)(g >> 32) == tag)) break; __builtin_amdgcn_s_sleep(POLL_SLEEP); }
  CFENCE();
  return (u32)g;
}
DEV u32 lds_ld(u32 a) { return __hip_atomic_load((LAS u32*)a, __ATOMIC_RELAXED, __HIP_MEMORY_SCOPE_WORKGROUP); }
DEV void lds_st(u32 a, u32 v) { __hip_atomic_store((LAS u32*)a, v, __ATOMIC_RELAXED, __HIP_MEMORY_SCOPE_WORKGROUP); }
DEV u32 lds_inc(u32 a) { return __builtin_amdgcn_readfirstlane(__hip_atomic_fetch_add((LAS u32*)a, 1u, __ATOMIC_RELAXED, __HIP_MEMORY_SCOPE_WORKGROUP)); }
DEV void lds_wait(u32 a, u32 want) {
#pragma nounroll
  for (int i = 0; i < (SPIN_CAP << 6); ++i) { if (__builtin_amdgcn_readfirstlane(lds_ld(a)) >= want) break; __builtin_amdgcn_s_sleep(1); }
  CFENCE();
}
#define WAIT_LGKM() asm volatile("s_waitcnt lgkmcnt(0)" ::: "memory")
#define STAMP(i) do {} while (0)
#define STAMPL(i, s) STAMP(i)

__global__ __launch_bounds__(512, 2) void fused_block(const u16* __restrict__ x, const u16* __restrict__ wqkv, const u16* __restrict__ wo,
                                                      const u16* __restrict__ wg, const u16* __restrict__ wu, const u16* __restrict__ wd,
                                                      const u16* __restrict__ cvec, const u16* __restrict__ wmod, u16* __restrict__ out, char* ws) {
  __shared__ __attribute__((aligned(16))) char smem[LDS_BYTES];
  const int b = blockIdx.x, tid = threadIdx.x, lane = tid & 63;
  const int wave = __builtin_amdgcn_readfirstlane(tid >> 6);
  const int l31 = lane & 31, hi = lane >> 5;
  const int xg = b & 7, slot = b >> 3;
  const int ai = 4 * (xg >> 1) + (slot & 3), ah = 8 * (xg & 1) + (slot >> 2);
  const u32 lb = (u32)(uintptr_t)(LAS char*)smem;
  const rsrc_t wsr = __builtin_amdgcn_make_buffer_rsrc(ws, 0, WS_USED, 0x00020000);
  STAMP(0);
  {
    u64 pc; asm volatile("s_getpc_b64 %0" : "=s"(pc));
    if (wave == 0) {
      const u32 off = min((u32)(slot * 1280 + min(lane, 9) * 128), (u32)CODE_BYTES);
      u32 keep;
      asm volatile("s_mov_b32 %0, m0\n\ts_mov_b32 m0, %3\n\ts_nop 2\n\tglobal_load_lds_dword %1, %2\n\ts_mov_b32 m0, %0" : "=&s"(keep) : "v"(off), "s"(pc), "s"(lb + L_ST) : "memory");
    }
  }
  asm volatile("" :: "s"(x), "s"(wqkv), "s"(wo), "s"(wg), "s"(wu), "s"(wd), "s"(cvec), "s"(wmod), "s"(out), "s"(ws) : "memory");
  SB();
  if (wave == 1) {
    const u32 off = min(lane, 12) << 21;
    u32 keep;
    asm volatile("s_mov_b32 %0, m0\n\ts_mov_b32 m0, %3\n\ts_nop 2\n\tglobal_load_lds_dword %1, %2\n\ts_mov_b32 m0, %0" : "=&s"(keep) : "v"(off), "s"(ws), "s"(lb + L_ST + 256) : "memory");
  }
  if (wave == 2 && lane < 16) lds_st(lb + L_SYNC + lane * 4, 0u);
  const u64 t_entry = __builtin_amdgcn_s_memrealtime();
  const u32 epoch_v = poll32(ws);
  u32 tag;

  f32x16 accQ, accK, accV;
  const int tq4 = (lane & 15) >> 2, tp4 = lane & 3, grp = lane >> 4;
  if (b < 42) {
    u32x4 wv[7];
    const u32 cv = cvec[56 * wave + min(lane, 55)];
#pragma unroll
    for (int n = 0; n < 7; ++n) wv[n] = *(const u32x4*)((const char*)wmod + (size_t)(56 * wave + 8 * n + (lane >> 3)) * 5376 + 128 * b + 16 * (lane & 7));
    BAR();
    const float cf = bflo(cv);
    const float sl = rbf(cf * __builtin_amdgcn_rcpf(1.f + __builtin_amdgcn_exp2f(-1.4426950408889634f * cf)));
    float acc[8] = {0.f, 0.f, 0.f, 0.f, 0.f, 0.f, 0.f, 0.f};
#pragma unroll
    for (int n = 0; n < 7; ++n) {
      const float sn = __uint_as_float(__builtin_amdgcn_ds_bpermute((8 * n + (lane >> 3)) * 4, __float_as_uint(sl)));
#pragma unroll
      for (int j = 0; j < 4; ++j) { acc[2 * j] = fmaf(sn, bflo(wv[n][j]), acc[2 * j]); acc[2 * j + 1] = fmaf(sn, bfhi(wv[n][j]), acc[2 * j + 1]); }
    }

#pragma unroll
    for (int j = 0; j < 8; ++j) acc[j] += DPPF(acc[j], 0x128);
#pragma unroll
    for (int j = 0; j < 4; ++j) { auto r = __builtin_amdgcn_permlane16_swap(__float_as_uint(acc[j]), __float_as_uint(acc[j + 4]), false, false); acc[j] = __uint_as_float(r[0]) + __uint_as_float(r[1]); }
#pragma unroll
    for (int j = 0; j < 2; ++j) { auto r = __builtin_amdgcn_permlane32_swap(__float_as_uint(acc[j]), __float_as_uint(acc[j + 2]), false, false); acc[j] = __uint_as_float(r[0]) + __uint_as_float(r[1]); }
    if (!(lane & 8)) *(LAS f32x2*)(lb + L_MODP + (wave * 64 + 8 * (lane & 7) + 2 * hi + 4 * (grp & 1)) * 4) = (f32x2){acc[0], acc[1]};
    WAIT_LGKM();
    u32 arrived = 0;
    if (lane == 0) arrived = __hip_atomic_fetch_add((LAS u32*)(lb + L_SYNC + 12), 1u, __ATOMIC_RELAXED, __HIP_MEMORY_SCOPE_WORKGROUP);
    if (__builtin_amdgcn_readfirstlane(arrived) == 7) {
      CFENCE();
      f32x2 t = {0.f, 0.f};
#pragma unroll
      for (int p = 0; p < 8; ++p) t += *(const LAS f32x2*)(lb + L_MODP + (p * 64 + 2 * l31) * 4);
      const u32 pv = pk2(t[0], t[1]), nv = __builtin_amdgcn_update_dpp(0u, pv, 0xB1, 0xf, 0xf, false);
      st128(wsr, (lane < 32 && !(lane & 1)) ? (u32)(32 * b + lane) * 8 : 0x80000000u, OFF_MODG, (u32x4){pv, epoch_v + 1u, nv, epoch_v + 1u});
      WAIT_VM(0);
      lds_st(lb + L_SYNC + 32, 1u);
    }
  } else if (wave < 3 && 3 * (b - 42) + wave < 512) {
    const int xgp = 3 * (b - 42) + wave, row = 4 * xgp + (lane & 3), c0i = lane >> 2;
    u32x4 xr[4];
#pragma unroll
    for (int n = 0; n < 4; ++n) xr[n] = *(const u32x4*)((const char*)x + (size_t)row * 896 + min(c0i + 16 * n, 55) * 16);
    BAR();
    if (c0i >= 8) xr[3] = (u32x4){0u, 0u, 0u, 0u};
    float s1[2] = {0.f, 0.f}, s2[2] = {0.f, 0.f};
#pragma unroll
    for (int n = 0; n < 4; ++n)
#pragma unroll
      for (int j = 0; j < 4; ++j) { s1[j & 1] = dot2(xr[n][j], 0x3F803F80u, s1[j & 1]); s2[j & 1] = dot2(xr[n][j], xr[n][j], s2[j & 1]); }
    float t1 = s1[0] + s1[1], t2 = s2[0] + s2[1];
    t1 += DPPF(t1, 0x124); t2 += DPPF(t2, 0x124); t1 += DPPF(t1, 0x128); t2 += DPPF(t2, 0x128);
    { auto r = __builtin_amdgcn_permlane16_swap(__float_as_uint(t1), __float_as_uint(t2), false, false); t1 = __uint_as_float(r[0]) + __uint_as_float(r[1]); }
    t1 = xhalf_sum(t1);
    { auto r = __builtin_amdgcn_permlane16_swap(__float_as_uint(t1), __float_as_uint(t1), false, false); t1 = __uint_as_float(r[0]); t2 = __uint_as_float(r[1]); }
    const float mean = t1 * (1.f / 448.f), ex2 = t2 * (1.f / 448.f);
    const float rstd = __builtin_amdgcn_rsqf(fmaxf(ex2 - mean * mean, 0.f) + 1e-5f), c0 = -mean * rstd;
    asm volatile("" :: "v"(c0));

#pragma unroll
    for (int n = 0; n < 4; ++n) {
      u32x4 o;
#pragma unroll
      for (int j = 0; j < 4; ++j) asm("v_cvt_pk_f16_f32 %0, %1, %2" : "=v"(o[j]) : "v"(fmaf(bflo(xr[n][j]), rstd, c0)), "v"(fmaf(bfhi(xr[n][j]), rstd, c0)));
      const u32 c = c0i + 16 * n;
      st128(wsr, c < 56 ? (c >> 1) * 1024 + ((row & 31) + 32 * (c & 1)) * 16 + (u32)(row >> 5) * 28672 : 0x80000000u, OFF_XH, o);
    }
    WAIT_VM(0);
    if (lane == 0) flag32(ws + OFF_XF + xgp * 128, epoch_v + 1u);
  } else if ((wave == 4 || wave == 5) && b < 210) {
    const int u = b - 42, part = u / 56, rem = u - part * 56, hp = rem / 7, kg = rem - hp * 7, s0 = 4 * kg + 2 * (wave - 4);
    const char* src = (const char*)wqkv + (size_t)(16 * s0) * 2688 + part * 896 + hp * 112;
    const u32 stg = lb + L_Q + (wave - 4) * 4096;
#pragma unroll
    for (int i = 0; i < 4; ++i) { const u32 c = lane & 7; dma16<false>(src + i * 8 * 2688, (lane >> 3) * 2688 + 16 * (c == 7 ? 6 : c), stg + i * 1024); }
    BAR();
    WAIT_VM(0);

#pragma unroll
    for (int e = 0; e < 4; ++e) {
      const u32 a = stg + (16 * (e >> 1) + 8 * (grp >> 1) + tq4) * 128 + (e & 1) * 56 + (grp & 1) * 32 + tp4 * 8;
      u32x4 f = cat(ldstr(a), ldstr(a + 512));
      if (l31 >= 28) f = (u32x4){0u, 0u, 0u, 0u};
      st128(wsr, lane * 16, OFF_WQT + (u32)((2 * hp + (e & 1)) * 84 + 3 * (s0 + (e >> 1)) + part) * 1024, f);
    }
    WAIT_VM(0);
    if (lane < 2) flag32(ws + OFF_WQF + ((2 * hp + lane) * 64 + 3 * (s0 >> 1) + part) * 128, epoch_v + 1u);
  } else {
    BAR();
  }
  tag = __builtin_amdgcn_readfirstlane(epoch_v) + 1u;
  STAMPL(7, "unit done");
  const int cg = b / 7, kg = b - cg * 7;
  if (wave >= 6) {
#pragma nounroll
    for (int i = 0; i < 4096 && __builtin_amdgcn_s_memrealtime() - t_entry < PREP_START; ++i) __builtin_amdgcn_s_sleep(1);
#pragma unroll
    for (int ph = 0; ph < 2; ++ph) {
      const int pw = wave & 1, tw = 2 * ph + pw, p = 4 * b + tw;
      const u32 stg = lb + L_STG + tw * 6144;
      if (b < 196) {
#pragma unroll
        for (int i = 0; i < 2; ++i) {
          const u32 vo = (u32)(64 * kg + 32 * ph + 16 * i + (lane >> 2)) * 3584 + (64 * cg + 32 * pw) * 2 + 16 * (lane & 3);
          dma16<false>(wg, vo, stg + (2 * i) * 1024); dma16<false>(wu, vo, stg + (2 * i + 1) * 1024);
        }
      }
      if (p < 784) {
        const int ds_ = p / 7, np = p - ds_ * 7;
#pragma unroll
        for (int i = 0; i < 2; ++i) dma16<false>(wd, (u32)(16 * ds_ + (lane >> 2)) * 896 + np * 128 + i * 64 + 16 * (lane & 3), stg + (4 + i) * 1024);
      } else if (p < 1008) {
        const int q = p - 784, h = q / 14, nq = q - h * 14;
#pragma unroll
        for (int i = 0; i < 2; ++i) dma16<false>(wo, (u32)min(28 * h + 16 * i + (lane >> 2), 447) * 896 + nq * 64 + 16 * (lane & 3), stg + (4 + i) * 1024);
      }
    }
    STAMPL(6, "prep issued");
    WAIT_VM(0);
    if (lane == 0) __hip_atomic_fetch_add((LAS u32*)(lb + L_SYNC + 52), 1u, __ATOMIC_RELAXED, __HIP_MEMORY_SCOPE_WORKGROUP);
    STAMPL(5, "prep landed");
  } else if (wave >= 3) {
#pragma nounroll
    for (int i = 0; i < 4096 && __builtin_amdgcn_s_memrealtime() - t_entry < FIRST_POLL; ++i) __builtin_amdgcn_s_sleep(1);
  }

  if (wave < 4) {
    u32x4 xf[28], pf[6]; u32 pfo[6], uvo[2]; float uvv[2];
    const int pw = wave & 1, ph = (wave >> 1) & 1;
    auto tile_read = [&]() __attribute__((always_inline)) {
      const int p = 4 * b + wave;
      const u32 stg = lb + L_STG + wave * 6144;
      const bool isd = p < 784;
      const int ds_ = p / 7, np = p - ds_ * 7, q = p - 784, h = q / 14, nq = q - h * 14;
      lds_wait(lb + L_SYNC + 52, 2);
      const int pi = ((tp4 & 1) << 1) | (tp4 >> 1);
#pragma unroll
      for (int i = 0; i < 2; ++i)
#pragma unroll
        for (int e = 0; e < 2; ++e) {
          const u32 a = stg + (2 * i + (grp & 1)) * 1024 + (8 * (grp >> 1) + tq4) * 64 + e * 32 + pi * 8;
          pf[2 * i + e] = cat(ldstr(a), ldstr(a + 256));
          pfo[2 * i + e] = lane * 16 + (b < 196 ? OFF_WGU + (u32)((4 * cg + 2 * pw + e) * 28 + 4 * kg + 2 * ph + i) * 1024 : 0x80000000u);
        }
#pragma unroll
      for (int i = 0; i < 2; ++i) {
        const u32 a = isd ? stg + (4 + i) * 1024 + (8 * (grp >> 1) + tq4) * 64 + (grp & 1) * 32 + tp4 * 8
                          : stg + 4096 + (16 * (grp >> 1) + 4 * (grp & 1) + tq4) * 64 + i * 32 + tp4 * 8;
        u32x4 f = cat(ldstr(a), ldstr(a + (isd ? 256 : 512)));
        if (!isd && grp == 3) { f[2] = 0u; f[3] = 0u; }
        pf[4 + i] = f;
        pfo[4 + i] = lane * 16 + (isd ? OFF_WDT + (u32)((2 * np + i) * 112 + ds_) * 1024 : p < 1008 ? OFF_WOT + (u32)((2 * nq + i) * 16 + h) * 1024 : 0x80000000u);
        uvo[i] = lane * 4 + (b < 196 ? OFF_UV + (u32)((2 * kg + ph) * 112 + 4 * cg + 2 * pw + i) * 256 : 0x80000000u);
      }
      asm volatile("" : "+v"(pf[0]), "+v"(pf[1]), "+v"(pf[2]), "+v"(pf[3]), "+v"(pf[4]), "+v"(pf[5]));
    };
    if (wave == 3) { wait_flags(ws + OFF_XF + (32 * ai + l31) * 128, tag); tile_read(); lds_wait(lb + L_SYNC + 36, 1); lds_st(lb + L_SYNC, 1u); }
    else { tile_read(); lds_wait(lb + L_SYNC, 1); }
    STAMPL(3, "xhat go");
    constexpr int XPRE = 12;
#define LDX(s) xf[s] = ld128(wsr, lane * 16 + ((s) & 3) * 1024, OFF_XH + (u32)((4 * ai + wave) * 28 + ((s) & ~3)) * 1024)
#pragma unroll
    for (int s = 0; s < 4; ++s) { LDX(s); SB(); }
    {
      float uu[2] = {0.f, 0.f}, vv[2] = {0.f, 0.f};
#pragma unroll
      for (int i = 0; i < 2; ++i) {
        const u32x4 be = lds128(lb + L_G2 + (2 * ph + i) * 32 + hi * 16), ga = lds128(lb + L_G2 + 128 + (2 * ph + i) * 32 + hi * 16);
#pragma unroll
        for (int e = 0; e < 2; ++e)
#pragma unroll
          for (int j = 0; j < 4; ++j) {
            const u32 w = pf[2 * i + e][j];
            vv[e] = dot2(w, be[j], vv[e]);
            pf[2 * i + e][j] = pk2(bflo(w) * bflo(ga[j]), bfhi(w) * bfhi(ga[j]));
            uu[e] = dot2(pf[2 * i + e][j], 0x3F803F80u, uu[e]);
          }
      }
#pragma unroll
      for (int e = 0; e < 2; ++e) { const float us = xhalf_sum(uu[e]), vs = xhalf_sum(vv[e]); uvv[e] = hi ? vs : us; }
      asm volatile("" : "+v"(pf[0]), "+v"(pf[1]), "+v"(pf[2]), "+v"(pf[3]), "+v"(uvv[0]), "+v"(uvv[1]));
      STAMPL(10, "prep tiled");
    }
#pragma unroll
    for (int s = 4; s < XPRE; ++s) { LDX(s); SB(); }
    lds_wait(lb + L_SYNC + 4, 4);

    STAMPL(8, "W1 ready");
    accQ = (f32x16){}; accK = (f32x16){}; accV = (f32x16){};
    {
      u32 gb = lb + L_GAM + hi * 16; asm volatile("" : "+v"(gb));
      u32 wb = lb + lane * 16, wb2 = wb + 43008; asm volatile("" : "+v"(wb), "+v"(wb2));
      u32x4 wf[3][3], hf[2], gs[3], be[3]; float ht[8];
#define RDG(s) gs[(s) % 3] = lds128(gb + (s) * 32)
#define RDB(s) be[(s) % 3] = lds128(gb + 896 + (s) * 32)
#define RDW1(s, p) wf[(s) % 3][p] = lds128(((s) < 14 ? wb : wb2) + (3 * ((s) % 14) + (p)) * 1024)
#define RDW(s) do { RDW1(s, 0); RDW1(s, 1); RDW1(s, 2); } while (0)
#define DOTS(s, h) do { _Pragma("unroll") for (int j = 2 * (h); j < 2 * (h) + 2; ++j) { \
        asm volatile("v_fma_mix_f32 %0, %1, %2, %3 op_sel_hi:[1,1,1]" : "=v"(ht[2 * j]) : "v"(xf[s][j]), "v"(gs[(s) % 3][j]), "v"(be[(s) % 3][j])); \
        asm volatile("v_fma_mix_f32 %0, %1, %2, %3 op_sel:[1,1,1] op_sel_hi:[1,1,1]" : "=v"(ht[2 * j + 1]) : "v"(xf[s][j]), "v"(gs[(s) % 3][j]), "v"(be[(s) % 3][j])); } } while (0)
#define CVTS(s) do { hf[(s) & 1] = (u32x4){pk2(ht[0], ht[1]), pk2(ht[2], ht[3]), pk2(ht[4], ht[5]), pk2(ht[6], ht[7])}; asm volatile("" : "+v"(hf[(s) & 1])); } while (0)
      RDG(0); RDB(0); RDW(0); RDG(1); RDB(1); RDW(1); RDG(2); RDB(2); RDW(2); DOTS(0, 0); DOTS(0, 1); CVTS(0); SB();
#pragma unroll
      for (int s = 0; s < 28; ++s) {
        if (s == 1) lds_wait(lb + L_SYNC + 8, 3);
        if (s == 13) lds_wait(lb + L_SYNC + 24, 3);
        SB(); accQ = mfma32(wf[s % 3][0], hf[s & 1], accQ); SB();
        if (s < 25) RDG(s + 3);
        if (s < 25) RDW1(s + 3, 0);
        if (s < 27) DOTS(s + 1, 0);
        SB(); accK = mfma32(wf[s % 3][1], hf[s & 1], accK); SB();
        if (s < 25) RDB(s + 3);
        if (s < 25) RDW1(s + 3, 1);
        if (s < 27) DOTS(s + 1, 1);
        SB(); accV = mfma32(hf[s & 1], wf[s % 3][2], accV); SB();
        if (s < 25) RDW1(s + 3, 2);
        if (s + XPRE < 28) LDX(s + XPRE);
        else if (s < 22) st128(wsr, pfo[s - 16], 0, pf[s - 16]);
        else if (s < 24) st32(wsr, uvo[s - 22], 0, __float_as_uint(uvv[s - 22]));
        if (s < 27) CVTS(s + 1);
        SB();
      }
    }
    STAMPL(5, "qkv mfma done");
    const float unscale = __uint_as_float(__builtin_amdgcn_readfirstlane(lds_ld(lb + L_SYNC + 28)));
    if (unscale != 1.f) { accQ *= unscale; accK *= unscale; accV *= unscale; }
    u32x4 k0, k1, v0, v1, q0, q1;
#pragma unroll
    for (int j = 0; j < 4; ++j) {
      k0[j] = pk2(accK[2 * j], accK[2 * j + 1]); k1[j] = pk2(accK[8 + 2 * j], accK[9 + 2 * j]);
      v0[j] = pk2(accV[2 * j], accV[2 * j + 1]); v1[j] = pk2(accV[8 + 2 * j], accV[9 + 2 * j]);
    }
    if (hi) { k1[2] = 0x3F803F80u; k1[3] = 0x00003F80u; }
    if (l31 == 28) { v0 = (u32x4){0x3F803F80u, 0x3F803F80u, 0x3F803F80u, 0x3F803F80u}; v1 = v0; }
    const u32 kvo = OFF_KV + (u32)(ah * 64 + ai * 4 + wave) * 4096;
    st128(wsr, lane * 16, kvo, k0); st128(wsr, lane * 16 + 1024, kvo, k1); st128(wsr, lane * 16 + 2048, kvo, v0); st128(wsr, lane * 16 + 3072, kvo, v1);
    SB();
    float kn = 0.f, qn = 0.f;
    constexpr float QS = 0.18898223650461362f * 1.4426950408889634f;
#pragma unroll
    for (int r = 0; r < 16; ++r) { kn = fmaf(accK[r], accK[r], kn); accQ[r] *= QS; qn = fmaf(accQ[r], accQ[r], qn); }
#pragma unroll
    for (int j = 0; j < 4; ++j) { q0[j] = pk2(accQ[2 * j], accQ[2 * j + 1]); q1[j] = pk2(accQ[8 + 2 * j], accQ[9 + 2 * j]); }
    *(LAS u32x4*)(lb + L_Q + wave * 2048 + lane * 16) = q0; *(LAS u32x4*)(lb + L_Q + wave * 2048 + 1024 + lane * 16) = q1;
    qn = xhalf_sum(qn); kn = wave_max(xhalf_sum(kn));
    if (!hi) *(LAS float*)(lb + L_QN + (wave * 32 + l31) * 4) = qn;
    WAIT_VM(0);
    if (lane == 0) flag64(ws + OFF_KVG + (ah * 64 + ai * 4 + wave) * 128, __float_as_uint(kn), tag);
    if (b < 42) lds_wait(lb + L_SYNC + 32, 1);
    if (lane == 0 && __hip_atomic_fetch_add((LAS u32*)(lb + L_SYNC + 44), 1u, __ATOMIC_RELAXED, __HIP_MEMORY_SCOPE_WORKGROUP) == 3u) flag32(ws + OFF_PREPF + b * 128, tag);
    STAMPL(6, "kv flagged");
  } else {
    if (wave == 4) {
      u64 gv[8];
      __builtin_amdgcn_s_setprio(3);
#pragma nounroll
      for (int i = 0; i < SPIN_CAP; ++i) {
        bool ok = true;
#pragma unroll
        for (int n = 0; n < 7; ++n) gv[n] = poll64(ws + OFF_MODG + (64 * n + lane) * 8);
        gv[7] = poll64(ws + OFF_MODG + (672 + 224 * hi + 32 * kg + l31) * 8);
#pragma unroll
        for (int n = 0; n < 8; ++n) ok = ok && (u32)(gv[n] >> 32) == tag;
        if (__all(ok)) break;
        __builtin_amdgcn_s_sleep(POLL_SLEEP);
      }
      CFENCE();
      __builtin_amdgcn_s_setprio(0);
      *(LAS u32*)(lb + L_G2 + lane * 4) = hi ? pk2(1.f + bflo((u32)gv[7]), 1.f + bfhi((u32)gv[7])) : (u32)gv[7];
      WAIT_LGKM();
      lds_st(lb + L_SYNC + 36, 1u);

      float lo[7], hi2[7], mxv = 0.f;
#pragma unroll
      for (int n = 0; n < 7; ++n) {
        u32 v = (u32)gv[n];
        if (64 * n + lane >= 224) v = pk2(1.f + bflo(v), 1.f + bfhi(v));
        lo[n] = bflo(v); hi2[n] = bfhi(v); mxv = fmaxf(mxv, fmaxf(fabsf(lo[n]), fabsf(hi2[n])));
      }
      const u32 ex = max((int)(__float_as_uint(wave_max(mxv)) >> 23) - (127 + 14), 0);
      const float scl = __uint_as_float((127u - min(ex, 114u)) << 23);
#pragma unroll
      for (int n = 0; n < 7; ++n) {
        u32 o; asm("v_cvt_pk_f16_f32 %0, %1, %2" : "=v"(o) : "v"(lo[n] * scl), "v"(hi2[n] * scl));
        const int g = 64 * n + lane;
        *(LAS u32*)(lb + L_GAM + (g >= 224 ? g - 224 : g + 224) * 4) = o;
      }
      lds_st(lb + L_SYNC + 28, (127u + min(ex, 114u)) << 23);
      WAIT_LGKM();
      if (lane == 0) __hip_atomic_fetch_add((LAS u32*)(lb + L_SYNC + 4), 1u, __ATOMIC_RELAXED, __HIP_MEMORY_SCOPE_WORKGROUP);
    } else {
      if (wave == 5) { wait_flags(ws + OFF_WQF + (ah * 64 + min(lane, 41)) * 128, tag); lds_wait(lb + L_SYNC + 36, 1); lds_st(lb + L_SYNC + 48, 1u); }
      else lds_wait(lb + L_SYNC + 48, 1);

      const char* wsrc = ws + OFF_WQT + (size_t)ah * 84 * 1024 + (wave - 5) * 4096;
#pragma unroll
      for (int n = 0; n < 7; ++n) dma16x4(wsrc + n * 12288, lane * 16, lb + (wave - 5) * 4096 + n * 12288);
    }
    if (wave >= 5) {
      WAIT_VM(24);
      if (lane == 0) __hip_atomic_fetch_add((LAS u32*)(lb + L_SYNC + 4), 1u, __ATOMIC_RELAXED, __HIP_MEMORY_SCOPE_WORKGROUP);
      WAIT_VM(12);
      if (lane == 0) __hip_atomic_fetch_add((LAS u32*)(lb + L_SYNC + 8), 1u, __ATOMIC_RELAXED, __HIP_MEMORY_SCOPE_WORKGROUP);
      WAIT_VM(0);
      if (lane == 0) __hip_atomic_fetch_add((LAS u32*)(lb + L_SYNC + 24), 1u, __ATOMIC_RELAXED, __HIP_MEMORY_SCOPE_WORKGROUP);
      STAMPL(9, "W landed");
    }
  }
  BAR();
  STAMPL(11, "B3");

  const int ort = 8 * xg + (slot >> 2), ocq = slot & 3;
  const int gi = ai, gfj = ah;
  u32x4 wo_f[16], wgu[28], xres, gaA, gaB;
  {
    u32x4 qf[4][2];
#pragma unroll
    for (int t = 0; t < 4; ++t) { qf[t][0] = lds128(lb + L_Q + t * 2048 + lane * 16); qf[t][1] = lds128(lb + L_Q + t * 2048 + 1024 + lane * 16); }
    float qn2[4];
#pragma unroll
    for (int t = 0; t < 4; ++t) qn2[t] = lds32f(lb + L_QN + (t * 32 + l31) * 4);
    WAIT_LGKM();
    if (lane == 0) __hip_atomic_fetch_add((LAS u32*)(lb + L_SYNC + 40), 1u, __ATOMIC_RELAXED, __HIP_MEMORY_SCOPE_WORKGROUP);
    if (wave == 3) {
      __builtin_amdgcn_s_sleep(KV_SLEEP);
      const float km = wave_max(__uint_as_float(wait_gran(ws + OFF_KVG + (ah * 64 + lane) * 128, tag)));
      lds_st(lb + L_SYNC + 60, __float_as_uint(km));
      WAIT_LGKM();
      lds_st(lb + L_SYNC + 56, 1u);
    } else lds_wait(lb + L_SYNC + 56, 1);
    const float kmax2 = __uint_as_float(lds_ld(lb + L_SYNC + 60));
    STAMPL(12, "kv flags seen");
    const u32 kvb = OFF_KV + (u32)(ah * 64 + 8 * wave) * 4096;
    u32x4 kf[2][2], vf[2][2];
    const u32 kvl = lane * 16;
#define LDKV(D, kt, off) asm volatile("buffer_load_dwordx4 %0, %2, %3, %4 offen offset:" #off " sc1\n\tbuffer_load_dwordx4 %1, %2, %3, %4 offen offset:" #off "+1024 sc1" \
      : "=&v"(D[(kt) & 1][0]), "=&v"(D[(kt) & 1][1]) : "v"(kvl), "s"(wsr), "s"(kvb + (kt) * 4096) : "memory")
#define WAITN(n) asm volatile("s_waitcnt vmcnt(%0)" :: "i"(n) : "memory")
#define PINKV(D, kt) asm volatile("" : "+v"(D[(kt) & 1][0]), "+v"(D[(kt) & 1][1]))
#define WAITKV(D, kt, n) do { WAITN(n); SB(); PINKV(D, kt); } while (0)
#define WAITKV2(D, kt, n0, n1) do { if (wave < 4) WAITN(n0); else WAITN(n1); SB(); PINKV(D, kt); } while (0)
    asm volatile("s_nop 4");
    LDKV(kf, 0, 0); LDKV(vf, 0, 2048); LDKV(kf, 1, 0);
    float sh[4]; bool safe = true;
#pragma unroll
    for (int t = 0; t < 4; ++t) { sh[t] = 1.02f * __builtin_amdgcn_sqrtf(qn2[t] * kmax2); safe = safe && (sh[t] <= 48.f); }
    if (!__all(safe)) {
      BAR();
      float mx[4] = {-INFINITY, -INFINITY, -INFINITY, -INFINITY};
#pragma unroll 1
      for (int kt = 0; kt < 8; ++kt) {
        const u32x4 ka = ld128(wsr, lane * 16, kvb + kt * 4096), kb = ld128(wsr, lane * 16 + 1024, kvb + kt * 4096);
#pragma unroll
        for (int t = 0; t < 4; ++t) {
          f32x16 s = mfma32(ka, qf[t][0], (f32x16){}); s = mfma32(kb, qf[t][1], s);
#pragma unroll
          for (int r = 0; r < 16; ++r) mx[t] = fmaxf(mx[t], s[r]);
        }
      }
#pragma unroll
      for (int t = 0; t < 4; ++t) { const float m = xhalf_max(mx[t]); if (!hi) *(LAS float*)(lb + L_MX + (wave * 128 + t * 32 + l31) * 4) = m; }
      BAR();
#pragma unroll
      for (int t = 0; t < 4; ++t) {
        float m = -INFINITY;
#pragma unroll
        for (int w = 0; w < 8; ++w) m = fmaxf(m, lds32f(lb + L_MX + (w * 128 + t * 32 + l31) * 4));
        sh[t] = m;
      }
    }
    if (hi) {
#pragma unroll
      for (int t = 0; t < 4; ++t) {
        const float a = rbf(-sh[t]), r1 = -sh[t] - a, c = rbf(r1);
        qf[t][1][2] = pk2(a, c); qf[t][1][3] = pk2(r1 - c, 0.f);
      }
    }
    f32x16 o[4] = {(f32x16){}, (f32x16){}, (f32x16){}, (f32x16){}};
    WAITKV(kf, 0, 4);
    f32x16 sc = mfma32(kf[0][0], qf[0][0], (f32x16){}); sc = mfma32(kf[0][1], qf[0][1], sc);
    u32x4 p0, p1;
#define EXP2(v) __builtin_amdgcn_exp2f(v)
    float ec, ed;
#define GRP(r, PREV, MF, CUR) do { float ea = EXP2(sc[r]), eb = EXP2(sc[(r) + 1]); asm volatile("" : "+v"(ea), "+v"(eb)); PREV; SB(); MF; SB(); \
      ec = EXP2(sc[(r) + 2]); ed = EXP2(sc[(r) + 3]); asm volatile("" : "+v"(ec), "+v"(ed)); { u32 pa = pk2(ea, eb); asm volatile("" : "+v"(pa)); CUR = pa; } SB(); } while (0)
#define PKP(D) do { u32 pb = pk2(ec, ed); asm volatile("" : "+v"(pb)); D = pb; } while (0)
    u32 pwb = lb + wave * 8192 + lane * 16, lwb = lb + L_LP + wave * 512 + l31 * 4; asm volatile("" : "+v"(pwb), "+v"(lwb));
#define WRP(t) do { _Pragma("unroll") for (int g = 0; g < 2; ++g) *(LAS u32x4*)(pwb + ((t) * 2 + g) * 1024) = (u32x4){pk2(o[t][8 * g], o[t][8 * g + 1]), pk2(o[t][8 * g + 2], o[t][8 * g + 3]), pk2(o[t][8 * g + 4], o[t][8 * g + 5]), pk2(o[t][8 * g + 6], o[t][8 * g + 7])}; \
      if (hi) *(LAS float*)(lwb + (t) * 128) = o[t][12]; } while (0)
    const int w6 = min(wave, 6);
    const u32 wo_base = OFF_WOT + (u32)(7 * ocq + w6) * 16384, wg_base = OFF_WGU + (u32)(7 * gfj + w6) * 28672;
    auto nslot = [](int kt, int t, bool) constexpr { const int sl = 4 * (kt - 2) + t;
      return (t == 0 && kt < 7 ? 2 : 0) + (t == 2 && kt < 6 ? 2 : 0) + (kt >= 2 ? (sl == 0) + (sl < 16) + (sl >= 6 && sl < 18) : 0); };
    auto nafter = [&](int k0, int t0, int k1, int t1, bool young) constexpr { int n = -2; for (int i = 4 * k0 + t0; i <= 4 * k1 + t1; ++i) n += nslot(i >> 2, i & 3, young); return n; };
#pragma unroll
    for (int kt = 0; kt < 8; ++kt) {
      if (kt == 0) __builtin_amdgcn_s_setprio(3);
      if (kt == 2) __builtin_amdgcn_s_setprio(2);
      if (kt == 4) __builtin_amdgcn_s_setprio(1);
      if (kt == 6 && wave < 4) __builtin_amdgcn_s_setprio(0);
      if (kt == 7) __builtin_amdgcn_s_setprio(0);
#pragma unroll
      for (int t = 0; t < 4; ++t) {
        f32x16 sn;
        const bool more = t < 3 || kt < 7, first = t == 0 && kt == 0;
        const int kn = t < 3 ? (kt & 1) : ((kt + 1) & 1), tn = (t + 1) & 3;
        if (t == 3 && kt < 7) {
          if (kt == 0) WAITKV(kf, 1, nafter(0, 0, 0, 2, false) + 2);
          else if (kt == 1) WAITKV(kf, 2, nafter(0, 2, 1, 2, false));
          else WAITKV(kf, kt + 1, nafter(kt - 1, 2, kt, 2, false));
        }
        GRP(0, if (!first) PKP(p1[3]), if (more) sn = mfma32(kf[kn][0], qf[tn][0], (f32x16){}), p0[0]);
        GRP(4, PKP(p0[1]), if (more) sn = mfma32(kf[kn][1], qf[tn][1], sn), p0[2]);
        GRP(8, PKP(p0[3]), if (t > 0) o[t - 1] = mfma32(vf[kt & 1][1], p1, o[t - 1]); else if (kt > 0) o[3] = mfma32(vf[(kt + 1) & 1][1], p1, o[3]), p1[0]);
        if (t == 0 && kt < 7) LDKV(vf, kt + 1, 2048);
        if (t == 2 && kt < 6) LDKV(kf, kt + 2, 0);
        if (kt == 1 && t == 0 && wave == 3) {
#pragma nounroll
          for (int i = 0; i < SPIN_CAP; ++i) {
            const u32 a = poll32(ws + OFF_PREPF + lane * 128), c = poll32(ws + OFF_PREPF + (64 + lane) * 128), d = poll32(ws + OFF_PREPF + (128 + lane) * 128), e = poll32(ws + OFF_PREPF + (192 + lane) * 128);
            if (__all(a == tag && c == tag && d == tag && e == tag)) break;
            __builtin_amdgcn_s_sleep(8);
          }
          CFENCE();
          if (lane == 0) __hip_atomic_fetch_add((LAS u32*)(lb + L_SYNC + 40), 1u, __ATOMIC_RELAXED, __HIP_MEMORY_SCOPE_WORKGROUP);
        }
        if (t == 0) {
          if (kt == 2) STAMPL(1, "kt2");
          if (kt == 4) STAMPL(2, "kt4");
          if (kt == 6) STAMPL(4, "kt6");
        }
        if (kt >= 2) {
          const int sl = 4 * (kt - 2) + t;
          if (sl == 0) {
            lds_wait(lb + L_SYNC + 40, 9);
            dma4_touch(ws + wg_base + 12288, ((slot & 3) * 32 + l31) * 128 + hi * 64, lb + L_ST + w6 * 256);
          }
          if (sl < 10) wo_f[sl] = ld128(wsr, lane * 16, wo_base + sl * 1024);
          else if (sl < 16) wgu[sl - 10] = ld128(wsr, lane * 16, wg_base + (sl - 10) * 1024);
          if (sl >= 6 && sl < 18) dma16<true>(ws + (sl < 12 ? wo_base + (4 + sl) * 1024 : wg_base + (sl - 6) * 1024), lane * 16, lb + L_PARK + w6 * 12288 + (sl - 6) * 1024);
        }
        if (t == 0) {
          if (kt == 0) WAITKV(vf, 0, nafter(0, 0, 0, 0, false) + 4);
          else if (kt == 1) WAITKV(vf, 1, nafter(0, 0, 1, 0, false));
          else WAITKV(vf, kt, nafter(kt - 1, 0, kt, 0, false));
        }
        SB();
        GRP(12, PKP(p1[1]), o[t] = mfma32(vf[kt & 1][0], p0, o[t]), p1[2]);
        if (kt == 7 && t > 0) { WRP(t - 1); SB(); }
        if (more) sc = sn;
      }
    }
    PKP(p1[3]);
    o[3] = mfma32(vf[1][1], p1, o[3]);
    WRP(3);
    STAMPL(13, "attn done");
    BAR();
    const int qt = wave >> 1, sf = wave & 1;
    u32x4 pa[8]; float pl[8];
    {
      u32 base = lb + qt * 2048 + sf * 1024 + lane * 16, lbase = lb + L_LP + qt * 128 + l31 * 4;
      asm volatile("" : "+v"(base), "+v"(lbase));
#pragma unroll
      for (int w = 0; w < 8; ++w) { pa[w] = lds128(base + w * 8192); pl[w] = lds32f(lbase + w * 512); }
      SB();
    }
    float a8[8]; u32 sel0 = 0x00003F80u, sel1 = 0x3F800000u; asm volatile("" : "+v"(sel0), "+v"(sel1));
#pragma unroll
    for (int j = 0; j < 4; ++j) {
      a8[2 * j] = bflo(pa[0][j]); a8[2 * j + 1] = bfhi(pa[0][j]);
#pragma unroll
      for (int w = 1; w < 8; ++w) { a8[2 * j] = dot2(pa[w][j], sel0, a8[2 * j]); a8[2 * j + 1] = dot2(pa[w][j], sel1, a8[2 * j + 1]); }
    }
    const float l = ((pl[0] + pl[4]) + (pl[1] + pl[5])) + ((pl[2] + pl[6]) + (pl[3] + pl[7]));
    const float rl = __builtin_amdgcn_rcpf(l);
    const u32x4 of = {pk2(a8[0] * rl, a8[1] * rl), pk2(a8[2] * rl, a8[3] * rl), pk2(a8[4] * rl, a8[5] * rl), pk2(a8[6] * rl, a8[7] * rl)};
    st128(wsr, (l31 >> 4) * 1024 + ((l31 & 15) + 16 * (2 * sf + hi)) * 16, OFF_OB + (u32)((4 * ai + qt) * 16 + ah) * 2048, of);
    WAIT_VM(0);
    if (lane == 0) flag32(ws + OFF_OF + (((4 * ai + qt) * 16 + ah) * 2 + sf) * 128, tag);
    BAR();
    STAMPL(14, "O flagged");
  }

  u32 uvp[14];
  const int otok = 16 * ((lane >> 4) & 1) + (lane & 15), ofe = 112 * ocq + 16 * min(wave, 6) + 8 * hi;
  if (wave < 7) {
    xres = *(const u32x4*)(x + (size_t)(32 * ort + otok) * 448 + ofe);
    gaA = ld128(wsr, ofe * 4, OFF_MODG + 448 * 8); gaB = ld128(wsr, ofe * 4 + 16, OFF_MODG + 448 * 8);
#pragma unroll
    for (int k = 0; k < 14; ++k) uvp[k] = ld32(wsr, lane * 4, OFF_UV + (u32)(k * 112 + 7 * gfj + wave) * 256);
#pragma unroll
    for (int s = 12; s < 28; ++s) wgu[s] = ld128(wsr, lane * 16 + (s & 3) * 1024, OFF_WGU + (u32)((7 * gfj + wave) * 28 + (s & ~3)) * 1024);
#pragma unroll
    for (int j = 0; j < 6; ++j) wo_f[10 + j] = lds128(lb + L_PARK + wave * 12288 + j * 1024 + lane * 16);
#pragma unroll
    for (int j = 6; j < 12; ++j) wgu[j] = lds128(lb + L_PARK + wave * 12288 + j * 1024 + lane * 16);
    lds_wait(lb + L_SYNC + 16, 1);
  } else {
    wait_flags(ws + OFF_OF + (ort * 32 + l31) * 128, tag);
    lds_st(lb + L_SYNC + 16, 1u);
  }
  STAMPL(15, "O flags seen");
  {
    const char* osrc = ws + OFF_OB + (size_t)(ort * 16 + 2 * wave) * 2048;
    dma16x4(osrc, lane * 16, lb + 4 * wave * 1024);
  }
  __builtin_amdgcn_s_waitcnt(0x0F70);
  CFENCE();
  BAR();
  STAMPL(16, "O landed");
  if (wave < 7) {
    f32x4 y0 = {0.f, 0.f, 0.f, 0.f}, y1 = y0;
    u32x4 oa[8];
#pragma unroll
    for (int i = 0; i < 6; ++i) oa[i] = lds128(lb + i * 1024 + lane * 16);
#pragma unroll
    for (int h = 0; h < 16; ++h) {
      if (h < 13) { oa[(2 * h + 6) & 7] = lds128(lb + (2 * h + 6) * 1024 + lane * 16); oa[(2 * h + 7) & 7] = lds128(lb + (2 * h + 7) * 1024 + lane * 16); }
      SB(); y0 = mfma16(wo_f[h], oa[(2 * h) & 7], y0); y1 = mfma16(wo_f[h], oa[(2 * h + 1) & 7], y1); SB();
    }
    float yv[8];
#pragma unroll
    for (int r = 0; r < 4; ++r) { auto sw = __builtin_amdgcn_permlane16_swap(__float_as_uint(y0[r]), __float_as_uint(y1[r]), false, false); yv[r] = __uint_as_float(sw[0]); yv[4 + r] = __uint_as_float(sw[1]); }
    const u32 gw[4] = {gaA[0], gaA[2], gaB[0], gaB[2]};
    u32x4 x2p; float s1 = 0.f, s2 = 0.f;
#pragma unroll
    for (int j = 0; j < 4; ++j) {
      x2p[j] = pk2(fmaf(bflo(gw[j]), yv[2 * j], bflo(xres[j])), fmaf(bfhi(gw[j]), yv[2 * j + 1], bfhi(xres[j])));
      s1 = dot2(x2p[j], 0x3F803F80u, s1); s2 = dot2(x2p[j], x2p[j], s2);
    }
    const u32 fo = (u32)(ort * 28 + 7 * ocq + wave) * 1024;
    st128(wsr, (otok + 32 * hi) * 16, OFF_X2 + fo, x2p);
    s1 = xhalf_sum(s1); s2 = xhalf_sum(s2);
    *(LAS float*)(lb + L_ST + wave * 256 + (hi * 32 + otok) * 4) = hi ? s2 : s1;
    WAIT_VM(0);
  }
  BAR();
  if (wave == 7) {
    float t = 0.f;
#pragma unroll
    for (int w = 0; w < 7; ++w) t += lds32f(lb + L_ST + w * 256 + lane * 4);
    const u32 nv = __builtin_amdgcn_update_dpp(0u, __float_as_uint(t), 0xB1, 0xf, 0xf, false);
    st128(wsr, !(lane & 1) ? (u32)lane * 8 : 0x80000000u, OFF_X2G + (u32)(ort * 4 + ocq) * 512, (u32x4){__float_as_uint(t), tag, nv, tag});
  } else {
    float uvs = 0.f;
#pragma unroll
    for (int k = 0; k < 14; ++k) uvs += __uint_as_float(uvp[k]);
    *(LAS float*)(lb + L_UV + wave * 256 + lane * 4) = uvs;
  }
  STAMPL(17, "x2 flagged");

  const int dsl = slot & 15, dq = 2 * (slot >> 4) + dsl / 7, dc = dsl % 7;
  const bool dact = dsl < 14;
  u32x4 wdf[28], x2r, gbA, gbB;
  if (wave == 7) {
    u64 g[16]; const char* gp = ws + OFF_X2G + (16 * gi * 64 + lane) * 8;
#pragma nounroll
    for (int i = 0; i < SPIN_CAP; ++i) {
      bool ok = true;
#pragma unroll
      for (int p = 0; p < 16; ++p) g[p] = poll64(gp + p * 512);
#pragma unroll
      for (int p = 0; p < 16; ++p) ok = ok && (u32)(g[p] >> 32) == tag;
      if (__all(ok)) break;
      __builtin_amdgcn_s_sleep(1);
    }
    CFENCE();
#pragma unroll
    for (int p = 0; p < 16; ++p) *(LAS u32*)(lb + L_SP2 + p * 256 + lane * 4) = (u32)g[p];
  }
  BAR();
  STAMPL(18, "x2 flags seen");
  {
    const char* asrc = ws + OFF_X2 + (size_t)(4 * gi) * 28 * 1024;
#pragma unroll
    for (int n = 0; n < 4; ++n) { const int g = min(wave + 8 * n, 27); dma16x4(asrc + g * 4096, lane * 16, lb + g * 4096); }
  }
  WAIT_VM(12);
  BAR();
  STAMPL(19, "A0 landed");
  const int dm = wave >> 2, dn = (wave >> 1) & 1, dgp = wave & 1;
  const u32 wdo = OFF_WDT + (u32)(2 * dc * 112 + 14 * wave) * 1024;
  if (wave < 7) {
    u32 sb = lb + L_SP2 + l31 * 4, ub = lb + L_UV + wave * 256 + hi * 16; asm volatile("" : "+v"(sb), "+v"(ub));
    u32 ab = lb + lane * 16, ab2 = ab + 57344; asm volatile("" : "+v"(ab), "+v"(ab2));
    f32x16 acc[2]; u32x4 af[4]; f32x4 uv[4]; float st[8], rs = 0.f, nm = 0.f, hv[8];
#define EP_RD(T) do { _Pragma("unroll") for (int c = 0; c < 4; ++c) { st[c] = lds32f(sb + ((T) * 4 + c) * 256); st[4 + c] = lds32f(sb + ((T) * 4 + c) * 256 + 128); } } while (0)
#define EP_UV(g) do { uv[0] = lds128f(ub + 32 * (g)); uv[1] = lds128f(ub + 32 * (g) + 64); uv[2] = lds128f(ub + 32 * (g) + 128); uv[3] = lds128f(ub + 32 * (g) + 192); } while (0)
#define EP_ST() do { float m = ((st[0] + st[1]) + (st[2] + st[3])) * (1.f / 448.f); asm volatile("" : "+v"(m)); const float e2 = (((st[4] + st[5]) + st[6]) + st[7]) * (1.f / 448.f); \
      rs = __builtin_amdgcn_rsqf(fmaxf(e2 - m * m, 0.f) + 1e-5f); nm = -m * rs; } while (0)
    float gt, up, ez;
    constexpr float K1 = -2.f * 0.7978845608028654f * 1.4426950408889634f, K3 = K1 * 0.044715f;
#define EP_A(A, e) do { const float tg = fma1(nm, uv[0][(e) & 3], uv[2][(e) & 3]), tu = fma1(nm, uv[1][(e) & 3], uv[3][(e) & 3]); gt = fma1(A[e], rs, tg); up = fma1(A[8 + (e)], rs, tu); asm volatile("" : "+v"(gt), "+v"(up)); } while (0)
#define EP_B() do { ez = __builtin_amdgcn_exp2f(gt * fmaf(gt * gt, K3, K1)); up *= gt; asm volatile("" : "+v"(ez), "+v"(up)); } while (0)
#define EP_C(e) do { hv[e] = up * __builtin_amdgcn_rcpf(1.f + ez); asm volatile("" : "+v"(hv[e])); } while (0)
#define EP_EL(A, e) do { EP_A(A, e); EP_B(); EP_C(e); } while (0)
#define EP_OUT(T) st128(wsr, lane * 16, OFF_HB + (u32)((4 * gi + (T)) * 112 + 7 * gfj + wave) * 1024, (u32x4){pk2(hv[0], hv[1]), pk2(hv[2], hv[3]), pk2(hv[4], hv[5]), pk2(hv[6], hv[7])})
#pragma unroll
    for (int i = 0; i < 3; ++i) af[i] = lds128(ab + i * 1024);
#pragma unroll
    for (int t = 0; t < 4; ++t) {
#pragma unroll
      for (int s = 0; s < 28; ++s) {
        const int n = t * 28 + s;
        if (n == 25) { STAMPL(28, "gu n25"); WAIT_VM(0); BAR();  }
        if (n + 3 < 112) af[(n + 3) & 3] = (n + 3 < 56) ? lds128(ab + (n + 3) * 1024) : lds128(ab2 + (n + 3 - 56) * 1024);
        SB();
        if (s == 0) acc[t & 1] = mfma32(wgu[s], af[n & 3], (f32x16){}); else acc[t & 1] = mfma32(wgu[s], af[n & 3], acc[t & 1]);
        SB();
        if (t > 0) {
          if (s == 0) { EP_RD(t - 1); EP_UV(0); }
          if (s == 2) EP_ST();
          if (s >= 3 && s < 27) { if (s % 3 == 0) EP_A(acc[(t - 1) & 1], s / 3 - 1); else if (s % 3 == 1) EP_B(); else EP_C(s / 3 - 1); }
          if (s == 13) EP_UV(1);
          if (s == 27) EP_OUT(t - 1);
          if (s == 27 && t == 1) STAMPL(20, "gu tile1");
          if (s == 27 && t == 2) STAMPL(21, "gu tile2");
          if (s == 27 && t == 3) STAMPL(22, "gu tile3");
          if (s % 4 == 1 && s < 24) { const int f = 6 * (t - 1) + s / 4; wdf[f] = ld128(wsr, lane * 16, wdo + (u32)((f & 1) * 112 + (f >> 1)) * 1024); }
          SB();
        }
      }
    }
    asm volatile("s_nop 11" : "+v"(acc[1]));
    EP_RD(3); EP_UV(0); EP_ST();
    {
      f32x4 uw[4];
      uw[0] = lds128f(ub + 32); uw[1] = lds128f(ub + 96); uw[2] = lds128f(ub + 160); uw[3] = lds128f(ub + 224);
      float g8[8], u8[8];
#pragma unroll
      for (int e = 0; e < 8; ++e) {
        const f32x4* W = e < 4 ? uv : uw;
        g8[e] = fma1(acc[1][e], rs, fma1(nm, W[0][e & 3], W[2][e & 3])); u8[e] = fma1(acc[1][8 + e], rs, fma1(nm, W[1][e & 3], W[3][e & 3]));
      }
#pragma unroll
      for (int e = 0; e < 8; ++e) hv[e] = (g8[e] * u8[e]) * __builtin_amdgcn_rcpf(1.f + __builtin_amdgcn_exp2f(g8[e] * fmaf(g8[e] * g8[e], K3, K1)));
    }
    EP_OUT(3);
    WAIT_VM(0);
    if (lane == 0) flag32(ws + OFF_HF + ((gi * 16 + gfj) * 8 + wave) * 128, tag);
  } else {
    WAIT_VM(0); BAR();
#pragma unroll
    for (int f = 0; f < 18; ++f) wdf[f] = ld128(wsr, lane * 16, wdo + (u32)((f & 1) * 112 + (f >> 1)) * 1024);
  }
  STAMPL(23, "h flagged");
  if (dact) {
    BAR();
#pragma unroll
    for (int f = 18; f < 28; ++f) wdf[f] = ld128(wsr, lane * 16, wdo + (u32)((f & 1) * 112 + (f >> 1)) * 1024);
    const int drt = 8 * xg + 2 * dq, dcol = 64 * dc + 32 * dn + 16 * dgp + 8 * hi;
    gbA = ld128(wsr, dcol * 4, OFF_MODG + 1120 * 8); gbB = ld128(wsr, dcol * 4 + 16, OFF_MODG + 1120 * 8);
    wait_flags(ws + OFF_HF + (((drt >> 2) * 16 + 2 * wave + ((lane >> 3) & 1)) * 8 + min(lane & 7, 6)) * 128, tag);
    STAMPL(24, "h flags seen");
    u32x4 hf[20];
#define LDH(i) hf[(i) % 20] = ld128(wsr, lane * 16 + (((i) % 14) & 3) * 1024, OFF_HB + (u32)((drt + (i) / 14) * 112 + 14 * wave + (((i) % 14) & ~3)) * 1024)
#pragma unroll
    for (int i = 0; i < 20; ++i) { LDH(i); SB(); }
    x2r = ld128(wsr, lane * 16, OFF_X2 + (u32)((drt + dm) * 28 + 4 * dc + 2 * dn + dgp) * 1024);
    f32x16 c0, c1;
    u32 pwb = lb + wave * 16384 + lane * 16; asm volatile("" : "+v"(pwb));
#pragma unroll
    for (int i = 0; i < 28; ++i) {
      const int m = i / 14, j = i % 14;
      SB();
      if (j == 0) { c0 = mfma32(wdf[0], hf[i % 20], (f32x16){}); c1 = mfma32(wdf[1], hf[i % 20], (f32x16){}); }
      else { c0 = mfma32(wdf[2 * j], hf[i % 20], c0); c1 = mfma32(wdf[2 * j + 1], hf[i % 20], c1); }
      SB();
      if (i < 8) LDH(i + 20);
      if (j == 13) {
#pragma unroll
        for (int g = 0; g < 4; ++g) {
          *(LAS f32x4*)(pwb + ((2 * m) * 4 + g) * 1024) = (f32x4){c0[4 * g], c0[4 * g + 1], c0[4 * g + 2], c0[4 * g + 3]};
          *(LAS f32x4*)(pwb + ((2 * m + 1) * 4 + g) * 1024) = (f32x4){c1[4 * g], c1[4 * g + 1], c1[4 * g + 2], c1[4 * g + 3]};
        }
      }
    }
    STAMPL(25, "down mfma done");
    BAR();
    f32x4 pa[8], pb[8];
    {
      u32 base = lb + (2 * dm + dn) * 4096 + dgp * 2048 + lane * 16, base2 = base + 65536;
      asm volatile("" : "+v"(base), "+v"(base2));
#pragma unroll
      for (int w = 0; w < 4; ++w) {
        pa[w] = lds128f(base + w * 16384); pb[w] = lds128f(base + w * 16384 + 1024);
        pa[4 + w] = lds128f(base2 + w * 16384); pb[4 + w] = lds128f(base2 + w * 16384 + 1024);
      }
      SB();
    }
    const f32x4 a0 = ((pa[0] + pa[4]) + (pa[1] + pa[5])) + ((pa[2] + pa[6]) + (pa[3] + pa[7])), a1 = ((pb[0] + pb[4]) + (pb[1] + pb[5])) + ((pb[2] + pb[6]) + (pb[3] + pb[7]));
    float yv[8];
#pragma unroll
    for (int r = 0; r < 4; ++r) { auto sw = __builtin_amdgcn_permlane32_swap(__float_as_uint(a0[r]), __float_as_uint(a1[r]), false, false); yv[r] = __uint_as_float(sw[0]); yv[4 + r] = __uint_as_float(sw[1]); }
    const u32 gw[4] = {gbA[0], gbA[2], gbB[0], gbB[2]};
    u32x4 ov;
#pragma unroll
    for (int j = 0; j < 4; ++j) ov[j] = pk2(fmaf(bflo(gw[j]), yv[2 * j], bflo(x2r[j])), fmaf(bfhi(gw[j]), yv[2 * j + 1], bfhi(x2r[j])));
    *(u32x4*)(out + (size_t)(32 * (drt + dm) + l31) * 448 + dcol) = ov;
    if (b == 0 && tid == 0) *(GAS u32*)ws = tag;
  }
  STAMPL(26, "end");
}

extern "C" void kernel_launch(void* const* d_in, const int* in_sizes, int n_in, void* d_out, int out_size, void* d_ws, size_t ws_size, hipStream_t stream) {
  fused_block<<<256, 512, 0, stream>>>((const u16*)d_in[0], (const u16*)d_in[1], (const u16*)d_in[2], (const u16*)d_in[4], (const u16*)d_in[5],
                                       (const u16*)d_in[6], (const u16*)d_in[7], (const u16*)d_in[8], (u16*)d_out, (char*)d_ws);
}
```

```cpp
#define PROBE 26
#define PROBE_MUL 0
#define PROBE_W 0
#include <hip/hip_runtime.h>
#include <stdint.h>

typedef unsigned short u16;
typedef unsigned u32;
typedef unsigned long long u64;
typedef __attribute__((ext_vector_type(2))) unsigned u32x2;
typedef __attribute__((ext_vector_type(4))) unsigned u32x4;
typedef __attribute__((ext_vector_type(2))) float f32x2;
typedef __attribute__((ext_vector_type(4))) float f32x4;
typedef __attribute__((ext_vector_type(16))) float f32x16;
typedef __attribute__((ext_vector_type(2))) __bf16 bf16x2;
typedef __attribute__((ext_vector_type(8))) __bf16 bf16x8;
typedef __attribute__((ext_vector_type(4))) short s16x4;
#define DEV __device__ __forceinline__
#define GAS __attribute__((address_space(1)))
#define LAS __attribute__((address_space(3)))
#define RLX_AGENT __ATOMIC_RELAXED, __HIP_MEMORY_SCOPE_AGENT

constexpr u32 OFF_MODG = 4096;
constexpr u32 OFF_X2G = 65536, OFF_UV = 13467648  , OFF_WQT = 524288, OFF_WOT = 1900544, OFF_WGU = 2359296, OFF_WDT = 5570560;
constexpr u32 OFF_KV = 7176192, OFF_OB = 11370496, OFF_X2 = 15302656, OFF_HB = 17137664, OFF_XH = 24477696;
constexpr u32 OFF_XF = 26312704, OFF_WQF = OFF_XF + 512 * 128, OFF_KVG = OFF_WQF + 1024 * 128, OFF_OF = OFF_KVG + 1024 * 128, OFF_HF = OFF_OF + 2048 * 128, OFF_PREPF = OFF_HF + 2048 * 128, WS_USED = OFF_PREPF + 256 * 128;
constexpr u32 OFF_STAMP = 29360128;
constexpr u32 L_STG = 90112  , L_Q = 131072, L_GAM = 139264, L_QN = 142848;
constexpr u32 L_PARK = 64512 + 1024  ;
constexpr u32 L_SP2 = 151552  , L_MODP = L_SP2, L_MX = L_SP2, L_LP = L_SP2, L_UV = 155648  , L_ST = 157696  , L_G2 = 159488  ;
constexpr u32 L_SYNC = 160000;
constexpr u32 LDS_BYTES = 163840;
constexpr int CODE_BYTES = 40000;
constexpr int SPIN_CAP = 1 << 15;
#define FIRST_POLL 280
#define KV_SLEEP 1
#define PREP_START 220
#define POLL_T 0
#define POLL_SLEEP 3

DEV float bflo(u32 w) { return __uint_as_float(w << 16); }
DEV float bfhi(u32 w) { return __uint_as_float(w & 0xffff0000u); }
DEV u32 pk2(float a, float b) { f32x2 v = {a, b}; return __builtin_bit_cast(u32, __builtin_convertvector(v, bf16x2)); }
DEV float rbf(float a) { return bflo(pk2(a, 0.f)); }
DEV float fma1(float a, float b, float c) { float r; asm("v_fma_f32 %0, %1, %2, %3" : "=v"(r) : "v"(a), "v"(b), "v"(c)); return r; }
DEV float dot2(u32 a, u32 b, float c) { return __builtin_amdgcn_fdot2_f32_bf16(__builtin_bit_cast(bf16x2, a), __builtin_bit_cast(bf16x2, b), c, false); }
DEV f32x16 mfma32(u32x4 a, u32x4 b, f32x16 c) { return __builtin_amdgcn_mfma_f32_32x32x16_bf16(__builtin_bit_cast(bf16x8, a), __builtin_bit_cast(bf16x8, b), c, 0, 0, 0); }
DEV f32x4 mfma16(u32x4 a, u32x4 b, f32x4 c) { return __builtin_amdgcn_mfma_f32_16x16x32_bf16(__builtin_bit_cast(bf16x8, a), __builtin_bit_cast(bf16x8, b), c, 0, 0, 0); }
DEV float xhalf_sum(float v) { auto r = __builtin_amdgcn_permlane32_swap(__float_as_uint(v), __float_as_uint(v), false, false); return __uint_as_float(r[0]) + __uint_as_float(r[1]); }
DEV float xhalf_max(float v) { auto r = __builtin_amdgcn_permlane32_swap(__float_as_uint(v), __float_as_uint(v), false, false); return fmaxf(__uint_as_float(r[0]), __uint_as_float(r[1])); }
#define DPPF(v, ctrl) __uint_as_float(__builtin_amdgcn_update_dpp(0u, __float_as_uint(v), ctrl, 0xf, 0xf, false))
DEV float wave_max(float v) {
  v = fmaxf(v, DPPF(v, 0x121)); v = fmaxf(v, DPPF(v, 0x122)); v = fmaxf(v, DPPF(v, 0x124)); v = fmaxf(v, DPPF(v, 0x128));
  auto r = __builtin_amdgcn_permlane16_swap(__float_as_uint(v), __float_as_uint(v), false, false);
  return xhalf_max(fmaxf(__uint_as_float(r[0]), __uint_as_float(r[1])));
}
#define WAIT_VM(n) asm volatile("s_waitcnt vmcnt(" #n ")" ::: "memory")
#define BAR() asm volatile("s_waitcnt lgkmcnt(0)\n\ts_barrier" ::: "memory")
#define SB() __builtin_amdgcn_sched_barrier(0)
#define CFENCE() asm volatile("" ::: "memory")
template <bool SC1> DEV void dma16(const void* sbase, u32 voff, u32 lds_dst) {
  u32 keep;
  if (SC1) asm volatile("s_mov_b32 %0, m0\n\ts_mov_b32 m0, %3\n\ts_nop 2\n\tglobal_load_lds_dwordx4 %1, %2 sc1\n\ts_mov_b32 m0, %0" : "=&s"(keep) : "v"(voff), "s"(sbase), "s"(lds_dst) : "memory");
  else asm volatile("s_mov_b32 %0, m0\n\ts_mov_b32 m0, %3\n\ts_nop 2\n\tglobal_load_lds_dwordx4 %1, %2\n\ts_mov_b32 m0, %0" : "=&s"(keep) : "v"(voff), "s"(sbase), "s"(lds_dst) : "memory");
}
DEV void dma16x4(const void* sbase, u32 voff, u32 lds_dst) {
  u32 keep;
  asm volatile("s_mov_b32 %0, m0\n\ts_mov_b32 m0, %3\n\ts_nop 2\n\tglobal_load_lds_dwordx4 %1, %2 sc1\n\tglobal_load_lds_dwordx4 %1, %2 offset:1024 sc1\n\t"
               "global_load_lds_dwordx4 %1, %2 offset:2048 sc1\n\tglobal_load_lds_dwordx4 %1, %2 offset:3072 sc1\n\ts_mov_b32 m0, %0" : "=&s"(keep) : "v"(voff), "s"(sbase), "s"(lds_dst) : "memory");
}
DEV void dma4_touch(const void* sbase, u32 voff, u32 lds_dst) {
  u32 keep;
  asm volatile("s_mov_b32 %0, m0\n\ts_mov_b32 m0, %3\n\ts_nop 2\n\tglobal_load_lds_dword %1, %2 sc1\n\ts_mov_b32 m0, %0" : "=&s"(keep) : "v"(voff), "s"(sbase), "s"(lds_dst) : "memory");
}
DEV u32x4 lds128(u32 a) { return *(const LAS u32x4*)a; }
DEV f32x4 lds128f(u32 a) { return *(const LAS f32x4*)a; }
DEV float lds32f(u32 a) { return *(const LAS float*)a; }
DEV u32x2 ldstr(u32 a) { return __builtin_bit_cast(u32x2, __builtin_amdgcn_ds_read_tr16_b64_v4i16((LAS s16x4*)a)); }
DEV u32x4 cat(u32x2 a, u32x2 b) { return (u32x4){a.x, a.y, b.x, b.y}; }
typedef __amdgpu_buffer_rsrc_t rsrc_t;
DEV u32x4 ld128(rsrc_t r, u32 voff, u32 soff) { return __builtin_amdgcn_raw_buffer_load_b128(r, voff, soff, 16); }
DEV u32 ld32(rsrc_t r, u32 voff, u32 soff) { return __builtin_amdgcn_raw_buffer_load_b32(r, voff, soff, 16); }
DEV void st128(rsrc_t r, u32 voff, u32 soff, u32x4 v) {
  __builtin_amdgcn_raw_buffer_store_b128(v, r, voff, soff, 16);
  asm volatile("s_nop 1" :: "v"(v) : "memory");
}
DEV void st32(rsrc_t r, u32 voff, u32 soff, u32 v) { __builtin_amdgcn_raw_buffer_store_b32(v, r, voff, soff, 16); }
DEV u32 poll32(const char* p) { return __hip_atomic_load((GAS u32*)p, RLX_AGENT); }
DEV u64 poll64(const char* p) { return __hip_atomic_load((GAS u64*)p, RLX_AGENT); }
DEV void flag32(char* p, u32 v) { __hip_atomic_store((GAS u32*)p, v, RLX_AGENT); }
DEV void flag64(char* p, u32 val, u32 tag) { __hip_atomic_store((GAS u64*)p, ((u64)tag << 32) | val, RLX_AGENT); }
DEV void wait_flags(const char* p, u32 tag) {
#pragma nounroll
  for (int i = 0; i < SPIN_CAP; ++i) { if (__all(poll32(p) == tag)) break; __builtin_amdgcn_s_sleep(POLL_SLEEP); }
  CFENCE();
}
DEV u32 wait_gran(const char* p, u32 tag) {
  u64 g = 0;
#pragma nounroll
  for (int i = 0; i < SPIN_CAP; ++i) { g = poll64(p); if (__all((u32)(g >> 32) == tag)) break; __builtin_amdgcn_s_sleep(POLL_SLEEP); }
  CFENCE();
  return (u32)g;
}
DEV u32 lds_ld(u32 a) { return __hip_atomic_load((LAS u32*)a, __ATOMIC_RELAXED, __HIP_MEMORY_SCOPE_WORKGROUP); }
DEV void lds_st(u32 a, u32 v) { __hip_atomic_store((LAS u32*)a, v, __ATOMIC_RELAXED, __HIP_MEMORY_SCOPE_WORKGROUP); }
DEV u32 lds_inc(u32 a) { return __builtin_amdgcn_readfirstlane(__hip_atomic_fetch_add((LAS u32*)a, 1u, __ATOMIC_RELAXED, __HIP_MEMORY_SCOPE_WORKGROUP)); }
DEV void lds_wait(u32 a, u32 want) {
#pragma nounroll
  for (int i = 0; i < (SPIN_CAP << 6); ++i) { if (__builtin_amdgcn_readfirstlane(lds_ld(a)) >= want) break; __builtin_amdgcn_s_sleep(1); }
  CFENCE();
}
#define WAIT_LGKM() asm volatile("s_waitcnt lgkmcnt(0)" ::: "memory")
#define STAMP(i) do {} while (0)
#undef STAMP
#define STAMP(i) do { if (((i) == 0 || (i) == PROBE) && wave == PROBE_W && lane == 0) __hip_atomic_store((GAS u64*)(ws + OFF_STAMP) + b * 2 + ((i) != 0), (u64)__builtin_amdgcn_s_memrealtime(), RLX_AGENT); } while (0)
__global__ void probe_spin(char* ws, int mul) {
  const u64* p = (const u64*)(ws + OFF_STAMP);
  u64 a = ~0ull, c = 0;
  for (int i = 0; i < 4; ++i) { const u64 a2 = p[(threadIdx.x + 64 * i) * 2], c2 = p[(threadIdx.x + 64 * i) * 2 + 1]; a = a2 < a ? a2 : a; if (c2 - a2 < 100000ull) c = c2 > c ? c2 : c; }
  for (int o = 32; o; o >>= 1) { const u64 a2 = __shfl_xor(a, o), c2 = __shfl_xor(c, o); a = a2 < a ? a2 : a; c = c2 > c ? c2 : c; }
  u64 d = (c - a) * mul; if (d > 20000) d = 20000;
  const u64 t0 = __builtin_amdgcn_s_memrealtime();
  while (__builtin_amdgcn_s_memrealtime() - t0 < d) __builtin_amdgcn_s_sleep(1);
}
#define STAMPL(i, s) STAMP(i)

__global__ __launch_bounds__(512, 2) void fused_block(const u16* __restrict__ x, const u16* __restrict__ wqkv, const u16* __restrict__ wo,
                                                      const u16* __restrict__ wg, const u16* __restrict__ wu, const u16* __restrict__ wd,
                                                      const u16* __restrict__ cvec, const u16* __restrict__ wmod, u16* __restrict__ out, char* ws) {
  __shared__ __attribute__((aligned(16))) char smem[LDS_BYTES];
  const int b = blockIdx.x, tid = threadIdx.x, lane = tid & 63;
  const int wave = __builtin_amdgcn_readfirstlane(tid >> 6);
  const int l31 = lane & 31, hi = lane >> 5;
  const int xg = b & 7, slot = b >> 3;
  const int ai = 4 * (xg >> 1) + (slot & 3), ah = 8 * (xg & 1) + (slot >> 2);
  const u32 lb = (u32)(uintptr_t)(LAS char*)smem;
  const rsrc_t wsr = __builtin_amdgcn_make_buffer_rsrc(ws, 0, WS_USED, 0x00020000);
  STAMP(0);
  {
    u64 pc; asm volatile("s_getpc_b64 %0" : "=s"(pc));
    if (wave == 0) {
      const u32 off = min((u32)(slot * 1280 + min(lane, 9) * 128), (u32)CODE_BYTES);
      u32 keep;
      asm volatile("s_mov_b32 %0, m0\n\ts_mov_b32 m0, %3\n\ts_nop 2\n\tglobal_load_lds_dword %1, %2\n\ts_mov_b32 m0, %0" : "=&s"(keep) : "v"(off), "s"(pc), "s"(lb + L_ST) : "memory");
    }
  }
  asm volatile("" :: "s"(x), "s"(wqkv), "s"(wo), "s"(wg), "s"(wu), "s"(wd), "s"(cvec), "s"(wmod), "s"(out), "s"(ws) : "memory");
  SB();
  if (wave == 1) {
    const u32 off = min(lane, 12) << 21;
    u32 keep;
    asm volatile("s_mov_b32 %0, m0\n\ts_mov_b32 m0, %3\n\ts_nop 2\n\tglobal_load_lds_dword %1, %2\n\ts_mov_b32 m0, %0" : "=&s"(keep) : "v"(off), "s"(ws), "s"(lb + L_ST + 256) : "memory");
  }
  if (wave == 2 && lane < 16) lds_st(lb + L_SYNC + lane * 4, 0u);
  const u64 t_entry = __builtin_amdgcn_s_memrealtime();
  const u32 epoch_v = poll32(ws);
  u32 tag;

  f32x16 accQ, accK, accV;
  const int tq4 = (lane & 15) >> 2, tp4 = lane & 3, grp = lane >> 4;
  if (b < 42) {
    u32x4 wv[7];
    const u32 cv = cvec[56 * wave + min(lane, 55)];
#pragma unroll
    for (int n = 0; n < 7; ++n) wv[n] = *(const u32x4*)((const char*)wmod + (size_t)(56 * wave + 8 * n + (lane >> 3)) * 5376 + 128 * b + 16 * (lane & 7));
    BAR();
    const float cf = bflo(cv);
    const float sl = rbf(cf * __builtin_amdgcn_rcpf(1.f + __builtin_amdgcn_exp2f(-1.4426950408889634f * cf)));
    float acc[8] = {0.f, 0.f, 0.f, 0.f, 0.f, 0.f, 0.f, 0.f};
#pragma unroll
    for (int n = 0; n < 7; ++n) {
      const float sn = __uint_as_float(__builtin_amdgcn_ds_bpermute((8 * n + (lane >> 3)) * 4, __float_as_uint(sl)));
#pragma unroll
      for (int j = 0; j < 4; ++j) { acc[2 * j] = fmaf(sn, bflo(wv[n][j]), acc[2 * j]); acc[2 * j + 1] = fmaf(sn, bfhi(wv[n][j]), acc[2 * j + 1]); }
    }

#pragma unroll
    for (int j = 0; j < 8; ++j) acc[j] += DPPF(acc[j], 0x128);
#pragma unroll
    for (int j = 0; j < 4; ++j) { auto r = __builtin_amdgcn_permlane16_swap(__float_as_uint(acc[j]), __float_as_uint(acc[j + 4]), false, false); acc[j] = __uint_as_float(r[0]) + __uint_as_float(r[1]); }
#pragma unroll
    for (int j = 0; j < 2; ++j) { auto r = __builtin_amdgcn_permlane32_swap(__float_as_uint(acc[j]), __float_as_uint(acc[j + 2]), false, false); acc[j] = __uint_as_float(r[0]) + __uint_as_float(r[1]); }
    if (!(lane & 8)) *(LAS f32x2*)(lb + L_MODP + (wave * 64 + 8 * (lane & 7) + 2 * hi + 4 * (grp & 1)) * 4) = (f32x2){acc[0], acc[1]};
    WAIT_LGKM();
    u32 arrived = 0;
    if (lane == 0) arrived = __hip_atomic_fetch_add((LAS u32*)(lb + L_SYNC + 12), 1u, __ATOMIC_RELAXED, __HIP_MEMORY_SCOPE_WORKGROUP);
    if (__builtin_amdgcn_readfirstlane(arrived) == 7) {
      CFENCE();
      f32x2 t = {0.f, 0.f};
#pragma unroll
      for (int p = 0; p < 8; ++p) t += *(const LAS f32x2*)(lb + L_MODP + (p * 64 + 2 * l31) * 4);
      const u32 pv = pk2(t[0], t[1]), nv = __builtin_amdgcn_update_dpp(0u, pv, 0xB1, 0xf, 0xf, false);
      st128(wsr, (lane < 32 && !(lane & 1)) ? (u32)(32 * b + lane) * 8 : 0x80000000u, OFF_MODG, (u32x4){pv, epoch_v + 1u, nv, epoch_v + 1u});
      WAIT_VM(0);
      lds_st(lb + L_SYNC + 32, 1u);
    }
  } else if (wave < 3 && 3 * (b - 42) + wave < 512) {
    const int xgp = 3 * (b - 42) + wave, row = 4 * xgp + (lane & 3), c0i = lane >> 2;
    u32x4 xr[4];
#pragma unroll
    for (int n = 0; n < 4; ++n) xr[n] = *(const u32x4*)((const char*)x + (size_t)row * 896 + min(c0i + 16 * n, 55) * 16);
    BAR();
    if (c0i >= 8) xr[3] = (u32x4){0u, 0u, 0u, 0u};
    float s1[2] = {0.f, 0.f}, s2[2] = {0.f, 0.f};
#pragma unroll
    for (int n = 0; n < 4; ++n)
#pragma unroll
      for (int j = 0; j < 4; ++j) { s1[j & 1] = dot2(xr[n][j], 0x3F803F80u, s1[j & 1]); s2[j & 1] = dot2(xr[n][j], xr[n][j], s2[j & 1]); }
    float t1 = s1[0] + s1[1], t2 = s2[0] + s2[1];
    t1 += DPPF(t1, 0x124); t2 += DPPF(t2, 0x124); t1 += DPPF(t1, 0x128); t2 += DPPF(t2, 0x128);
    { auto r = __builtin_amdgcn_permlane16_swap(__float_as_uint(t1), __float_as_uint(t2), false, false); t1 = __uint_as_float(r[0]) + __uint_as_float(r[1]); }
    t1 = xhalf_sum(t1);
    { auto r = __builtin_amdgcn_permlane16_swap(__float_as_uint(t1), __float_as_uint(t1), false, false); t1 = __uint_as_float(r[0]); t2 = __uint_as_float(r[1]); }
    const float mean = t1 * (1.f / 448.f), ex2 = t2 * (1.f / 448.f);
    const float rstd = __builtin_amdgcn_rsqf(fmaxf(ex2 - mean * mean, 0.f) + 1e-5f), c0 = -mean * rstd;
    asm volatile("" :: "v"(c0));

#pragma unroll
    for (int n = 0; n < 4; ++n) {
      u32x4 o;
#pragma unroll
      for (int j = 0; j < 4; ++j) asm("v_cvt_pk_f16_f32 %0, %1, %2" : "=v"(o[j]) : "v"(fmaf(bflo(xr[n][j]), rstd, c0)), "v"(fmaf(bfhi(xr[n][j]), rstd, c0)));
      const u32 c = c0i + 16 * n;
      st128(wsr, c < 56 ? (c >> 1) * 1024 + ((row & 31) + 32 * (c & 1)) * 16 + (u32)(row >> 5) * 28672 : 0x80000000u, OFF_XH, o);
    }
    WAIT_VM(0);
    if (lane == 0) flag32(ws + OFF_XF + xgp * 128, epoch_v + 1u);
  } else if ((wave == 4 || wave == 5) && b < 210) {
    const int u = b - 42, part = u / 56, rem = u - part * 56, hp = rem / 7, kg = rem - hp * 7, s0 = 4 * kg + 2 * (wave - 4);
    const char* src = (const char*)wqkv + (size_t)(16 * s0) * 2688 + part * 896 + hp * 112;
    const u32 stg = lb + L_Q + (wave - 4) * 4096;
#pragma unroll
    for (int i = 0; i < 4; ++i) { const u32 c = lane & 7; dma16<false>(src + i * 8 * 2688, (lane >> 3) * 2688 + 16 * (c == 7 ? 6 : c), stg + i * 1024); }
    BAR();
    WAIT_VM(0);

#pragma unroll
    for (int e = 0; e < 4; ++e) {
      const u32 a = stg + (16 * (e >> 1) + 8 * (grp >> 1) + tq4) * 128 + (e & 1) * 56 + (grp & 1) * 32 + tp4 * 8;
      u32x4 f = cat(ldstr(a), ldstr(a + 512));
      if (l31 >= 28) f = (u32x4){0u, 0u, 0u, 0u};
      st128(wsr, lane * 16, OFF_WQT + (u32)((2 * hp + (e & 1)) * 84 + 3 * (s0 + (e >> 1)) + part) * 1024, f);
    }
    WAIT_VM(0);
    if (lane < 2) flag32(ws + OFF_WQF + ((2 * hp + lane) * 64 + 3 * (s0 >> 1) + part) * 128, epoch_v + 1u);
  } else {
    BAR();
  }
  tag = __builtin_amdgcn_readfirstlane(epoch_v) + 1u;
  STAMPL(7, "unit done");
  const int cg = b / 7, kg = b - cg * 7;
  if (wave >= 6) {
#pragma nounroll
    for (int i = 0; i < 4096 && __builtin_amdgcn_s_memrealtime() - t_entry < PREP_START; ++i) __builtin_amdgcn_s_sleep(1);
#pragma unroll
    for (int ph = 0; ph < 2; ++ph) {
      const int pw = wave & 1, tw = 2 * ph + pw, p = 4 * b + tw;
      const u32 stg = lb + L_STG + tw * 6144;
      if (b < 196) {
#pragma unroll
        for (int i = 0; i < 2; ++i) {
          const u32 vo = (u32)(64 * kg + 32 * ph + 16 * i + (lane >> 2)) * 3584 + (64 * cg + 32 * pw) * 2 + 16 * (lane & 3);
          dma16<false>(wg, vo, stg + (2 * i) * 1024); dma16<false>(wu, vo, stg + (2 * i + 1) * 1024);
        }
      }
      if (p < 784) {
        const int ds_ = p / 7, np = p - ds_ * 7;
#pragma unroll
        for (int i = 0; i < 2; ++i) dma16<false>(wd, (u32)(16 * ds_ + (lane >> 2)) * 896 + np * 128 + i * 64 + 16 * (lane & 3), stg + (4 + i) * 1024);
      } else if (p < 1008) {
        const int q = p - 784, h = q / 14, nq = q - h * 14;
#pragma unroll
        for (int i = 0; i < 2; ++i) dma16<false>(wo, (u32)min(28 * h + 16 * i + (lane >> 2), 447) * 896 + nq * 64 + 16 * (lane & 3), stg + (4 + i) * 1024);
      }
    }
    STAMPL(6, "prep issued");
    WAIT_VM(0);
    if (lane == 0) __hip_atomic_fetch_add((LAS u32*)(lb + L_SYNC + 52), 1u, __ATOMIC_RELAXED, __HIP_MEMORY_SCOPE_WORKGROUP);
    STAMPL(5, "prep landed");
  } else if (wave >= 3) {
#pragma nounroll
    for (int i = 0; i < 4096 && __builtin_amdgcn_s_memrealtime() - t_entry < FIRST_POLL; ++i) __builtin_amdgcn_s_sleep(1);
  }

  if (wave < 4) {
    u32x4 xf[28], pf[6]; u32 pfo[6], uvo[2]; float uvv[2];
    const int pw = wave & 1, ph = (wave >> 1) & 1;
    auto tile_read = [&]() __attribute__((always_inline)) {
      const int p = 4 * b + wave;
      const u32 stg = lb + L_STG + wave * 6144;
      const bool isd = p < 784;
      const int ds_ = p / 7, np = p - ds_ * 7, q = p - 784, h = q / 14, nq = q - h * 14;
      lds_wait(lb + L_SYNC + 52, 2);
      const int pi = ((tp4 & 1) << 1) | (tp4 >> 1);
#pragma unroll
      for (int i = 0; i < 2; ++i)
#pragma unroll
        for (int e = 0; e < 2; ++e) {
          const u32 a = stg + (2 * i + (grp & 1)) * 1024 + (8 * (grp >> 1) + tq4) * 64 + e * 32 + pi * 8;
          pf[2 * i + e] = cat(ldstr(a), ldstr(a + 256));
          pfo[2 * i + e] = lane * 16 + (b < 196 ? OFF_WGU + (u32)((4 * cg + 2 * pw + e) * 28 + 4 * kg + 2 * ph + i) * 1024 : 0x80000000u);
        }
#pragma unroll
      for (int i = 0; i < 2; ++i) {
        const u32 a = isd ? stg + (4 + i) * 1024 + (8 * (grp >> 1) + tq4) * 64 + (grp & 1) * 32 + tp4 * 8
                          : stg + 4096 + (16 * (grp >> 1) + 4 * (grp & 1) + tq4) * 64 + i * 32 + tp4 * 8;
        u32x4 f = cat(ldstr(a), ldstr(a + (isd ? 256 : 512)));
        if (!isd && grp == 3) { f[2] = 0u; f[3] = 0u; }
        pf[4 + i] = f;
        pfo[4 + i] = lane * 16 + (isd ? OFF_WDT + (u32)((2 * np + i) * 112 + ds_) * 1024 : p < 1008 ? OFF_WOT + (u32)((2 * nq + i) * 16 + h) * 1024 : 0x80000000u);
        uvo[i] = lane * 4 + (b < 196 ? OFF_UV + (u32)((2 * kg + ph) * 112 + 4 * cg + 2 * pw + i) * 256 : 0x80000000u);
      }
      asm volatile("" : "+v"(pf[0]), "+v"(pf[1]), "+v"(pf[2]), "+v"(pf[3]), "+v"(pf[4]), "+v"(pf[5]));
    };
    if (wave == 3) { wait_flags(ws + OFF_XF + (32 * ai + l31) * 128, tag); tile_read(); lds_wait(lb + L_SYNC + 36, 1); lds_st(lb + L_SYNC, 1u); }
    else { tile_read(); lds_wait(lb + L_SYNC, 1); }
    STAMPL(3, "xhat go");
    constexpr int XPRE = 12;
#define LDX(s) xf[s] = ld128(wsr, lane * 16 + ((s) & 3) * 1024, OFF_XH + (u32)((4 * ai + wave) * 28 + ((s) & ~3)) * 1024)
#pragma unroll
    for (int s = 0; s < 4; ++s) { LDX(s); SB(); }
    {
      float uu[2] = {0.f, 0.f}, vv[2] = {0.f, 0.f};
#pragma unroll
      for (int i = 0; i < 2; ++i) {
        const u32x4 be = lds128(lb + L_G2 + (2 * ph + i) * 32 + hi * 16), ga = lds128(lb + L_G2 + 128 + (2 * ph + i) * 32 + hi * 16);
#pragma unroll
        for (int e = 0; e < 2; ++e)
#pragma unroll
          for (int j = 0; j < 4; ++j) {
            const u32 w = pf[2 * i + e][j];
            vv[e] = dot2(w, be[j], vv[e]);
            pf[2 * i + e][j] = pk2(bflo(w) * bflo(ga[j]), bfhi(w) * bfhi(ga[j]));
            uu[e] = dot2(pf[2 * i + e][j], 0x3F803F80u, uu[e]);
          }
      }
#pragma unroll
      for (int e = 0; e < 2; ++e) { const float us = xhalf_sum(uu[e]), vs = xhalf_sum(vv[e]); uvv[e] = hi ? vs : us; }
      asm volatile("" : "+v"(pf[0]), "+v"(pf[1]), "+v"(pf[2]), "+v"(pf[3]), "+v"(uvv[0]), "+v"(uvv[1]));
      STAMPL(10, "prep tiled");
    }
#pragma unroll
    for (int s = 4; s < XPRE; ++s) { LDX(s); SB(); }
    lds_wait(lb + L_SYNC + 4, 4);

    STAMPL(8, "W1 ready");
    accQ = (f32x16){}; accK = (f32x16){}; accV = (f32x16){};
    {
      u32 gb = lb + L_GAM + hi * 16; asm volatile("" : "+v"(gb));
      u32 wb = lb + lane * 16, wb2 = wb + 43008; asm volatile("" : "+v"(wb), "+v"(wb2));
      u32x4 wf[3][3], hf[2], gs[3], be[3]; float ht[8];
#define RDG(s) gs[(s) % 3] = lds128(gb + (s) * 32)
#define RDB(s) be[(s) % 3] = lds128(gb + 896 + (s) * 32)
#define RDW1(s, p) wf[(s) % 3][p] = lds128(((s) < 14 ? wb : wb2) + (3 * ((s) % 14) + (p)) * 1024)
#define RDW(s) do { RDW1(s, 0); RDW1(s, 1); RDW1(s, 2); } while (0)
#define DOTS(s, h) do { _Pragma("unroll") for (int j = 2 * (h); j < 2 * (h) + 2; ++j) { \
        asm volatile("v_fma_mix_f32 %0, %1, %2, %3 op_sel_hi:[1,1,1]" : "=v"(ht[2 * j]) : "v"(xf[s][j]), "v"(gs[(s) % 3][j]), "v"(be[(s) % 3][j])); \
        asm volatile("v_fma_mix_f32 %0, %1, %2, %3 op_sel:[1,1,1] op_sel_hi:[1,1,1]" : "=v"(ht[2 * j + 1]) : "v"(xf[s][j]), "v"(gs[(s) % 3][j]), "v"(be[(s) % 3][j])); } } while (0)
#define CVTS(s) do { hf[(s) & 1] = (u32x4){pk2(ht[0], ht[1]), pk2(ht[2], ht[3]), pk2(ht[4], ht[5]), pk2(ht[6], ht[7])}; asm volatile("" : "+v"(hf[(s) & 1])); } while (0)
      RDG(0); RDB(0); RDW(0); RDG(1); RDB(1); RDW(1); RDG(2); RDB(2); RDW(2); DOTS(0, 0); DOTS(0, 1); CVTS(0); SB();
#pragma unroll
      for (int s = 0; s < 28; ++s) {
        if (s == 1) lds_wait(lb + L_SYNC + 8, 3);
        if (s == 13) lds_wait(lb + L_SYNC + 24, 3);
        SB(); accQ = mfma32(wf[s % 3][0], hf[s & 1], accQ); SB();
        if (s < 25) RDG(s + 3);
        if (s < 25) RDW1(s + 3, 0);
        if (s < 27) DOTS(s + 1, 0);
        SB(); accK = mfma32(wf[s % 3][1], hf[s & 1], accK); SB();
        if (s < 25) RDB(s + 3);
        if (s < 25) RDW1(s + 3, 1);
        if (s < 27) DOTS(s + 1, 1);
        SB(); accV = mfma32(hf[s & 1], wf[s % 3][2], accV); SB();
        if (s < 25) RDW1(s + 3, 2);
        if (s + XPRE < 28) LDX(s + XPRE);
        else if (s < 22) st128(wsr, pfo[s - 16], 0, pf[s - 16]);
        else if (s < 24) st32(wsr, uvo[s - 22], 0, __float_as_uint(uvv[s - 22]));
        if (s < 27) CVTS(s + 1);
        SB();
      }
    }
    STAMPL(5, "qkv mfma done");
    const float unscale = __uint_as_float(__builtin_amdgcn_readfirstlane(lds_ld(lb + L_SYNC + 28)));
    if (unscale != 1.f) { accQ *= unscale; accK *= unscale; accV *= unscale; }
    u32x4 k0, k1, v0, v1, q0, q1;
#pragma unroll
    for (int j = 0; j < 4; ++j) {
      k0[j] = pk2(accK[2 * j], accK[2 * j + 1]); k1[j] = pk2(accK[8 + 2 * j], accK[9 + 2 * j]);
      v0[j] = pk2(accV[2 * j], accV[2 * j + 1]); v1[j] = pk2(accV[8 + 2 * j], accV[9 + 2 * j]);
    }
    if (hi) { k1[2] = 0x3F803F80u; k1[3] = 0x00003F80u; }
    if (l31 == 28) { v0 = (u32x4){0x3F803F80u, 0x3F803F80u, 0x3F803F80u, 0x3F803F80u}; v1 = v0; }
    const u32 kvo = OFF_KV + (u32)(ah * 64 + ai * 4 + wave) * 4096;
    st128(wsr, lane * 16, kvo, k0); st128(wsr, lane * 16 + 1024, kvo, k1); st128(wsr, lane * 16 + 2048, kvo, v0); st128(wsr, lane * 16 + 3072, kvo, v1);
    SB();
    float kn = 0.f, qn = 0.f;
    constexpr float QS = 0.18898223650461362f * 1.4426950408889634f;
#pragma unroll
    for (int r = 0; r < 16; ++r) { kn = fmaf(accK[r], accK[r], kn); accQ[r] *= QS; qn = fmaf(accQ[r], accQ[r], qn); }
#pragma unroll
    for (int j = 0; j < 4; ++j) { q0[j] = pk2(accQ[2 * j], accQ[2 * j + 1]); q1[j] = pk2(accQ[8 + 2 * j], accQ[9 + 2 * j]); }
    *(LAS u32x4*)(lb + L_Q + wave * 2048 + lane * 16) = q0; *(LAS u32x4*)(lb + L_Q + wave * 2048 + 1024 + lane * 16) = q1;
    qn = xhalf_sum(qn); kn = wave_max(xhalf_sum(kn));
    if (!hi) *(LAS float*)(lb + L_QN + (wave * 32 + l31) * 4) = qn;
    WAIT_VM(0);
    if (lane == 0) flag64(ws + OFF_KVG + (ah * 64 + ai * 4 + wave) * 128, __float_as_uint(kn), tag);
    if (b < 42) lds_wait(lb + L_SYNC + 32, 1);
    if (lane == 0 && __hip_atomic_fetch_add((LAS u32*)(lb + L_SYNC + 44), 1u, __ATOMIC_RELAXED, __HIP_MEMORY_SCOPE_WORKGROUP) == 3u) flag32(ws + OFF_PREPF + b * 128, tag);
    STAMPL(6, "kv flagged");
  } else {
    if (wave == 4) {
      u64 gv[8];
      __builtin_amdgcn_s_setprio(3);
#pragma nounroll
      for (int i = 0; i < SPIN_CAP; ++i) {
        bool ok = true;
#pragma unroll
        for (int n = 0; n < 7; ++n) gv[n] = poll64(ws + OFF_MODG + (64 * n + lane) * 8);
        gv[7] = poll64(ws + OFF_MODG + (672 + 224 * hi + 32 * kg + l31) * 8);
#pragma unroll
        for (int n = 0; n < 8; ++n) ok = ok && (u32)(gv[n] >> 32) == tag;
        if (__all(ok)) break;
        __builtin_amdgcn_s_sleep(POLL_SLEEP);
      }
      CFENCE();
      __builtin_amdgcn_s_setprio(0);
      *(LAS u32*)(lb + L_G2 + lane * 4) = hi ? pk2(1.f + bflo((u32)gv[7]), 1.f + bfhi((u32)gv[7])) : (u32)gv[7];
      WAIT_LGKM();
      lds_st(lb + L_SYNC + 36, 1u);

      float lo[7], hi2[7], mxv = 0.f;
#pragma unroll
      for (int n = 0; n < 7; ++n) {
        u32 v = (u32)gv[n];
        if (64 * n + lane >= 224) v = pk2(1.f + bflo(v), 1.f + bfhi(v));
        lo[n] = bflo(v); hi2[n] = bfhi(v); mxv = fmaxf(mxv, fmaxf(fabsf(lo[n]), fabsf(hi2[n])));
      }
      const u32 ex = max((int)(__float_as_uint(wave_max(mxv)) >> 23) - (127 + 14), 0);
      const float scl = __uint_as_float((127u - min(ex, 114u)) << 23);
#pragma unroll
      for (int n = 0; n < 7; ++n) {
        u32 o; asm("v_cvt_pk_f16_f32 %0, %1, %2" : "=v"(o) : "v"(lo[n] * scl), "v"(hi2[n] * scl));
        const int g = 64 * n + lane;
        *(LAS u32*)(lb + L_GAM + (g >= 224 ? g - 224 : g + 224) * 4) = o;
      }
      lds_st(lb + L_SYNC + 28, (127u + min(ex, 114u)) << 23);
      WAIT_LGKM();
      if (lane == 0) __hip_atomic_fetch_add((LAS u32*)(lb + L_SYNC + 4), 1u, __ATOMIC_RELAXED, __HIP_MEMORY_SCOPE_WORKGROUP);
    } else {
      if (wave == 5) { wait_flags(ws + OFF_WQF + (ah * 64 + min(lane, 41)) * 128, tag); lds_wait(lb + L_SYNC + 36, 1); lds_st(lb + L_SYNC + 48, 1u); }
      else lds_wait(lb + L_SYNC + 48, 1);

      const char* wsrc = ws + OFF_WQT + (size_t)ah * 84 * 1024 + (wave - 5) * 4096;
#pragma unroll
      for (int n = 0; n < 7; ++n) dma16x4(wsrc + n * 12288, lane * 16, lb + (wave - 5) * 4096 + n * 12288);
    }
    if (wave >= 5) {
      WAIT_VM(24);
      if (lane == 0) __hip_atomic_fetch_add((LAS u32*)(lb + L_SYNC + 4), 1u, __ATOMIC_RELAXED, __HIP_MEMORY_SCOPE_WORKGROUP);
      WAIT_VM(12);
      if (lane == 0) __hip_atomic_fetch_add((LAS u32*)(lb + L_SYNC + 8), 1u, __ATOMIC_RELAXED, __HIP_MEMORY_SCOPE_WORKGROUP);
      WAIT_VM(0);
      if (lane == 0) __hip_atomic_fetch_add((LAS u32*)(lb + L_SYNC + 24), 1u, __ATOMIC_RELAXED, __HIP_MEMORY_SCOPE_WORKGROUP);
      STAMPL(9, "W landed");
    }
  }
  BAR();
  STAMPL(11, "B3");

  const int ort = 8 * xg + (slot >> 2), ocq = slot & 3;
  const int gi = ai, gfj = ah;
  u32x4 wo_f[16], wgu[28], xres, gaA, gaB;
  {
    u32x4 qf[4][2];
#pragma unroll
    for (int t = 0; t < 4; ++t) { qf[t][0] = lds128(lb + L_Q + t * 2048 + lane * 16); qf[t][1] = lds128(lb + L_Q + t * 2048 + 1024 + lane * 16); }
    float qn2[4];
#pragma unroll
    for (int t = 0; t < 4; ++t) qn2[t] = lds32f(lb + L_QN + (t * 32 + l31) * 4);
    WAIT_LGKM();
    if (lane == 0) __hip_atomic_fetch_add((LAS u32*)(lb + L_SYNC + 40), 1u, __ATOMIC_RELAXED, __HIP_MEMORY_SCOPE_WORKGROUP);
    if (wave == 3) {
      __builtin_amdgcn_s_sleep(KV_SLEEP);
      const float km = wave_max(__uint_as_float(wait_gran(ws + OFF_KVG + (ah * 64 + lane) * 128, tag)));
      lds_st(lb + L_SYNC + 60, __float_as_uint(km));
      WAIT_LGKM();
      lds_st(lb + L_SYNC + 56, 1u);
    } else lds_wait(lb + L_SYNC + 56, 1);
    const float kmax2 = __uint_as_float(lds_ld(lb + L_SYNC + 60));
    STAMPL(12, "kv flags seen");
    const u32 kvb = OFF_KV + (u32)(ah * 64 + 8 * wave) * 4096;
    u32x4 kf[2][2], vf[2][2];
    const u32 kvl = lane * 16;
#define LDKV(D, kt, off) asm volatile("buffer_load_dwordx4 %0, %2, %3, %4 offen offset:" #off " sc1\n\tbuffer_load_dwordx4 %1, %2, %3, %4 offen offset:" #off "+1024 sc1" \
      : "=&v"(D[(kt) & 1][0]), "=&v"(D[(kt) & 1][1]) : "v"(kvl), "s"(wsr), "s"(kvb + (kt) * 4096) : "memory")
#define WAITN(n) asm volatile("s_waitcnt vmcnt(%0)" :: "i"(n) : "memory")
#define PINKV(D, kt) asm volatile("" : "+v"(D[(kt) & 1][0]), "+v"(D[(kt) & 1][1]))
#define WAITKV(D, kt, n) do { WAITN(n); SB(); PINKV(D, kt); } while (0)
#define WAITKV2(D, kt, n0, n1) do { if (wave < 4) WAITN(n0); else WAITN(n1); SB(); PINKV(D, kt); } while (0)
    asm volatile("s_nop 4");
    LDKV(kf, 0, 0); LDKV(vf, 0, 2048); LDKV(kf, 1, 0);
    float sh[4]; bool safe = true;
#pragma unroll
    for (int t = 0; t < 4; ++t) { sh[t] = 1.02f * __builtin_amdgcn_sqrtf(qn2[t] * kmax2); safe = safe && (sh[t] <= 48.f); }
    if (!__all(safe)) {
      BAR();
      float mx[4] = {-INFINITY, -INFINITY, -INFINITY, -INFINITY};
#pragma unroll 1
      for (int kt = 0; kt < 8; ++kt) {
        const u32x4 ka = ld128(wsr, lane * 16, kvb + kt * 4096), kb = ld128(wsr, lane * 16 + 1024, kvb + kt * 4096);
#pragma unroll
        for (int t = 0; t < 4; ++t) {
          f32x16 s = mfma32(ka, qf[t][0], (f32x16){}); s = mfma32(kb, qf[t][1], s);
#pragma unroll
          for (int r = 0; r < 16; ++r) mx[t] = fmaxf(mx[t], s[r]);
        }
      }
#pragma unroll
      for (int t = 0; t < 4; ++t) { const float m = xhalf_max(mx[t]); if (!hi) *(LAS float*)(lb + L_MX + (wave * 128 + t * 32 + l31) * 4) = m; }
      BAR();
#pragma unroll
      for (int t = 0; t < 4; ++t) {
        float m = -INFINITY;
#pragma unroll
        for (int w = 0; w < 8; ++w) m = fmaxf(m, lds32f(lb + L_MX + (w * 128 + t * 32 + l31) * 4));
        sh[t] = m;
      }
    }
    if (hi) {
#pragma unroll
      for (int t = 0; t < 4; ++t) {
        const float a = rbf(-sh[t]), r1 = -sh[t] - a, c = rbf(r1);
        qf[t][1][2] = pk2(a, c); qf[t][1][3] = pk2(r1 - c, 0.f);
      }
    }
    f32x16 o[4] = {(f32x16){}, (f32x16){}, (f32x16){}, (f32x16){}};
    WAITKV(kf, 0, 4);
    f32x16 sc = mfma32(kf[0][0], qf[0][0], (f32x16){}); sc = mfma32(kf[0][1], qf[0][1], sc);
    u32x4 p0, p1;
#define EXP2(v) __builtin_amdgcn_exp2f(v)
    float ec, ed;
#define GRP(r, PREV, MF, CUR) do { float ea = EXP2(sc[r]), eb = EXP2(sc[(r) + 1]); asm volatile("" : "+v"(ea), "+v"(eb)); PREV; SB(); MF; SB(); \
      ec = EXP2(sc[(r) + 2]); ed = EXP2(sc[(r) + 3]); asm volatile("" : "+v"(ec), "+v"(ed)); { u32 pa = pk2(ea, eb); asm volatile("" : "+v"(pa)); CUR = pa; } SB(); } while (0)
#define PKP(D) do { u32 pb = pk2(ec, ed); asm volatile("" : "+v"(pb)); D = pb; } while (0)
    u32 pwb = lb + wave * 8192 + lane * 16, lwb = lb + L_LP + wave * 512 + l31 * 4; asm volatile("" : "+v"(pwb), "+v"(lwb));
#define WRP(t) do { _Pragma("unroll") for (int g = 0; g < 2; ++g) *(LAS u32x4*)(pwb + ((t) * 2 + g) * 1024) = (u32x4){pk2(o[t][8 * g], o[t][8 * g + 1]), pk2(o[t][8 * g + 2], o[t][8 * g + 3]), pk2(o[t][8 * g + 4], o[t][8 * g + 5]), pk2(o[t][8 * g + 6], o[t][8 * g + 7])}; \
      if (hi) *(LAS float*)(lwb + (t) * 128) = o[t][12]; } while (0)
    const int w6 = min(wave, 6);
    const u32 wo_base = OFF_WOT + (u32)(7 * ocq + w6) * 16384, wg_base = OFF_WGU + (u32)(7 * gfj + w6) * 28672;
    auto nslot = [](int kt, int t, bool) constexpr { const int sl = 4 * (kt - 2) + t;
      return (t == 0 && kt < 7 ? 2 : 0) + (t == 2 && kt < 6 ? 2 : 0) + (kt >= 2 ? (sl == 0) + (sl < 16) + (sl >= 6 && sl < 18) : 0); };
    auto nafter = [&](int k0, int t0, int k1, int t1, bool young) constexpr { int n = -2; for (int i = 4 * k0 + t0; i <= 4 * k1 + t1; ++i) n += nslot(i >> 2, i & 3, young); return n; };
#pragma unroll
    for (int kt = 0; kt < 8; ++kt) {
      if (kt == 0) __builtin_amdgcn_s_setprio(3);
      if (kt == 2) __builtin_amdgcn_s_setprio(2);
      if (kt == 4) __builtin_amdgcn_s_setprio(1);
      if (kt == 6 && wave < 4) __builtin_amdgcn_s_setprio(0);
      if (kt == 7) __builtin_amdgcn_s_setprio(0);
#pragma unroll
      for (int t = 0; t < 4; ++t) {
        f32x16 sn;
        const bool more = t < 3 || kt < 7, first = t == 0 && kt == 0;
        const int kn = t < 3 ? (kt & 1) : ((kt + 1) & 1), tn = (t + 1) & 3;
        if (t == 3 && kt < 7) {
          if (kt == 0) WAITKV(kf, 1, nafter(0, 0, 0, 2, false) + 2);
          else if (kt == 1) WAITKV(kf, 2, nafter(0, 2, 1, 2, false));
          else WAITKV(kf, kt + 1, nafter(kt - 1, 2, kt, 2, false));
        }
        GRP(0, if (!first) PKP(p1[3]), if (more) sn = mfma32(kf[kn][0], qf[tn][0], (f32x16){}), p0[0]);
        GRP(4, PKP(p0[1]), if (more) sn = mfma32(kf[kn][1], qf[tn][1], sn), p0[2]);
        GRP(8, PKP(p0[3]), if (t > 0) o[t - 1] = mfma32(vf[kt & 1][1], p1, o[t - 1]); else if (kt > 0) o[3] = mfma32(vf[(kt + 1) & 1][1], p1, o[3]), p1[0]);
        if (t == 0 && kt < 7) LDKV(vf, kt + 1, 2048);
        if (t == 2 && kt < 6) LDKV(kf, kt + 2, 0);
        if (kt == 1 && t == 0 && wave == 3) {
#pragma nounroll
          for (int i = 0; i < SPIN_CAP; ++i) {
            const u32 a = poll32(ws + OFF_PREPF + lane * 128), c = poll32(ws + OFF_PREPF + (64 + lane) * 128), d = poll32(ws + OFF_PREPF + (128 + lane) * 128), e = poll32(ws + OFF_PREPF + (192 + lane) * 128);
            if (__all(a == tag && c == tag && d == tag && e == tag)) break;
            __builtin_amdgcn_s_sleep(8);
          }
          CFENCE();
          if (lane == 0) __hip_atomic_fetch_add((LAS u32*)(lb + L_SYNC + 40), 1u, __ATOMIC_RELAXED, __HIP_MEMORY_SCOPE_WORKGROUP);
        }
        if (t == 0) {
          if (kt == 2) STAMPL(1, "kt2");
          if (kt == 4) STAMPL(2, "kt4");
          if (kt == 6) STAMPL(4, "kt6");
        }
        if (kt >= 2) {
          const int sl = 4 * (kt - 2) + t;
          if (sl == 0) {
            lds_wait(lb + L_SYNC + 40, 9);
            dma4_touch(ws + wg_base + 12288, ((slot & 3) * 32 + l31) * 128 + hi * 64, lb + L_ST + w6 * 256);
          }
          if (sl < 10) wo_f[sl] = ld128(wsr, lane * 16, wo_base + sl * 1024);
          else if (sl < 16) wgu[sl - 10] = ld128(wsr, lane * 16, wg_base + (sl - 10) * 1024);
          if (sl >= 6 && sl < 18) dma16<true>(ws + (sl < 12 ? wo_base + (4 + sl) * 1024 : wg_base + (sl - 6) * 1024), lane * 16, lb + L_PARK + w6 * 12288 + (sl - 6) * 1024);
        }
        if (t == 0) {
          if (kt == 0) WAITKV(vf, 0, nafter(0, 0, 0, 0, false) + 4);
          else if (kt == 1) WAITKV(vf, 1, nafter(0, 0, 1, 0, false));
          else WAITKV(vf, kt, nafter(kt - 1, 0, kt, 0, false));
        }
        SB();
        GRP(12, PKP(p1[1]), o[t] = mfma32(vf[kt & 1][0], p0, o[t]), p1[2]);
        if (kt == 7 && t > 0) { WRP(t - 1); SB(); }
        if (more) sc = sn;
      }
    }
    PKP(p1[3]);
    o[3] = mfma32(vf[1][1], p1, o[3]);
    WRP(3);
    STAMPL(13, "attn done");
    BAR();
    const int qt = wave >> 1, sf = wave & 1;
    u32x4 pa[8]; float pl[8];
    {
      u32 base = lb + qt * 2048 + sf * 1024 + lane * 16, lbase = lb + L_LP + qt * 128 + l31 * 4;
      asm volatile("" : "+v"(base), "+v"(lbase));
#pragma unroll
      for (int w = 0; w < 8; ++w) { pa[w] = lds128(base + w * 8192); pl[w] = lds32f(lbase + w * 512); }
      SB();
    }
    float a8[8]; u32 sel0 = 0x00003F80u, sel1 = 0x3F800000u; asm volatile("" : "+v"(sel0), "+v"(sel1));
#pragma unroll
    for (int j = 0; j < 4; ++j) {
      a8[2 * j] = bflo(pa[0][j]); a8[2 * j + 1] = bfhi(pa[0][j]);
#pragma unroll
      for (int w = 1; w < 8; ++w) { a8[2 * j] = dot2(pa[w][j], sel0, a8[2 * j]); a8[2 * j + 1] = dot2(pa[w][j], sel1, a8[2 * j + 1]); }
    }
    const float l = ((pl[0] + pl[4]) + (pl[1] + pl[5])) + ((pl[2] + pl[6]) + (pl[3] + pl[7]));
    const float rl = __builtin_amdgcn_rcpf(l);
    const u32x4 of = {pk2(a8[0] * rl, a8[1] * rl), pk2(a8[2] * rl, a8[3] * rl), pk2(a8[4] * rl, a8[5] * rl), pk2(a8[6] * rl, a8[7] * rl)};
    st128(wsr, (l31 >> 4) * 1024 + ((l31 & 15) + 16 * (2 * sf + hi)) * 16, OFF_OB + (u32)((4 * ai + qt) * 16 + ah) * 2048, of);
    WAIT_VM(0);
    if (lane == 0) flag32(ws + OFF_OF + (((4 * ai + qt) * 16 + ah) * 2 + sf) * 128, tag);
    BAR();
    STAMPL(14, "O flagged");
  }

  u32 uvp[14];
  const int otok = 16 * ((lane >> 4) & 1) + (lane & 15), ofe = 112 * ocq + 16 * min(wave, 6) + 8 * hi;
  if (wave < 7) {
    xres = *(const u32x4*)(x + (size_t)(32 * ort + otok) * 448 + ofe);
    gaA = ld128(wsr, ofe * 4, OFF_MODG + 448 * 8); gaB = ld128(wsr, ofe * 4 + 16, OFF_MODG + 448 * 8);
#pragma unroll
    for (int k = 0; k < 14; ++k) uvp[k] = ld32(wsr, lane * 4, OFF_UV + (u32)(k * 112 + 7 * gfj + wave) * 256);
#pragma unroll
    for (int s = 12; s < 28; ++s) wgu[s] = ld128(wsr, lane * 16 + (s & 3) * 1024, OFF_WGU + (u32)((7 * gfj + wave) * 28 + (s & ~3)) * 1024);
#pragma unroll
    for (int j = 0; j < 6; ++j) wo_f[10 + j] = lds128(lb + L_PARK + wave * 12288 + j * 1024 + lane * 16);
#pragma unroll
    for (int j = 6; j < 12; ++j) wgu[j] = lds128(lb + L_PARK + wave * 12288 + j * 1024 + lane * 16);
    lds_wait(lb + L_SYNC + 16, 1);
  } else {
    wait_flags(ws + OFF_OF + (ort * 32 + l31) * 128, tag);
    lds_st(lb + L_SYNC + 16, 1u);
  }
  STAMPL(15, "O flags seen");
  {
    const char* osrc = ws + OFF_OB + (size_t)(ort * 16 + 2 * wave) * 2048;
    dma16x4(osrc, lane * 16, lb + 4 * wave * 1024);
  }
  __builtin_amdgcn_s_waitcnt(0x0F70);
  CFENCE();
  BAR();
  STAMPL(16, "O landed");
  if (wave < 7) {
    f32x4 y0 = {0.f, 0.f, 0.f, 0.f}, y1 = y0;
    u32x4 oa[8];
#pragma unroll
    for (int i = 0; i < 6; ++i) oa[i] = lds128(lb + i * 1024 + lane * 16);
#pragma unroll
    for (int h = 0; h < 16; ++h) {
      if (h < 13) { oa[(2 * h + 6) & 7] = lds128(lb + (2 * h + 6) * 1024 + lane * 16); oa[(2 * h + 7) & 7] = lds128(lb + (2 * h + 7) * 1024 + lane * 16); }
      SB(); y0 = mfma16(wo_f[h], oa[(2 * h) & 7], y0); y1 = mfma16(wo_f[h], oa[(2 * h + 1) & 7], y1); SB();
    }
    float yv[8];
#pragma unroll
    for (int r = 0; r < 4; ++r) { auto sw = __builtin_amdgcn_permlane16_swap(__float_as_uint(y0[r]), __float_as_uint(y1[r]), false, false); yv[r] = __uint_as_float(sw[0]); yv[4 + r] = __uint_as_float(sw[1]); }
    const u32 gw[4] = {gaA[0], gaA[2], gaB[0], gaB[2]};
    u32x4 x2p; float s1 = 0.f, s2 = 0.f;
#pragma unroll
    for (int j = 0; j < 4; ++j) {
      x2p[j] = pk2(fmaf(bflo(gw[j]), yv[2 * j], bflo(xres[j])), fmaf(bfhi(gw[j]), yv[2 * j + 1], bfhi(xres[j])));
      s1 = dot2(x2p[j], 0x3F803F80u, s1); s2 = dot2(x2p[j], x2p[j], s2);
    }
    const u32 fo = (u32)(ort * 28 + 7 * ocq + wave) * 1024;
    st128(wsr, (otok + 32 * hi) * 16, OFF_X2 + fo, x2p);
    s1 = xhalf_sum(s1); s2 = xhalf_sum(s2);
    *(LAS float*)(lb + L_ST + wave * 256 + (hi * 32 + otok) * 4) = hi ? s2 : s1;
    WAIT_VM(0);
  }
  BAR();
  if (wave == 7) {
    float t = 0.f;
#pragma unroll
    for (int w = 0; w < 7; ++w) t += lds32f(lb + L_ST + w * 256 + lane * 4);
    const u32 nv = __builtin_amdgcn_update_dpp(0u, __float_as_uint(t), 0xB1, 0xf, 0xf, false);
    st128(wsr, !(lane & 1) ? (u32)lane * 8 : 0x80000000u, OFF_X2G + (u32)(ort * 4 + ocq) * 512, (u32x4){__float_as_uint(t), tag, nv, tag});
  } else {
    float uvs = 0.f;
#pragma unroll
    for (int k = 0; k < 14; ++k) uvs += __uint_as_float(uvp[k]);
    *(LAS float*)(lb + L_UV + wave * 256 + lane * 4) = uvs;
  }
  STAMPL(17, "x2 flagged");

  const int dsl = slot & 15, dq = 2 * (slot >> 4) + dsl / 7, dc = dsl % 7;
  const bool dact = dsl < 14;
  u32x4 wdf[28], x2r, gbA, gbB;
  if (wave == 7) {
    u64 g[16]; const char* gp = ws + OFF_X2G + (16 * gi * 64 + lane) * 8;
#pragma nounroll
    for (int i = 0; i < SPIN_CAP; ++i) {
      bool ok = true;
#pragma unroll
      for (int p = 0; p < 16; ++p) g[p] = poll64(gp + p * 512);
#pragma unroll
      for (int p = 0; p < 16; ++p) ok = ok && (u32)(g[p] >> 32) == tag;
      if (__all(ok)) break;
      __builtin_amdgcn_s_sleep(1);
    }
    CFENCE();
#pragma unroll
    for (int p = 0; p < 16; ++p) *(LAS u32*)(lb + L_SP2 + p * 256 + lane * 4) = (u32)g[p];
  }
  BAR();
  STAMPL(18, "x2 flags seen");
  {
    const char* asrc = ws + OFF_X2 + (size_t)(4 * gi) * 28 * 1024;
#pragma unroll
    for (int n = 0; n < 4; ++n) { const int g = min(wave + 8 * n, 27); dma16x4(asrc + g * 4096, lane * 16, lb + g * 4096); }
  }
  WAIT_VM(12);
  BAR();
  STAMPL(19, "A0 landed");
  const int dm = wave >> 2, dn = (wave >> 1) & 1, dgp = wave & 1;
  const u32 wdo = OFF_WDT + (u32)(2 * dc * 112 + 14 * wave) * 1024;
  if (wave < 7) {
    u32 sb = lb + L_SP2 + l31 * 4, ub = lb + L_UV + wave * 256 + hi * 16; asm volatile("" : "+v"(sb), "+v"(ub));
    u32 ab = lb + lane * 16, ab2 = ab + 57344; asm volatile("" : "+v"(ab), "+v"(ab2));
    f32x16 acc[2]; u32x4 af[4]; f32x4 uv[4]; float st[8], rs = 0.f, nm = 0.f, hv[8];
#define EP_RD(T) do { _Pragma("unroll") for (int c = 0; c < 4; ++c) { st[c] = lds32f(sb + ((T) * 4 + c) * 256); st[4 + c] = lds32f(sb + ((T) * 4 + c) * 256 + 128); } } while (0)
#define EP_UV(g) do { uv[0] = lds128f(ub + 32 * (g)); uv[1] = lds128f(ub + 32 * (g) + 64); uv[2] = lds128f(ub + 32 * (g) + 128); uv[3] = lds128f(ub + 32 * (g) + 192); } while (0)
#define EP_ST() do { float m = ((st[0] + st[1]) + (st[2] + st[3])) * (1.f / 448.f); asm volatile("" : "+v"(m)); const float e2 = (((st[4] + st[5]) + st[6]) + st[7]) * (1.f / 448.f); \
      rs = __builtin_amdgcn_rsqf(fmaxf(e2 - m * m, 0.f) + 1e-5f); nm = -m * rs; } while (0)
    float gt, up, ez;
    constexpr float K1 = -2.f * 0.7978845608028654f * 1.4426950408889634f, K3 = K1 * 0.044715f;
#define EP_A(A, e) do { const float tg = fma1(nm, uv[0][(e) & 3], uv[2][(e) & 3]), tu = fma1(nm, uv[1][(e) & 3], uv[3][(e) & 3]); gt = fma1(A[e], rs, tg); up = fma1(A[8 + (e)], rs, tu); asm volatile("" : "+v"(gt), "+v"(up)); } while (0)
#define EP_B() do { ez = __builtin_amdgcn_exp2f(gt * fmaf(gt * gt, K3, K1)); up *= gt; asm volatile("" : "+v"(ez), "+v"(up)); } while (0)
#define EP_C(e) do { hv[e] = up * __builtin_amdgcn_rcpf(1.f + ez); asm volatile("" : "+v"(hv[e])); } while (0)
#define EP_EL(A, e) do { EP_A(A, e); EP_B(); EP_C(e); } while (0)
#define EP_OUT(T) st128(wsr, lane * 16, OFF_HB + (u32)((4 * gi + (T)) * 112 + 7 * gfj + wave) * 1024, (u32x4){pk2(hv[0], hv[1]), pk2(hv[2], hv[3]), pk2(hv[4], hv[5]), pk2(hv[6], hv[7])})
#pragma unroll
    for (int i = 0; i < 3; ++i) af[i] = lds128(ab + i * 1024);
#pragma unroll
    for (int t = 0; t < 4; ++t) {
#pragma unroll
      for (int s = 0; s < 28; ++s) {
        const int n = t * 28 + s;
        if (n == 25) { STAMPL(28, "gu n25"); WAIT_VM(0); BAR();  }
        if (n + 3 < 112) af[(n + 3) & 3] = (n + 3 < 56) ? lds128(ab + (n + 3) * 1024) : lds128(ab2 + (n + 3 - 56) * 1024);
        SB();
        if (s == 0) acc[t & 1] = mfma32(wgu[s], af[n & 3], (f32x16){}); else acc[t & 1] = mfma32(wgu[s], af[n & 3], acc[t & 1]);
        SB();
        if (t > 0) {
          if (s == 0) { EP_RD(t - 1); EP_UV(0); }
          if (s == 2) EP_ST();
          if (s >= 3 && s < 27) { if (s % 3 == 0) EP_A(acc[(t - 1) & 1], s / 3 - 1); else if (s % 3 == 1) EP_B(); else EP_C(s / 3 - 1); }
          if (s == 13) EP_UV(1);
          if (s == 27) EP_OUT(t - 1);
          if (s == 27 && t == 1) STAMPL(20, "gu tile1");
          if (s == 27 && t == 2) STAMPL(21, "gu tile2");
          if (s == 27 && t == 3) STAMPL(22, "gu tile3");
          if (s % 4 == 1 && s < 24) { const int f = 6 * (t - 1) + s / 4; wdf[f] = ld128(wsr, lane * 16, wdo + (u32)((f & 1) * 112 + (f >> 1)) * 1024); }
          SB();
        }
      }
    }
    asm volatile("s_nop 11" : "+v"(acc[1]));
    EP_RD(3); EP_UV(0); EP_ST();
    {
      f32x4 uw[4];
      uw[0] = lds128f(ub + 32); uw[1] = lds128f(ub + 96); uw[2] = lds128f(ub + 160); uw[3] = lds128f(ub + 224);
      float g8[8], u8[8];
#pragma unroll
      for (int e = 0; e < 8; ++e) {
        const f32x4* W = e < 4 ? uv : uw;
        g8[e] = fma1(acc[1][e], rs, fma1(nm, W[0][e & 3], W[2][e & 3])); u8[e] = fma1(acc[1][8 + e], rs, fma1(nm, W[1][e & 3], W[3][e & 3]));
      }
#pragma unroll
      for (int e = 0; e < 8; ++e) hv[e] = (g8[e] * u8[e]) * __builtin_amdgcn_rcpf(1.f + __builtin_amdgcn_exp2f(g8[e] * fmaf(g8[e] * g8[e], K3, K1)));
    }
    EP_OUT(3);
    WAIT_VM(0);
    if (lane == 0) flag32(ws + OFF_HF + ((gi * 16 + gfj) * 8 + wave) * 128, tag);
  } else {
    WAIT_VM(0); BAR();
#pragma unroll
    for (int f = 0; f < 18; ++f) wdf[f] = ld128(wsr, lane * 16, wdo + (u32)((f & 1) * 112 + (f >> 1)) * 1024);
  }
  STAMPL(23, "h flagged");
  if (dact) {
    BAR();
#pragma unroll
    for (int f = 18; f < 28; ++f) wdf[f] = ld128(wsr, lane * 16, wdo + (u32)((f & 1) * 112 + (f >> 1)) * 1024);
    const int drt = 8 * xg + 2 * dq, dcol = 64 * dc + 32 * dn + 16 * dgp + 8 * hi;
    gbA = ld128(wsr, dcol * 4, OFF_MODG + 1120 * 8); gbB = ld128(wsr, dcol * 4 + 16, OFF_MODG + 1120 * 8);
    wait_flags(ws + OFF_HF + (((drt >> 2) * 16 + 2 * wave + ((lane >> 3) & 1)) * 8 + min(lane & 7, 6)) * 128, tag);
    STAMPL(24, "h flags seen");
    u32x4 hf[20];
#define LDH(i) hf[(i) % 20] = ld128(wsr, lane * 16 + (((i) % 14) & 3) * 1024, OFF_HB + (u32)((drt + (i) / 14) * 112 + 14 * wave + (((i) % 14) & ~3)) * 1024)
#pragma unroll
    for (int i = 0; i < 20; ++i) { LDH(i); SB(); }
    x2r = ld128(wsr, lane * 16, OFF_X2 + (u32)((drt + dm) * 28 + 4 * dc + 2 * dn + dgp) * 1024);
    f32x16 c0, c1;
    u32 pwb = lb + wave * 16384 + lane * 16; asm volatile("" : "+v"(pwb));
#pragma unroll
    for (int i = 0; i < 28; ++i) {
      const int m = i / 14, j = i % 14;
      SB();
      if (j == 0) { c0 = mfma32(wdf[0], hf[i % 20], (f32x16){}); c1 = mfma32(wdf[1], hf[i % 20], (f32x16){}); }
      else { c0 = mfma32(wdf[2 * j], hf[i % 20], c0); c1 = mfma32(wdf[2 * j + 1], hf[i % 20], c1); }
      SB();
      if (i < 8) LDH(i + 20);
      if (j == 13) {
#pragma unroll
        for (int g = 0; g < 4; ++g) {
          *(LAS f32x4*)(pwb + ((2 * m) * 4 + g) * 1024) = (f32x4){c0[4 * g], c0[4 * g + 1], c0[4 * g + 2], c0[4 * g + 3]};
          *(LAS f32x4*)(pwb + ((2 * m + 1) * 4 + g) * 1024) = (f32x4){c1[4 * g], c1[4 * g + 1], c1[4 * g + 2], c1[4 * g + 3]};
        }
      }
    }
    STAMPL(25, "down mfma done");
    BAR();
    f32x4 pa[8], pb[8];
    {
      u32 base = lb + (2 * dm + dn) * 4096 + dgp * 2048 + lane * 16, base2 = base + 65536;
      asm volatile("" : "+v"(base), "+v"(base2));
#pragma unroll
      for (int w = 0; w < 4; ++w) {
        pa[w] = lds128f(base + w * 16384); pb[w] = lds128f(base + w * 16384 + 1024);
        pa[4 + w] = lds128f(base2 + w * 16384); pb[4 + w] = lds128f(base2 + w * 16384 + 1024);
      }
      SB();
    }
    const f32x4 a0 = ((pa[0] + pa[4]) + (pa[1] + pa[5])) + ((pa[2] + pa[6]) + (pa[3] + pa[7])), a1 = ((pb[0] + pb[4]) + (pb[1] + pb[5])) + ((pb[2] + pb[6]) + (pb[3] + pb[7]));
    float yv[8];
#pragma unroll
    for (int r = 0; r < 4; ++r) { auto sw = __builtin_amdgcn_permlane32_swap(__float_as_uint(a0[r]), __float_as_uint(a1[r]), false, false); yv[r] = __uint_as_float(sw[0]); yv[4 + r] = __uint_as_float(sw[1]); }
    const u32 gw[4] = {gbA[0], gbA[2], gbB[0], gbB[2]};
    u32x4 ov;
#pragma unroll
    for (int j = 0; j < 4; ++j) ov[j] = pk2(fmaf(bflo(gw[j]), yv[2 * j], bflo(x2r[j])), fmaf(bfhi(gw[j]), yv[2 * j + 1], bfhi(x2r[j])));
    *(u32x4*)(out + (size_t)(32 * (drt + dm) + l31) * 448 + dcol) = ov;
    if (b == 0 && tid == 0) *(GAS u32*)ws = tag;
  }
  STAMPL(26, "end");
}

extern "C" void kernel_launch(void* const* d_in, const int* in_sizes, int n_in, void* d_out, int out_size, void* d_ws, size_t ws_size, hipStream_t stream) {
  fused_block<<<256, 512, 0, stream>>>((const u16*)d_in[0], (const u16*)d_in[1], (const u16*)d_in[2], (const u16*)d_in[4], (const u16*)d_in[5],
                                       (const u16*)d_in[6], (const u16*)d_in[7], (const u16*)d_in[8], (u16*)d_out, (char*)d_ws);
  probe_spin<<<1, 64, 0, stream>>>((char*)d_ws, PROBE_MUL);
}
```

```cpp
#define PRETOUCH 2
#include <hip/hip_runtime.h>
#include <stdint.h>

typedef unsigned short u16;
typedef unsigned u32;
typedef unsigned long long u64;
typedef __attribute__((ext_vector_type(2))) unsigned u32x2;
typedef __attribute__((ext_vector_type(4))) unsigned u32x4;
typedef __attribute__((ext_vector_type(2))) float f32x2;
typedef __attribute__((ext_vector_type(4))) float f32x4;
typedef __attribute__((ext_vector_type(16))) float f32x16;
typedef __attribute__((ext_vector_type(2))) __bf16 bf16x2;
typedef __attribute__((ext_vector_type(8))) __bf16 bf16x8;
typedef __attribute__((ext_vector_type(4))) short s16x4;
#define DEV __device__ __forceinline__
#define GAS __attribute__((address_space(1)))
#define LAS __attribute__((address_space(3)))
#define RLX_AGENT __ATOMIC_RELAXED, __HIP_MEMORY_SCOPE_AGENT

constexpr u32 OFF_MODG0 = 4096, MODG_REP = 11264;
constexpr u32 OFF_X2G = 131072, OFF_UV = 13467648  , OFF_WQT = 524288, OFF_WOT = 1900544, OFF_WGU = 2359296, OFF_WDT = 5570560;
constexpr u32 OFF_KV = 7176192, OFF_OB = 11370496, OFF_X2 = 15302656, OFF_HB = 17137664, OFF_XH = 24477696;
constexpr u32 OFF_XF = 26312704, OFF_WQF = OFF_XF + 512 * 128, OFF_KVG = OFF_WQF + 1024 * 128, OFF_OF = OFF_KVG + 1024 * 128, OFF_HF = OFF_OF + 2048 * 128, OFF_PREPF = OFF_HF + 2048 * 128, WS_USED = OFF_PREPF + 256 * 128;
constexpr u32 OFF_STAMP = 29360128;
constexpr u32 L_STG = 90112  , L_Q = 131072, L_GAM = 139264, L_QN = 142848;
constexpr u32 L_PARK = 64512 + 1024  ;
constexpr u32 L_SP2 = 151552  , L_MODP = L_SP2, L_MX = L_SP2, L_LP = L_SP2, L_UV = 155648  , L_ST = 157696  , L_G2 = 159488  ;
constexpr u32 L_SYNC = 160000;
constexpr u32 LDS_BYTES = 163840;
constexpr int CODE_BYTES = 42000;
constexpr int SPIN_CAP = 1 << 15;
#define FIRST_POLL 265
#define KV_SLEEP 1
#define MOD_POLL 250
#define NREP 8
#define GATE 0
#define XP0 8
#define NOCODEPF 0
#define TOUCH_AT 0
#define LATE_PRE 5
#define GU_RING 6
#define PREP_START 130
#define POLL_T 0
#define POLL_SLEEP 3

DEV float bflo(u32 w) { return __uint_as_float(w << 16); }
DEV float bfhi(u32 w) { return __uint_as_float(w & 0xffff0000u); }
DEV u32 pk2(float a, float b) { f32x2 v = {a, b}; return __builtin_bit_cast(u32, __builtin_convertvector(v, bf16x2)); }
DEV float rbf(float a) { return bflo(pk2(a, 0.f)); }
DEV float fma1(float a, float b, float c) { float r; asm("v_fma_f32 %0, %1, %2, %3" : "=v"(r) : "v"(a), "v"(b), "v"(c)); return r; }
DEV float dot2(u32 a, u32 b, float c) { return __builtin_amdgcn_fdot2_f32_bf16(__builtin_bit_cast(bf16x2, a), __builtin_bit_cast(bf16x2, b), c, false); }
DEV f32x16 mfma32(u32x4 a, u32x4 b, f32x16 c) { return __builtin_amdgcn_mfma_f32_32x32x16_bf16(__builtin_bit_cast(bf16x8, a), __builtin_bit_cast(bf16x8, b), c, 0, 0, 0); }
DEV f32x4 mfma16(u32x4 a, u32x4 b, f32x4 c) { return __builtin_amdgcn_mfma_f32_16x16x32_bf16(__builtin_bit_cast(bf16x8, a), __builtin_bit_cast(bf16x8, b), c, 0, 0, 0); }
DEV float xhalf_sum(float v) { auto r = __builtin_amdgcn_permlane32_swap(__float_as_uint(v), __float_as_uint(v), false, false); return __uint_as_float(r[0]) + __uint_as_float(r[1]); }
DEV float xhalf_max(float v) { auto r = __builtin_amdgcn_permlane32_swap(__float_as_uint(v), __float_as_uint(v), false, false); return fmaxf(__uint_as_float(r[0]), __uint_as_float(r[1])); }
#define DPPF(v, ctrl) __uint_as_float(__builtin_amdgcn_update_dpp(0u, __float_as_uint(v), ctrl, 0xf, 0xf, false))
DEV float wave_max(float v) {
  v = fmaxf(v, DPPF(v, 0x121)); v = fmaxf(v, DPPF(v, 0x122)); v = fmaxf(v, DPPF(v, 0x124)); v = fmaxf(v, DPPF(v, 0x128));
  auto r = __builtin_amdgcn_permlane16_swap(__float_as_uint(v), __float_as_uint(v), false, false);
  return xhalf_max(fmaxf(__uint_as_float(r[0]), __uint_as_float(r[1])));
}
template <int I> struct IC { static constexpr int value = I; };
template <int I, int N, class F> DEV void static_for(F&& f) { if constexpr (I < N) { f(IC<I>{}); static_for<I + 1, N>(f); } }
#define WAIT_VM(n) asm volatile("s_waitcnt vmcnt(" #n ")" ::: "memory")
#define BAR() asm volatile("s_waitcnt lgkmcnt(0)\n\ts_barrier" ::: "memory")
#define SB() __builtin_amdgcn_sched_barrier(0)
#define CFENCE() asm volatile("" ::: "memory")
template <bool SC1> DEV void dma16(const void* sbase, u32 voff, u32 lds_dst) {
  u32 keep;
  if (SC1) asm volatile("s_mov_b32 %0, m0\n\ts_mov_b32 m0, %3\n\ts_nop 2\n\tglobal_load_lds_dwordx4 %1, %2 sc1\n\ts_mov_b32 m0, %0" : "=&s"(keep) : "v"(voff), "s"(sbase), "s"(lds_dst) : "memory");
  else asm volatile("s_mov_b32 %0, m0\n\ts_mov_b32 m0, %3\n\ts_nop 2\n\tglobal_load_lds_dwordx4 %1, %2\n\ts_mov_b32 m0, %0" : "=&s"(keep) : "v"(voff), "s"(sbase), "s"(lds_dst) : "memory");
}
DEV void dma16x4(const void* sbase, u32 voff, u32 lds_dst) {
  u32 keep;
  asm volatile("s_mov_b32 %0, m0\n\ts_mov_b32 m0, %3\n\ts_nop 2\n\tglobal_load_lds_dwordx4 %1, %2 sc1\n\tglobal_load_lds_dwordx4 %1, %2 offset:1024 sc1\n\t"
               "global_load_lds_dwordx4 %1, %2 offset:2048 sc1\n\tglobal_load_lds_dwordx4 %1, %2 offset:3072 sc1\n\ts_mov_b32 m0, %0" : "=&s"(keep) : "v"(voff), "s"(sbase), "s"(lds_dst) : "memory");
}
DEV void dma4_touch(const void* sbase, u32 voff, u32 lds_dst) {
  u32 keep;
  asm volatile("s_mov_b32 %0, m0\n\ts_mov_b32 m0, %3\n\ts_nop 2\n\tglobal_load_lds_dword %1, %2 sc1\n\ts_mov_b32 m0, %0" : "=&s"(keep) : "v"(voff), "s"(sbase), "s"(lds_dst) : "memory");
}
DEV void dma4_plain(const void* sbase, u32 voff, u32 lds_dst) {
  u32 keep;
  asm volatile("s_mov_b32 %0, m0\n\ts_mov_b32 m0, %3\n\ts_nop 2\n\tglobal_load_lds_dword %1, %2\n\ts_mov_b32 m0, %0" : "=&s"(keep) : "v"(voff), "s"(sbase), "s"(lds_dst) : "memory");
}
DEV u32x4 lds128(u32 a) { return *(const LAS u32x4*)a; }
DEV f32x4 lds128f(u32 a) { return *(const LAS f32x4*)a; }
DEV float lds32f(u32 a) { return *(const LAS float*)a; }
DEV u32x2 ldstr(u32 a) { return __builtin_bit_cast(u32x2, __builtin_amdgcn_ds_read_tr16_b64_v4i16((LAS s16x4*)a)); }
DEV u32x4 cat(u32x2 a, u32x2 b) { return (u32x4){a.x, a.y, b.x, b.y}; }
typedef __amdgpu_buffer_rsrc_t rsrc_t;
DEV u32x4 ld128(rsrc_t r, u32 voff, u32 soff) { return __builtin_amdgcn_raw_buffer_load_b128(r, voff, soff, 16); }
DEV u32 ld32(rsrc_t r, u32 voff, u32 soff) { return __builtin_amdgcn_raw_buffer_load_b32(r, voff, soff, 16); }
DEV void st128(rsrc_t r, u32 voff, u32 soff, u32x4 v) {
  __builtin_amdgcn_raw_buffer_store_b128(v, r, voff, soff, 16);
  asm volatile("s_nop 1" :: "v"(v) : "memory");
}
DEV void st32(rsrc_t r, u32 voff, u32 soff, u32 v) { __builtin_amdgcn_raw_buffer_store_b32(v, r, voff, soff, 16); }
DEV u32 poll32(const char* p) { return __hip_atomic_load((GAS u32*)p, RLX_AGENT); }
DEV u64 poll64(const char* p) { return __hip_atomic_load((GAS u64*)p, RLX_AGENT); }
DEV void flag32(char* p, u32 v) { __hip_atomic_store((GAS u32*)p, v, RLX_AGENT); }
DEV void flag64(char* p, u32 val, u32 tag) { __hip_atomic_store((GAS u64*)p, ((u64)tag << 32) | val, RLX_AGENT); }
DEV void wait_flags(const char* p, u32 tag) {
#pragma nounroll
  for (int i = 0; i < SPIN_CAP; ++i) { if (__all(poll32(p) == tag)) break; __builtin_amdgcn_s_sleep(POLL_SLEEP); }
  CFENCE();
}
DEV u32 wait_gran(const char* p, u32 tag) {
  u64 g = 0;
#pragma nounroll
  for (int i = 0; i < SPIN_CAP; ++i) { g = poll64(p); if (__all((u32)(g >> 32) == tag)) break; __builtin_amdgcn_s_sleep(POLL_SLEEP); }
  CFENCE();
  return (u32)g;
}
DEV u32 lds_ld(u32 a) { return __hip_atomic_load((LAS u32*)a, __ATOMIC_RELAXED, __HIP_MEMORY_SCOPE_WORKGROUP); }
DEV void lds_st(u32 a, u32 v) { __hip_atomic_store((LAS u32*)a, v, __ATOMIC_RELAXED, __HIP_MEMORY_SCOPE_WORKGROUP); }
DEV u32 lds_inc(u32 a) { return __builtin_amdgcn_readfirstlane(__hip_atomic_fetch_add((LAS u32*)a, 1u, __ATOMIC_RELAXED, __HIP_MEMORY_SCOPE_WORKGROUP)); }
DEV void lds_wait(u32 a, u32 want) {
#pragma nounroll
  for (int i = 0; i < (SPIN_CAP << 6); ++i) { if (__builtin_amdgcn_readfirstlane(lds_ld(a)) >= want) break; __builtin_amdgcn_s_sleep(1); }
  CFENCE();
}
#define WAIT_LGKM() asm volatile("s_waitcnt lgkmcnt(0)" ::: "memory")
#define STAMP(i) do {} while (0)
#define STAMPL(i, s) STAMP(i)

__global__ __launch_bounds__(512, 2) void fused_block(const u16* __restrict__ x, const u16* __restrict__ wqkv, const u16* __restrict__ wo,
                                                      const u16* __restrict__ wg, const u16* __restrict__ wu, const u16* __restrict__ wd,
                                                      const u16* __restrict__ cvec, const u16* __restrict__ wmod, u16* __restrict__ out, char* ws) {
  __shared__ __attribute__((aligned(16))) char smem[LDS_BYTES];
  const int b = blockIdx.x, tid = threadIdx.x, lane = tid & 63;
  const int wave = __builtin_amdgcn_readfirstlane(tid >> 6);
  const int l31 = lane & 31, hi = lane >> 5;
  const int xg = b & 7, slot = b >> 3;
  const int ai = 4 * (xg >> 1) + (slot & 3), ah = 8 * (xg & 1) + (slot >> 2);
  const u32 lb = (u32)(uintptr_t)(LAS char*)smem;
  const rsrc_t wsr = __builtin_amdgcn_make_buffer_rsrc(ws, 0, WS_USED, 0x00020000);
  STAMP(0);
  {
    u64 pc; asm volatile("s_getpc_b64 %0" : "=s"(pc));
    if (wave == 0 && !NOCODEPF) {
      const u32 off = min((u32)(slot * 1408 + min(lane, 10) * 128), (u32)CODE_BYTES);
      u32 keep;
      asm volatile("s_mov_b32 %0, m0\n\ts_mov_b32 m0, %3\n\ts_nop 2\n\tglobal_load_lds_dword %1, %2\n\ts_mov_b32 m0, %0" : "=&s"(keep) : "v"(off), "s"(pc), "s"(lb + L_ST) : "memory");
    }
  }
  asm volatile("" :: "s"(x), "s"(wqkv), "s"(wo), "s"(wg), "s"(wu), "s"(wd), "s"(cvec), "s"(wmod), "s"(out), "s"(ws) : "memory");
  SB();
  if (PRETOUCH >= 1 && wave == 1)
    dma4_plain(ws, lane < 16 || PRETOUCH < 2 ? min(lane, 12) << 21 : min(slot * 14 + min(lane - 16, 13), 415) << 16, lb + L_ST + 256);
  if (PRETOUCH >= 2 && wave == 3) dma4_plain(out, min(lane, 27) << 16, lb + L_ST + 512);
  if (wave == 2 && lane < 32) lds_st(lb + L_SYNC + lane * 4, 0u);
  const u64 t_entry = __builtin_amdgcn_s_memrealtime();
  const u32 epoch_v = poll32(ws);
  u32 tag;

  f32x16 accQ, accK, accV;
  const u32 OFF_MODG = OFF_MODG0 + (NREP == 8 ? xg : 0) * MODG_REP;
  const int tq4 = (lane & 15) >> 2, tp4 = lane & 3, grp = lane >> 4;
  if (b < 42) {
    u32x4 wv[7];
    const u32 cv = cvec[56 * wave + min(lane, 55)];
#pragma unroll
    for (int n = 0; n < 7; ++n) wv[n] = *(const u32x4*)((const char*)wmod + (size_t)(56 * wave + 8 * n + (lane >> 3)) * 5376 + 128 * b + 16 * (lane & 7));
    BAR();
    const float cf = bflo(cv);
    const float sl = rbf(cf * __builtin_amdgcn_rcpf(1.f + __builtin_amdgcn_exp2f(-1.4426950408889634f * cf)));
    float acc[8] = {0.f, 0.f, 0.f, 0.f, 0.f, 0.f, 0.f, 0.f};
#pragma unroll
    for (int n = 0; n < 7; ++n) {
      const float sn = __uint_as_float(__builtin_amdgcn_ds_bpermute((8 * n + (lane >> 3)) * 4, __float_as_uint(sl)));
#pragma unroll
      for (int j = 0; j < 4; ++j) { acc[2 * j] = fmaf(sn, bflo(wv[n][j]), acc[2 * j]); acc[2 * j + 1] = fmaf(sn, bfhi(wv[n][j]), acc[2 * j + 1]); }
    }

#pragma unroll
    for (int j = 0; j < 8; ++j) acc[j] += DPPF(acc[j], 0x128);
#pragma unroll
    for (int j = 0; j < 4; ++j) { auto r = __builtin_amdgcn_permlane16_swap(__float_as_uint(acc[j]), __float_as_uint(acc[j + 4]), false, false); acc[j] = __uint_as_float(r[0]) + __uint_as_float(r[1]); }
#pragma unroll
    for (int j = 0; j < 2; ++j) { auto r = __builtin_amdgcn_permlane32_swap(__float_as_uint(acc[j]), __float_as_uint(acc[j + 2]), false, false); acc[j] = __uint_as_float(r[0]) + __uint_as_float(r[1]); }
    if (!(lane & 8)) *(LAS f32x2*)(lb + L_MODP + (wave * 64 + 8 * (lane & 7) + 2 * hi + 4 * (grp & 1)) * 4) = (f32x2){acc[0], acc[1]};
    WAIT_LGKM();
    u32 arrived = 0;
    if (lane == 0) arrived = __hip_atomic_fetch_add((LAS u32*)(lb + L_SYNC + 12), 1u, __ATOMIC_RELAXED, __HIP_MEMORY_SCOPE_WORKGROUP);
    if (__builtin_amdgcn_readfirstlane(arrived) == 7) {
      CFENCE();
      f32x2 t = {0.f, 0.f};
#pragma unroll
      for (int p = 0; p < 8; ++p) t += *(const LAS f32x2*)(lb + L_MODP + (p * 64 + 2 * l31) * 4);
      const u32 pv = pk2(t[0], t[1]);
      const u32 g0 = __builtin_amdgcn_ds_bpermute((2 * (lane & 15)) * 4, pv), g1 = __builtin_amdgcn_ds_bpermute((2 * (lane & 15) + 1) * 4, pv);
#pragma unroll
      for (int r = 0; r < NREP / 4; ++r) st128(wsr, (u32)(32 * b + 2 * (lane & 15)) * 8 + (4 * r + grp) * MODG_REP, OFF_MODG0, (u32x4){g0, epoch_v + 1u, g1, epoch_v + 1u});
      lds_st(lb + L_SYNC + 20, 1u);
      WAIT_VM(0);
      lds_st(lb + L_SYNC + 32, 1u);
    }
  } else if (wave < 3 && 3 * (b - 42) + wave < 512) {
    const int xgp = 3 * (b - 42) + wave, row = 4 * xgp + (lane & 3), c0i = lane >> 2;
    u32x4 xr[4];
#pragma unroll
    for (int n = 0; n < 4; ++n) xr[n] = *(const u32x4*)((const char*)x + (size_t)row * 896 + min(c0i + 16 * n, 55) * 16);
    BAR();
    if (c0i >= 8) xr[3] = (u32x4){0u, 0u, 0u, 0u};
    float s1[2] = {0.f, 0.f}, s2[2] = {0.f, 0.f};
#pragma unroll
    for (int n = 0; n < 4; ++n)
#pragma unroll
      for (int j = 0; j < 4; ++j) { s1[j & 1] = dot2(xr[n][j], 0x3F803F80u, s1[j & 1]); s2[j & 1] = dot2(xr[n][j], xr[n][j], s2[j & 1]); }
    float t1 = s1[0] + s1[1], t2 = s2[0] + s2[1];
    t1 += DPPF(t1, 0x124); t2 += DPPF(t2, 0x124); t1 += DPPF(t1, 0x128); t2 += DPPF(t2, 0x128);
    { auto r = __builtin_amdgcn_permlane16_swap(__float_as_uint(t1), __float_as_uint(t2), false, false); t1 = __uint_as_float(r[0]) + __uint_as_float(r[1]); }
    t1 = xhalf_sum(t1);
    { auto r = __builtin_amdgcn_permlane16_swap(__float_as_uint(t1), __float_as_uint(t1), false, false); t1 = __uint_as_float(r[0]); t2 = __uint_as_float(r[1]); }
    const float mean = t1 * (1.f / 448.f), ex2 = t2 * (1.f / 448.f);
    const float rstd = __builtin_amdgcn_rsqf(fmaxf(ex2 - mean * mean, 0.f) + 1e-5f), c0 = -mean * rstd;
    asm volatile("" :: "v"(c0));

#pragma unroll
    for (int n = 0; n < 4; ++n) {
      u32x4 o;
#pragma unroll
      for (int j = 0; j < 4; ++j) asm("v_cvt_pk_f16_f32 %0, %1, %2" : "=v"(o[j]) : "v"(fmaf(bflo(xr[n][j]), rstd, c0)), "v"(fmaf(bfhi(xr[n][j]), rstd, c0)));
      const u32 c = c0i + 16 * n;
      st128(wsr, c < 56 ? (c >> 1) * 1024 + ((row & 31) + 32 * (c & 1)) * 16 + (u32)(row >> 5) * 28672 : 0x80000000u, OFF_XH, o);
    }
    WAIT_VM(0);
    if (lane == 0) flag32(ws + OFF_XF + xgp * 128, epoch_v + 1u);
  } else if ((wave == 4 || wave == 5) && b < 210) {
    const int u = b - 42, part = u / 56, rem = u - part * 56, hp = rem / 7, kg = rem - hp * 7, s0 = 4 * kg + 2 * (wave - 4);
    const char* src = (const char*)wqkv + (size_t)(16 * s0) * 2688 + part * 896 + hp * 112;
    const u32 stg = lb + L_Q + (wave - 4) * 4096;
#pragma unroll
    for (int i = 0; i < 4; ++i) { const u32 c = lane & 7; dma16<false>(src + i * 8 * 2688, (lane >> 3) * 2688 + 16 * (c == 7 ? 6 : c), stg + i * 1024); }
    BAR();
    WAIT_VM(0);

#pragma unroll
    for (int e = 0; e < 4; ++e) {
      const u32 a = stg + (16 * (e >> 1) + 8 * (grp >> 1) + tq4) * 128 + (e & 1) * 56 + (grp & 1) * 32 + tp4 * 8;
      u32x4 f = cat(ldstr(a), ldstr(a + 512));
      if (l31 >= 28) f = (u32x4){0u, 0u, 0u, 0u};
      st128(wsr, lane * 16, OFF_WQT + (u32)((2 * hp + (e & 1)) * 84 + 3 * (s0 + (e >> 1)) + part) * 1024, f);
    }
    WAIT_VM(0);
    if (lane < 2) flag32(ws + OFF_WQF + ((2 * hp + lane) * 64 + 3 * (s0 >> 1) + part) * 128, epoch_v + 1u);
  } else {
    BAR();
  }
  tag = __builtin_amdgcn_readfirstlane(epoch_v) + 1u;
  STAMPL(7, "unit done");
  const int cg = b / 7, kg = b - cg * 7;
  if (wave >= 6) {
#pragma nounroll
    for (int i = 0; i < 4096 && __builtin_amdgcn_s_memrealtime() - t_entry < PREP_START; ++i) __builtin_amdgcn_s_sleep(1);
    if (b < 42) lds_wait(lb + L_SYNC + 20, 1);
#pragma unroll
    for (int ph = 0; ph < 2; ++ph) {
      const int pw = wave & 1, tw = 2 * ph + pw, p = 4 * b + tw;
      const u32 stg = lb + L_STG + tw * 6144;
      if (b < 196) {
#pragma unroll
        for (int i = 0; i < 2; ++i) {
          const u32 vo = (u32)(64 * kg + 32 * ph + 16 * i + (lane >> 2)) * 3584 + (64 * cg + 32 * pw) * 2 + 16 * (lane & 3);
          dma16<false>(wg, vo, stg + (2 * i) * 1024); dma16<false>(wu, vo, stg + (2 * i + 1) * 1024);
        }
      }
      if (p < 784) {
        const int ds_ = p / 7, np = p - ds_ * 7;
#pragma unroll
        for (int i = 0; i < 2; ++i) dma16<false>(wd, (u32)(16 * ds_ + (lane >> 2)) * 896 + np * 128 + i * 64 + 16 * (lane & 3), stg + (4 + i) * 1024);
      } else if (p < 1008) {
        const int q = p - 784, h = q / 14, nq = q - h * 14;
#pragma unroll
        for (int i = 0; i < 2; ++i) dma16<false>(wo, (u32)min(28 * h + 16 * i + (lane >> 2), 447) * 896 + nq * 64 + 16 * (lane & 3), stg + (4 + i) * 1024);
      }
    }
    STAMPL(6, "prep issued");
    WAIT_VM(0);
    if (lane == 0) __hip_atomic_fetch_add((LAS u32*)(lb + L_SYNC + 52), 1u, __ATOMIC_RELAXED, __HIP_MEMORY_SCOPE_WORKGROUP);
    STAMPL(5, "prep landed");
  } else {
#pragma nounroll
    for (int i = 0; i < 4096 && __builtin_amdgcn_s_memrealtime() - t_entry < (wave == 4 ? MOD_POLL : wave >= 3 ? FIRST_POLL : TOUCH_AT); ++i) __builtin_amdgcn_s_sleep(1);
    if (TOUCH_AT && wave < 3) {
      dma4_touch(ws + OFF_WQT + (size_t)ah * 86016 + (slot & 3) * 21504, min(wave * 64 + lane, 167) * 128, lb + L_ST + wave * 256);
      if (wave < 2) dma4_touch(ws + OFF_XH + (size_t)ai * 114688 + (slot >> 2) * 14336, min(wave * 64 + lane, 111) * 128, lb + L_ST + 768 + wave * 256);
    }
  }

  if (wave < 4) {
    u32x4 xf[28], pf[6]; u32 pfo[6], uvo[2]; float uvv[2];
    const int pw = wave & 1, ph = (wave >> 1) & 1;
    auto tile_read = [&]() __attribute__((always_inline)) {
      const int p = 4 * b + wave;
      const u32 stg = lb + L_STG + wave * 6144;
      const bool isd = p < 784;
      const int ds_ = p / 7, np = p - ds_ * 7, q = p - 784, h = q / 14, nq = q - h * 14;
      lds_wait(lb + L_SYNC + 52, 2);
      const int pi = ((tp4 & 1) << 1) | (tp4 >> 1);
#pragma unroll
      for (int i = 0; i < 2; ++i)
#pragma unroll
        for (int e = 0; e < 2; ++e) {
          const u32 a = stg + (2 * i + (grp & 1)) * 1024 + (8 * (grp >> 1) + tq4) * 64 + e * 32 + pi * 8;
          pf[2 * i + e] = cat(ldstr(a), ldstr(a + 256));
          pfo[2 * i + e] = lane * 16 + (b < 196 ? OFF_WGU + (u32)((4 * cg + 2 * pw + e) * 28 + 4 * kg + 2 * ph + i) * 1024 : 0x80000000u);
        }
#pragma unroll
      for (int i = 0; i < 2; ++i) {
        const u32 a = isd ? stg + (4 + i) * 1024 + (8 * (grp >> 1) + tq4) * 64 + (grp & 1) * 32 + tp4 * 8
                          : stg + 4096 + (16 * (grp >> 1) + 4 * (grp & 1) + tq4) * 64 + i * 32 + tp4 * 8;
        u32x4 f = cat(ldstr(a), ldstr(a + (isd ? 256 : 512)));
        if (!isd && grp == 3) { f[2] = 0u; f[3] = 0u; }
        pf[4 + i] = f;
        pfo[4 + i] = lane * 16 + (isd ? OFF_WDT + (u32)((2 * np + i) * 112 + ds_) * 1024 : p < 1008 ? OFF_WOT + (u32)((2 * nq + i) * 16 + h) * 1024 : 0x80000000u);
        uvo[i] = lane * 4 + (b < 196 ? OFF_UV + (u32)((2 * kg + ph) * 112 + 4 * cg + 2 * pw + i) * 256 : 0x80000000u);
      }
      asm volatile("" : "+v"(pf[0]), "+v"(pf[1]), "+v"(pf[2]), "+v"(pf[3]), "+v"(pf[4]), "+v"(pf[5]));
    };
    if (wave == 3) { wait_flags(ws + OFF_XF + (32 * ai + l31) * 128, tag); STAMPL(1, "xhat seen"); if (GATE) lds_wait(lb + L_SYNC + 36, 1); lds_st(lb + L_SYNC, 1u); }
    else { tile_read(); lds_wait(lb + L_SYNC, 1); }
    STAMPL(3, "xhat go");
    constexpr int XPRE = 12;
#define LDX(s) xf[s] = ld128(wsr, lane * 16 + ((s) & 3) * 1024, OFF_XH + (u32)((4 * ai + wave) * 28 + ((s) & ~3)) * 1024)
#pragma unroll
    for (int s = 0; s < XP0; ++s) { LDX(s); SB(); }
    if (wave == 3) tile_read();
    if (!GATE) lds_wait(lb + L_SYNC + 36, 1);
    {
      float uu[2] = {0.f, 0.f}, vv[2] = {0.f, 0.f};
#pragma unroll
      for (int i = 0; i < 2; ++i) {
        const u32x4 be = lds128(lb + L_G2 + (2 * ph + i) * 32 + hi * 16), ga = lds128(lb + L_G2 + 128 + (2 * ph + i) * 32 + hi * 16);
#pragma unroll
        for (int e = 0; e < 2; ++e)
#pragma unroll
          for (int j = 0; j < 4; ++j) {
            const u32 w = pf[2 * i + e][j];
            vv[e] = dot2(w, be[j], vv[e]);
            pf[2 * i + e][j] = pk2(bflo(w) * bflo(ga[j]), bfhi(w) * bfhi(ga[j]));
            uu[e] = dot2(pf[2 * i + e][j], 0x3F803F80u, uu[e]);
          }
      }
#pragma unroll
      for (int e = 0; e < 2; ++e) { const float us = xhalf_sum(uu[e]), vs = xhalf_sum(vv[e]); uvv[e] = hi ? vs : us; }
      asm volatile("" : "+v"(pf[0]), "+v"(pf[1]), "+v"(pf[2]), "+v"(pf[3]), "+v"(uvv[0]), "+v"(uvv[1]));
      STAMPL(10, "prep tiled");
    }
#pragma unroll
    for (int s = XP0; s < XPRE; ++s) { LDX(s); SB(); }
    lds_wait(lb + L_SYNC + 4, 4);

    STAMPL(8, "W1 ready");
    accQ = (f32x16){}; accK = (f32x16){}; accV = (f32x16){};
    {
      u32 gb = lb + L_GAM + hi * 16; asm volatile("" : "+v"(gb));
      u32 wb = lb + lane * 16, wb2 = wb + 43008; asm volatile("" : "+v"(wb), "+v"(wb2));
      u32x4 wf[3][3], hf[2], gs[3], be[3]; float ht[8];
#define RDG(s) gs[(s) % 3] = lds128(gb + (s) * 32)
#define RDB(s) be[(s) % 3] = lds128(gb + 896 + (s) * 32)
#define RDW1(s, p) wf[(s) % 3][p] = lds128(((s) < 14 ? wb : wb2) + (3 * ((s) % 14) + (p)) * 1024)
#define RDW(s) do { RDW1(s, 0); RDW1(s, 1); RDW1(s, 2); } while (0)
#define DOTS(s, h) do { _Pragma("unroll") for (int j = 2 * (h); j < 2 * (h) + 2; ++j) { \
        asm volatile("v_fma_mix_f32 %0, %1, %2, %3 op_sel_hi:[1,1,1]" : "=v"(ht[2 * j]) : "v"(xf[s][j]), "v"(gs[(s) % 3][j]), "v"(be[(s) % 3][j])); \
        asm volatile("v_fma_mix_f32 %0, %1, %2, %3 op_sel:[1,1,1] op_sel_hi:[1,1,1]" : "=v"(ht[2 * j + 1]) : "v"(xf[s][j]), "v"(gs[(s) % 3][j]), "v"(be[(s) % 3][j])); } } while (0)
#define CVTS(s) do { hf[(s) & 1] = (u32x4){pk2(ht[0], ht[1]), pk2(ht[2], ht[3]), pk2(ht[4], ht[5]), pk2(ht[6], ht[7])}; asm volatile("" : "+v"(hf[(s) & 1])); } while (0)
      RDG(0); RDB(0); RDW(0); RDG(1); RDB(1); RDW(1); RDG(2); RDB(2); RDW(2); DOTS(0, 0); DOTS(0, 1); CVTS(0); SB();
#pragma unroll
      for (int s = 0; s < 28; ++s) {
        if (s == 1) lds_wait(lb + L_SYNC + 8, 3);
        if (s == 13) lds_wait(lb + L_SYNC + 24, 3);
        SB(); accQ = mfma32(wf[s % 3][0], hf[s & 1], accQ); SB();
        if (s < 25) RDG(s + 3);
        if (s < 25) RDW1(s + 3, 0);
        if (s < 27) DOTS(s + 1, 0);
        SB(); accK = mfma32(wf[s % 3][1], hf[s & 1], accK); SB();
        if (s < 25) RDB(s + 3);
        if (s < 25) RDW1(s + 3, 1);
        if (s < 27) DOTS(s + 1, 1);
        SB(); accV = mfma32(hf[s & 1], wf[s % 3][2], accV); SB();
        if (s < 25) RDW1(s + 3, 2);
        if (s + XPRE < 28) LDX(s + XPRE);
        else if (s < 22) st128(wsr, pfo[s - 16], 0, pf[s - 16]);
        else if (s < 24) st32(wsr, uvo[s - 22], 0, __float_as_uint(uvv[s - 22]));
        if (s < 27) CVTS(s + 1);
        SB();
      }
    }
    STAMPL(5, "qkv mfma done");
    const float unscale = __uint_as_float(__builtin_amdgcn_readfirstlane(lds_ld(lb + L_SYNC + 28)));
    if (unscale != 1.f) { accQ *= unscale; accK *= unscale; accV *= unscale; }
    u32x4 k0, k1, v0, v1, q0, q1;
#pragma unroll
    for (int j = 0; j < 4; ++j) {
      k0[j] = pk2(accK[2 * j], accK[2 * j + 1]); k1[j] = pk2(accK[8 + 2 * j], accK[9 + 2 * j]);
      v0[j] = pk2(accV[2 * j], accV[2 * j + 1]); v1[j] = pk2(accV[8 + 2 * j], accV[9 + 2 * j]);
    }
    if (hi) { k1[2] = 0x3F803F80u; k1[3] = 0x00003F80u; }
    if (l31 == 28) { v0 = (u32x4){0x3F803F80u, 0x3F803F80u, 0x3F803F80u, 0x3F803F80u}; v1 = v0; }
    const u32 kvo = OFF_KV + (u32)(ah * 64 + ai * 4 + wave) * 4096;
    st128(wsr, lane * 16, kvo, k0); st128(wsr, lane * 16 + 1024, kvo, k1); st128(wsr, lane * 16 + 2048, kvo, v0); st128(wsr, lane * 16 + 3072, kvo, v1);
    SB();
    float kn = 0.f, qn = 0.f;
    constexpr float QS = 0.18898223650461362f * 1.4426950408889634f;
#pragma unroll
    for (int r = 0; r < 16; ++r) { kn = fmaf(accK[r], accK[r], kn); accQ[r] *= QS; qn = fmaf(accQ[r], accQ[r], qn); }
#pragma unroll
    for (int j = 0; j < 4; ++j) { q0[j] = pk2(accQ[2 * j], accQ[2 * j + 1]); q1[j] = pk2(accQ[8 + 2 * j], accQ[9 + 2 * j]); }
    *(LAS u32x4*)(lb + L_Q + wave * 2048 + lane * 16) = q0; *(LAS u32x4*)(lb + L_Q + wave * 2048 + 1024 + lane * 16) = q1;
    qn = xhalf_sum(qn); kn = wave_max(xhalf_sum(kn));
    if (!hi) *(LAS float*)(lb + L_QN + (wave * 32 + l31) * 4) = qn;
    WAIT_VM(0);
    if (lane == 0) flag64(ws + OFF_KVG + (ah * 64 + ai * 4 + wave) * 128, __float_as_uint(kn), tag);
    if (b < 42) lds_wait(lb + L_SYNC + 32, 1);
    if (lane == 0 && __hip_atomic_fetch_add((LAS u32*)(lb + L_SYNC + 44), 1u, __ATOMIC_RELAXED, __HIP_MEMORY_SCOPE_WORKGROUP) == 3u) flag32(ws + OFF_PREPF + b * 128, tag);
    STAMPL(6, "kv flagged");
  } else {
    if (wave == 4) {
      u64 gv[8];
      __builtin_amdgcn_s_setprio(3);
#pragma nounroll
      for (int i = 0; i < SPIN_CAP; ++i) {
        bool ok = true;
#pragma unroll
        for (int n = 0; n < 7; ++n) gv[n] = poll64(ws + OFF_MODG + (64 * n + lane) * 8);
        gv[7] = poll64(ws + OFF_MODG + (672 + 224 * hi + 32 * kg + l31) * 8);
#pragma unroll
        for (int n = 0; n < 8; ++n) ok = ok && (u32)(gv[n] >> 32) == tag;
        if (__all(ok)) break;
        __builtin_amdgcn_s_sleep(POLL_SLEEP);
      }
      CFENCE();
      __builtin_amdgcn_s_setprio(0);
      STAMPL(2, "mod seen");
      *(LAS u32*)(lb + L_G2 + lane * 4) = hi ? pk2(1.f + bflo((u32)gv[7]), 1.f + bfhi((u32)gv[7])) : (u32)gv[7];
      WAIT_LGKM();
      lds_st(lb + L_SYNC + 36, 1u);

      float lo[7], hi2[7], mxv = 0.f;
#pragma unroll
      for (int n = 0; n < 7; ++n) {
        u32 v = (u32)gv[n];
        if (64 * n + lane >= 224) v = pk2(1.f + bflo(v), 1.f + bfhi(v));
        lo[n] = bflo(v); hi2[n] = bfhi(v); mxv = fmaxf(mxv, fmaxf(fabsf(lo[n]), fabsf(hi2[n])));
      }
      const u32 ex = max((int)(__float_as_uint(wave_max(mxv)) >> 23) - (127 + 14), 0);
      const float scl = __uint_as_float((127u - min(ex, 114u)) << 23);
#pragma unroll
      for (int n = 0; n < 7; ++n) {
        u32 o; asm("v_cvt_pk_f16_f32 %0, %1, %2" : "=v"(o) : "v"(lo[n] * scl), "v"(hi2[n] * scl));
        const int g = 64 * n + lane;
        *(LAS u32*)(lb + L_GAM + (g >= 224 ? g - 224 : g + 224) * 4) = o;
      }
      lds_st(lb + L_SYNC + 28, (127u + min(ex, 114u)) << 23);
      WAIT_LGKM();
      if (lane == 0) __hip_atomic_fetch_add((LAS u32*)(lb + L_SYNC + 4), 1u, __ATOMIC_RELAXED, __HIP_MEMORY_SCOPE_WORKGROUP);
    } else {
      if (wave == 5) { wait_flags(ws + OFF_WQF + (ah * 64 + min(lane, 41)) * 128, tag); STAMPL(4, "W seen"); if (GATE) lds_wait(lb + L_SYNC + 36, 1); lds_st(lb + L_SYNC + 48, 1u); }
      else lds_wait(lb + L_SYNC + 48, 1);

      const char* wsrc = ws + OFF_WQT + (size_t)ah * 84 * 1024 + (wave - 5) * 4096;
#define WDMA(n) dma16x4(wsrc + (n) * 12288, lane * 16, lb + (wave - 5) * 4096 + (n) * 12288)
      WDMA(0); WDMA(1);
      WAIT_VM(4);
      if (lane == 0) __hip_atomic_fetch_add((LAS u32*)(lb + L_SYNC + 4), 1u, __ATOMIC_RELAXED, __HIP_MEMORY_SCOPE_WORKGROUP);
      WDMA(2); WDMA(3); WDMA(4);
      WAIT_VM(4);
      if (lane == 0) __hip_atomic_fetch_add((LAS u32*)(lb + L_SYNC + 8), 1u, __ATOMIC_RELAXED, __HIP_MEMORY_SCOPE_WORKGROUP);
      WDMA(5); WDMA(6);
      WAIT_VM(0);
      if (lane == 0) __hip_atomic_fetch_add((LAS u32*)(lb + L_SYNC + 24), 1u, __ATOMIC_RELAXED, __HIP_MEMORY_SCOPE_WORKGROUP);
      STAMPL(9, "W landed");
    }
  }
  BAR();
  STAMPL(11, "B3");

  const int ort = 8 * xg + (slot >> 2), ocq = slot & 3;
  const int gi = ai, gfj = ah;
  u32x4 wo_f[16], wgu[28], xres, gaA, gaB;
  {
    u32x4 qf[4][2];
#pragma unroll
    for (int t = 0; t < 4; ++t) { qf[t][0] = lds128(lb + L_Q + t * 2048 + lane * 16); qf[t][1] = lds128(lb + L_Q + t * 2048 + 1024 + lane * 16); }
    float qn2[4];
#pragma unroll
    for (int t = 0; t < 4; ++t) qn2[t] = lds32f(lb + L_QN + (t * 32 + l31) * 4);
    WAIT_LGKM();
    if (lane == 0) __hip_atomic_fetch_add((LAS u32*)(lb + L_SYNC + 40), 1u, __ATOMIC_RELAXED, __HIP_MEMORY_SCOPE_WORKGROUP);
    if (wave == 3) {
      __builtin_amdgcn_s_sleep(KV_SLEEP);
      const float km = wave_max(__uint_as_float(wait_gran(ws + OFF_KVG + (ah * 64 + lane) * 128, tag)));
      lds_st(lb + L_SYNC + 60, __float_as_uint(km));
      WAIT_LGKM();
      lds_st(lb + L_SYNC + 56, 1u);
    } else lds_wait(lb + L_SYNC + 56, 1);
    const float kmax2 = __uint_as_float(lds_ld(lb + L_SYNC + 60));
    STAMPL(12, "kv flags seen");
    const u32 kvb = OFF_KV + (u32)(ah * 64 + 8 * wave) * 4096;
    u32x4 kf[2][2], vf[2][2];
    const u32 kvl = lane * 16;
#define LDKV(D, kt, off) asm volatile("buffer_load_dwordx4 %0, %2, %3, %4 offen offset:" #off " sc1\n\tbuffer_load_dwordx4 %1, %2, %3, %4 offen offset:" #off "+1024 sc1" \
      : "=&v"(D[(kt) & 1][0]), "=&v"(D[(kt) & 1][1]) : "v"(kvl), "s"(wsr), "s"(kvb + (kt) * 4096) : "memory")
#define WAITN(n) asm volatile("s_waitcnt vmcnt(%0)" :: "i"(n) : "memory")
#define PINKV(D, kt) asm volatile("" : "+v"(D[(kt) & 1][0]), "+v"(D[(kt) & 1][1]))
#define WAITKV(D, kt, n) do { WAITN(n); SB(); PINKV(D, kt); } while (0)
#define WAITKV2(D, kt, n0, n1) do { if (wave < 4) WAITN(n0); else WAITN(n1); SB(); PINKV(D, kt); } while (0)
    asm volatile("s_nop 4");
    LDKV(kf, 0, 0); LDKV(vf, 0, 2048); LDKV(kf, 1, 0);
    float sh[4]; bool safe = true;
#pragma unroll
    for (int t = 0; t < 4; ++t) { sh[t] = 1.02f * __builtin_amdgcn_sqrtf(qn2[t] * kmax2); safe = safe && (sh[t] <= 48.f); }
    if (!__all(safe)) {
      BAR();
      float mx[4] = {-INFINITY, -INFINITY, -INFINITY, -INFINITY};
#pragma unroll 1
      for (int kt = 0; kt < 8; ++kt) {
        const u32x4 ka = ld128(wsr, lane * 16, kvb + kt * 4096), kb = ld128(wsr, lane * 16 + 1024, kvb + kt * 4096);
#pragma unroll
        for (int t = 0; t < 4; ++t) {
          f32x16 s = mfma32(ka, qf[t][0], (f32x16){}); s = mfma32(kb, qf[t][1], s);
#pragma unroll
          for (int r = 0; r < 16; ++r) mx[t] = fmaxf(mx[t], s[r]);
        }
      }
#pragma unroll
      for (int t = 0; t < 4; ++t) { const float m = xhalf_max(mx[t]); if (!hi) *(LAS float*)(lb + L_MX + (wave * 128 + t * 32 + l31) * 4) = m; }
      BAR();
#pragma unroll
      for (int t = 0; t < 4; ++t) {
        float m = -INFINITY;
#pragma unroll
        for (int w = 0; w < 8; ++w) m = fmaxf(m, lds32f(lb + L_MX + (w * 128 + t * 32 + l31) * 4));
        sh[t] = m;
      }
    }
    if (hi) {
#pragma unroll
      for (int t = 0; t < 4; ++t) {
        const float a = rbf(-sh[t]), r1 = -sh[t] - a, c = rbf(r1);
        qf[t][1][2] = pk2(a, c); qf[t][1][3] = pk2(r1 - c, 0.f);
      }
    }
    f32x16 o[4] = {(f32x16){}, (f32x16){}, (f32x16){}, (f32x16){}};
    WAITKV(kf, 0, 4);
    f32x16 sc = mfma32(kf[0][0], qf[0][0], (f32x16){}); sc = mfma32(kf[0][1], qf[0][1], sc);
    u32x4 p0, p1;
#define EXP2(v) __builtin_amdgcn_exp2f(v)
    float ec, ed;
#define GRP(r, PREV, MF, CUR) do { float ea = EXP2(sc[r]), eb = EXP2(sc[(r) + 1]); asm volatile("" : "+v"(ea), "+v"(eb)); PREV; SB(); MF; SB(); \
      ec = EXP2(sc[(r) + 2]); ed = EXP2(sc[(r) + 3]); asm volatile("" : "+v"(ec), "+v"(ed)); { u32 pa = pk2(ea, eb); asm volatile("" : "+v"(pa)); CUR = pa; } SB(); } while (0)
#define PKP(D) do { u32 pb = pk2(ec, ed); asm volatile("" : "+v"(pb)); D = pb; } while (0)
    u32 pwb = lb + wave * 8192 + lane * 16, lwb = lb + L_LP + wave * 512 + l31 * 4; asm volatile("" : "+v"(pwb), "+v"(lwb));
#define WRP(t) do { _Pragma("unroll") for (int g = 0; g < 2; ++g) *(LAS u32x4*)(pwb + ((t) * 2 + g) * 1024) = (u32x4){pk2(o[t][8 * g], o[t][8 * g + 1]), pk2(o[t][8 * g + 2], o[t][8 * g + 3]), pk2(o[t][8 * g + 4], o[t][8 * g + 5]), pk2(o[t][8 * g + 6], o[t][8 * g + 7])}; \
      if (hi) *(LAS float*)(lwb + (t) * 128) = o[t][12]; } while (0)
    const int w6 = min(wave, 6);
    const u32 wo_base = OFF_WOT + (u32)(7 * ocq + w6) * 16384, wg_base = OFF_WGU + (u32)(7 * gfj + w6) * 28672;
    auto nslot = [](int kt, int t, bool) constexpr { const int sl = 4 * (kt - 2) + t;
      return (t == 0 && kt < 7 ? 2 : 0) + (t == 2 && kt < 6 ? 2 : 0) + (kt >= 2 ? (sl == 0) + (sl < 16) + (sl >= 6 && sl < 18) + (sl == 18) : 0); };
    auto nafter = [&](int k0, int t0, int k1, int t1, bool young) constexpr { int n = -2; for (int i = 4 * k0 + t0; i <= 4 * k1 + t1; ++i) n += nslot(i >> 2, i & 3, young); return n; };
#pragma unroll
    for (int kt = 0; kt < 8; ++kt) {
      if (kt == 0) __builtin_amdgcn_s_setprio(3);
      if (kt == 2) __builtin_amdgcn_s_setprio(2);
      if (kt == 4) __builtin_amdgcn_s_setprio(1);
      if (kt == 6 && wave < 4) __builtin_amdgcn_s_setprio(0);
      if (kt == 7) __builtin_amdgcn_s_setprio(0);
#pragma unroll
      for (int t = 0; t < 4; ++t) {
        f32x16 sn;
        const bool more = t < 3 || kt < 7, first = t == 0 && kt == 0;
        const int kn = t < 3 ? (kt & 1) : ((kt + 1) & 1), tn = (t + 1) & 3;
        if (t == 3 && kt < 7) {
          if (kt == 0) WAITKV(kf, 1, nafter(0, 0, 0, 2, false) + 2);
          else if (kt == 1) WAITKV(kf, 2, nafter(0, 2, 1, 2, false));
          else WAITKV(kf, kt + 1, nafter(kt - 1, 2, kt, 2, false));
        }
        GRP(0, if (!first) PKP(p1[3]), if (more) sn = mfma32(kf[kn][0], qf[tn][0], (f32x16){}), p0[0]);
        GRP(4, PKP(p0[1]), if (more) sn = mfma32(kf[kn][1], qf[tn][1], sn), p0[2]);
        GRP(8, PKP(p0[3]), if (t > 0) o[t - 1] = mfma32(vf[kt & 1][1], p1, o[t - 1]); else if (kt > 0) o[3] = mfma32(vf[(kt + 1) & 1][1], p1, o[3]), p1[0]);
        if (t == 0 && kt < 7) LDKV(vf, kt + 1, 2048);
        if (t == 2 && kt < 6) LDKV(kf, kt + 2, 0);
        if (kt == 1 && t == 0 && wave == 3) {
#pragma nounroll
          for (int i = 0; i < SPIN_CAP; ++i) {
            const u32 a = poll32(ws + OFF_PREPF + lane * 128), c = poll32(ws + OFF_PREPF + (64 + lane) * 128), d = poll32(ws + OFF_PREPF + (128 + lane) * 128), e = poll32(ws + OFF_PREPF + (192 + lane) * 128);
            if (__all(a == tag && c == tag && d == tag && e == tag)) break;
            __builtin_amdgcn_s_sleep(8);
          }
          CFENCE();
          if (lane == 0) __hip_atomic_fetch_add((LAS u32*)(lb + L_SYNC + 40), 1u, __ATOMIC_RELAXED, __HIP_MEMORY_SCOPE_WORKGROUP);
        }
        if (t == 0) {
        }
        if (kt >= 2) {
          const int sl = 4 * (kt - 2) + t;
          if (sl == 0) {
            lds_wait(lb + L_SYNC + 40, 9);
            dma4_touch(ws + wg_base + 12288, ((slot & 3) * 32 + l31) * 128 + hi * 64, lb + L_ST + w6 * 256);
          }
          if (sl == 18) dma4_touch(ws + OFF_WDT + (size_t)((slot & 15) % 7) * 229376 + ((slot >> 4) * 2 + ((slot & 15) >= 7)) * 57344, (w6 * 64 + lane) * 128, lb + L_ST + w6 * 256);
          if (sl < 10) wo_f[sl] = ld128(wsr, lane * 16, wo_base + sl * 1024);
          else if (sl < 16) wgu[sl - 10] = ld128(wsr, lane * 16, wg_base + (sl - 10) * 1024);
          if (sl >= 6 && sl < 18) dma16<true>(ws + (sl < 12 ? wo_base + (4 + sl) * 1024 : wg_base + (sl - 6) * 1024), lane * 16, lb + L_PARK + w6 * 12288 + (sl - 6) * 1024);
        }
        if (t == 0) {
          if (kt == 0) WAITKV(vf, 0, nafter(0, 0, 0, 0, false) + 4);
          else if (kt == 1) WAITKV(vf, 1, nafter(0, 0, 1, 0, false));
          else WAITKV(vf, kt, nafter(kt - 1, 0, kt, 0, false));
        }
        SB();
        GRP(12, PKP(p1[1]), o[t] = mfma32(vf[kt & 1][0], p0, o[t]), p1[2]);
        if (kt == 7 && t > 0) { WRP(t - 1); SB(); }
        if (more) sc = sn;
      }
    }
    PKP(p1[3]);
    o[3] = mfma32(vf[1][1], p1, o[3]);
    WRP(3);
    STAMPL(13, "attn done");
    BAR();
    const int qt = wave >> 1, sf = wave & 1;
    u32x4 pa[8]; float pl[8];
    {
      u32 base = lb + qt * 2048 + sf * 1024 + lane * 16, lbase = lb + L_LP + qt * 128 + l31 * 4;
      asm volatile("" : "+v"(base), "+v"(lbase));
#pragma unroll
      for (int w = 0; w < 8; ++w) { pa[w] = lds128(base + w * 8192); pl[w] = lds32f(lbase + w * 512); }
      SB();
    }
    float a8[8]; u32 sel0 = 0x00003F80u, sel1 = 0x3F800000u; asm volatile("" : "+v"(sel0), "+v"(sel1));
#pragma unroll
    for (int j = 0; j < 4; ++j) {
      a8[2 * j] = bflo(pa[0][j]); a8[2 * j + 1] = bfhi(pa[0][j]);
#pragma unroll
      for (int w = 1; w < 8; ++w) { a8[2 * j] = dot2(pa[w][j], sel0, a8[2 * j]); a8[2 * j + 1] = dot2(pa[w][j], sel1, a8[2 * j + 1]); }
    }
    const float l = ((pl[0] + pl[4]) + (pl[1] + pl[5])) + ((pl[2] + pl[6]) + (pl[3] + pl[7]));
    const float rl = __builtin_amdgcn_rcpf(l);
    const u32x4 of = {pk2(a8[0] * rl, a8[1] * rl), pk2(a8[2] * rl, a8[3] * rl), pk2(a8[4] * rl, a8[5] * rl), pk2(a8[6] * rl, a8[7] * rl)};
    st128(wsr, (l31 >> 4) * 1024 + ((l31 & 15) + 16 * (2 * sf + hi)) * 16, OFF_OB + (u32)((4 * ai + qt) * 16 + ah) * 2048, of);
    WAIT_VM(0);
    if (lane == 0) flag32(ws + OFF_OF + (((4 * ai + qt) * 16 + ah) * 2 + sf) * 128, tag);
    BAR();
    STAMPL(14, "O flagged");
  }

  u32 uvp[14];
  const int otok = 16 * ((lane >> 4) & 1) + (lane & 15), ofe = 112 * ocq + 16 * min(wave, 6) + 8 * hi;
  if (wave < 7) {
    xres = *(const u32x4*)(x + (size_t)(32 * ort + otok) * 448 + ofe);
    gaA = ld128(wsr, ofe * 4, OFF_MODG + 448 * 8); gaB = ld128(wsr, ofe * 4 + 16, OFF_MODG + 448 * 8);
#pragma unroll
    for (int k = 0; k < 14; ++k) uvp[k] = ld32(wsr, lane * 4, OFF_UV + (u32)(k * 112 + 7 * gfj + wave) * 256);
#pragma unroll
    for (int s = 12; s < 26; ++s) wgu[s] = ld128(wsr, lane * 16 + (s & 3) * 1024, OFF_WGU + (u32)((7 * gfj + wave) * 28 + (s & ~3)) * 1024);
#pragma unroll
    for (int j = 0; j < 6; ++j) wo_f[10 + j] = lds128(lb + L_PARK + wave * 12288 + j * 1024 + lane * 16);
#pragma unroll
    for (int j = 6; j < 12; ++j) wgu[j] = lds128(lb + L_PARK + wave * 12288 + j * 1024 + lane * 16);
    lds_wait(lb + L_SYNC + 16, 1);
  } else {
    wait_flags(ws + OFF_OF + (ort * 32 + l31) * 128, tag);
    lds_st(lb + L_SYNC + 16, 1u);
  }
  STAMPL(15, "O flags seen");
  {
    const char* osrc = ws + OFF_OB + (size_t)(ort * 16 + 2 * wave) * 2048;
    dma16x4(osrc, lane * 16, lb + 4 * wave * 1024);
  }
  if (wave < 7) {
    float uvs = 0.f;
#pragma unroll
    for (int k = 0; k < 14; ++k) uvs += __uint_as_float(uvp[k]);
    *(LAS float*)(lb + L_UV + wave * 256 + lane * 4) = uvs;
  }
  __builtin_amdgcn_s_waitcnt(0x0F70);
  CFENCE();
  BAR();
  STAMPL(16, "O landed");
  if (wave < 7) {
    f32x4 y0 = {0.f, 0.f, 0.f, 0.f}, y1 = y0;
    u32x4 oa[10];
#pragma unroll
    for (int i = 0; i < 8; ++i) oa[i] = lds128(lb + i * 1024 + lane * 16);
#pragma unroll
    for (int h = 0; h < 16; ++h) {
      if (h < 12) { oa[(2 * h + 8) % 10] = lds128(lb + (2 * h + 8) * 1024 + lane * 16); oa[(2 * h + 9) % 10] = lds128(lb + (2 * h + 9) * 1024 + lane * 16); }
      SB(); y0 = mfma16(wo_f[h], oa[(2 * h) % 10], y0); y1 = mfma16(wo_f[h], oa[(2 * h + 1) % 10], y1); SB();
    }
    float yv[8];
#pragma unroll
    for (int r = 0; r < 4; ++r) { auto sw = __builtin_amdgcn_permlane16_swap(__float_as_uint(y0[r]), __float_as_uint(y1[r]), false, false); yv[r] = __uint_as_float(sw[0]); yv[4 + r] = __uint_as_float(sw[1]); }
    const u32 gw[4] = {gaA[0], gaA[2], gaB[0], gaB[2]};
    u32x4 x2p; float s1 = 0.f, s2 = 0.f;
#pragma unroll
    for (int j = 0; j < 4; ++j) {
      x2p[j] = pk2(fmaf(bflo(gw[j]), yv[2 * j], bflo(xres[j])), fmaf(bfhi(gw[j]), yv[2 * j + 1], bfhi(xres[j])));
      s1 = dot2(x2p[j], 0x3F803F80u, s1); s2 = dot2(x2p[j], x2p[j], s2);
    }
    const u32 fo = (u32)(ort * 28 + 7 * ocq + wave) * 1024;
    st128(wsr, (otok + 32 * hi) * 16, OFF_X2 + fo, x2p);
    s1 = xhalf_sum(s1); s2 = xhalf_sum(s2);
    *(LAS float*)(lb + L_ST + wave * 256 + (hi * 32 + otok) * 4) = hi ? s2 : s1;
    WAIT_VM(0);
  }
  BAR();
  if (wave == 7) {
    float t = 0.f;
#pragma unroll
    for (int w = 0; w < 7; ++w) t += lds32f(lb + L_ST + w * 256 + lane * 4);
    const u32 nv = __builtin_amdgcn_update_dpp(0u, __float_as_uint(t), 0xB1, 0xf, 0xf, false);
    st128(wsr, !(lane & 1) ? (u32)lane * 8 : 0x80000000u, OFF_X2G + (u32)(ort * 4 + ocq) * 512, (u32x4){__float_as_uint(t), tag, nv, tag});
  }
  if (wave < 7) {
#pragma unroll
    for (int s = 26; s < 28; ++s) wgu[s] = ld128(wsr, lane * 16 + (s & 3) * 1024, OFF_WGU + (u32)((7 * gfj + wave) * 28 + (s & ~3)) * 1024);
  }
  STAMPL(17, "x2 flagged");

  const int dsl = slot & 15, dblk = 2 * xg + (slot >> 4), drt = 4 * dblk + dsl / 7, dc = dsl % 7;
  const bool dact = dsl < 14;
  u32x4 wdf[28], x2r, gbA, gbB;
  if (wave == 7) {
    u64 g[16]; const char* gp = ws + OFF_X2G + (16 * gi * 64 + lane) * 8;
#pragma nounroll
    for (int i = 0; i < SPIN_CAP; ++i) {
      bool ok = true;
#pragma unroll
      for (int p = 0; p < 16; ++p) g[p] = poll64(gp + p * 512);
#pragma unroll
      for (int p = 0; p < 16; ++p) ok = ok && (u32)(g[p] >> 32) == tag;
      if (__all(ok)) break;
      __builtin_amdgcn_s_sleep(1);
    }
    CFENCE();
#pragma unroll
    for (int p = 0; p < 16; ++p) *(LAS u32*)(lb + L_SP2 + p * 256 + lane * 4) = (u32)g[p];
  }
  BAR();
  STAMPL(18, "x2 flags seen");
  const char* asrc = ws + OFF_X2 + (size_t)(4 * gi) * 28 * 1024;
#define ADMA(n) do { const int g_ = min(wave + 8 * (n), 27); dma16x4(asrc + g_ * 4096, lane * 16, lb + g_ * 4096); } while (0)
  ADMA(0);
  WAIT_VM(0);
  BAR();
  STAMPL(19, "A0 landed");
  const int dm = wave >> 2, dn = (wave >> 1) & 1, dgp = wave & 1;
  const u32 wdo = OFF_WDT + (u32)(2 * dc * 112 + 14 * wave) * 1024;
  const u32 pk_free = lb + 114688 + wave * 4096, pk_a = lb + wave * 3072;
#define PARKA(i) ((i) < 4 ? pk_free + (i) * 1024 : pk_a + ((i) < 7 ? (i) - 4 : 28 + (i) - 7) * 1024)
#define PARKD(i) dma16<true>(ws + wdo + (((i) & 1) * 112 + 9 + ((i) >> 1)) * 1024, lane * 16, PARKA(i))
  if (wave < 7) {
    u32 sb = lb + L_SP2 + l31 * 4, ub = lb + L_UV + wave * 256 + hi * 16; asm volatile("" : "+v"(sb), "+v"(ub));
    u32 ab = lb + lane * 16, ab2 = ab + 57344; asm volatile("" : "+v"(ab), "+v"(ab2));
    constexpr int RD = GU_RING;
    f32x16 acc[2]; u32x4 af[RD + 1]; f32x4 uv[4]; float st[8], rs = 0.f, nm = 0.f, hv[8];
#define EP_RD(T) do { _Pragma("unroll") for (int c = 0; c < 4; ++c) { st[c] = lds32f(sb + ((T) * 4 + c) * 256); st[4 + c] = lds32f(sb + ((T) * 4 + c) * 256 + 128); } } while (0)
#define EP_UV(g) do { uv[0] = lds128f(ub + 32 * (g)); uv[1] = lds128f(ub + 32 * (g) + 64); uv[2] = lds128f(ub + 32 * (g) + 128); uv[3] = lds128f(ub + 32 * (g) + 192); } while (0)
#define EP_ST() do { float m = ((st[0] + st[1]) + (st[2] + st[3])) * (1.f / 448.f); asm volatile("" : "+v"(m)); const float e2 = (((st[4] + st[5]) + st[6]) + st[7]) * (1.f / 448.f); \
      rs = __builtin_amdgcn_rsqf(fmaxf(e2 - m * m, 0.f) + 1e-5f); nm = -m * rs; } while (0)
    constexpr float K1 = -2.f * 0.7978845608028654f * 1.4426950408889634f, K3 = K1 * 0.044715f;
    float ea[8], eb[8], k1v = K1; asm volatile("" : "+v"(k1v));
#define PIN1(v) asm volatile("" : "+v"(v))
#define EP_STAGE(A, e, j) do { \
      if ((j) == 0) { ea[e] = fma1(nm, uv[0][(e) & 3], uv[2][(e) & 3]); eb[e] = fma1(nm, uv[1][(e) & 3], uv[3][(e) & 3]); PIN1(ea[e]); PIN1(eb[e]); } \
      if ((j) == 1) { ea[e] = fma1(A[e], rs, ea[e]); eb[e] = fma1(A[8 + (e)], rs, eb[e]); PIN1(ea[e]); PIN1(eb[e]); } \
      if ((j) == 2) { hv[e] = ea[e] * ea[e]; PIN1(hv[e]); eb[e] *= ea[e]; PIN1(eb[e]); } \
      if ((j) == 3) { hv[e] = fmaf(hv[e], K3, k1v); PIN1(hv[e]); } \
      if ((j) == 4) { hv[e] *= ea[e]; PIN1(hv[e]); } \
      if ((j) == 5) { hv[e] = __builtin_amdgcn_exp2f(hv[e]); PIN1(hv[e]); } \
      if ((j) == 6) { hv[e] += 1.f; PIN1(hv[e]); } \
      if ((j) == 7) { hv[e] = __builtin_amdgcn_rcpf(hv[e]); PIN1(hv[e]); } \
      if ((j) == 8) { hv[e] *= eb[e]; PIN1(hv[e]); } } while (0)
#define EP_OUT(T) st128(wsr, lane * 16, OFF_HB + (u32)((4 * gi + (T)) * 112 + 7 * gfj + wave) * 1024, (u32x4){pk2(hv[0], hv[1]), pk2(hv[2], hv[3]), pk2(hv[4], hv[5]), pk2(hv[6], hv[7])})
#pragma unroll
    for (int i = 0; i < RD; ++i) af[i] = lds128(ab + i * 1024);
    static_for<0, 112>([&](auto NN) __attribute__((always_inline)) {
      {
        constexpr int n = NN.value, t = n / 28, s = n % 28;
        if (n == 28 - RD + 4) { STAMPL(28, "gu n25"); WAIT_VM(0); BAR();  }
        if constexpr (n + RD < 112) af[(n + RD) % (RD + 1)] = (n + RD < 56) ? lds128(ab + (n + RD) * 1024) : lds128(ab2 + (n + RD - 56) * 1024);
        if (s == 0) __builtin_amdgcn_s_setprio(3 - t);
        if constexpr (n == 0 || n == 3 || n == 6) ADMA(1 + n / 3);
        SB();
        if (s == 0) acc[t & 1] = mfma32(wgu[s], af[n % (RD + 1)], (f32x16){}); else acc[t & 1] = mfma32(wgu[s], af[n % (RD + 1)], acc[t & 1]);
        SB();
        if constexpr (t > 0) {
          if (s == 0) { EP_RD(t - 1); EP_UV(0); }
          if (s == 2) EP_ST();
          static_for<0, 8>([&](auto EE) __attribute__((always_inline)) { constexpr int e = EE.value, j = s - 3 - 2 * e; if constexpr (j >= 0 && j < 9) EP_STAGE(acc[(t - 1) & 1], e, j); });
          if (s == 9) { SB(); EP_UV(1); }
          if (s == 26) EP_OUT(t - 1);
          if (s == 2 && t < 3) asm volatile("s_barrier" ::: "memory");
          if constexpr (t == 1 && s % 3 == 1 && s >= 4 && s < 25) PARKD(s / 3 - 1);
          if constexpr (t == 2 && s % 6 == 4 && s < 18) PARKD(7 + s / 6);
          if (s == 12 && t == 3) {
            WAIT_VM(6);
            if (lane == 0 && __hip_atomic_fetch_add((LAS u32*)(lb + L_SYNC + 68), 1u, __ATOMIC_RELAXED, __HIP_MEMORY_SCOPE_WORKGROUP) == 6u) flag32(ws + OFF_HF + ((gi * 16 + gfj) * 8 + 7) * 128, tag);
          }
          if (s == 27 && t == 1) STAMPL(20, "gu tile1");
          if (s == 27 && t == 2) STAMPL(21, "gu tile2");
          if (s == 27 && t == 3) STAMPL(22, "gu tile3");
          if constexpr (t < 3 ? (s % 6 == 1 && s < 24) : (s % 2 == 1 && s < 20)) { constexpr int f = t < 3 ? 4 * (t - 1) + s / 6 : 8 + s / 2; wdf[f] = ld128(wsr, lane * 16, wdo + (u32)((f & 1) * 112 + (f >> 1)) * 1024); }
          SB();
        }
      }
    });
    asm volatile("s_nop 11" : "+v"(acc[1]));
    EP_RD(3); EP_UV(0); EP_ST();
    {
      f32x4 uw[4];
      uw[0] = lds128f(ub + 32); uw[1] = lds128f(ub + 96); uw[2] = lds128f(ub + 160); uw[3] = lds128f(ub + 224);
      float g8[8], u8[8];
#pragma unroll
      for (int e = 0; e < 8; ++e) {
        const f32x4* W = e < 4 ? uv : uw;
        g8[e] = fma1(acc[1][e], rs, fma1(nm, W[0][e & 3], W[2][e & 3])); u8[e] = fma1(acc[1][8 + e], rs, fma1(nm, W[1][e & 3], W[3][e & 3]));
      }
#pragma unroll
      for (int e = 0; e < 8; ++e) hv[e] = (g8[e] * u8[e]) * __builtin_amdgcn_rcpf(1.f + __builtin_amdgcn_exp2f(g8[e] * fmaf(g8[e] * g8[e], K3, k1v)));
    }
    EP_OUT(3);
    WAIT_VM(0);
    if (lane == 0) flag32(ws + OFF_HF + ((gi * 16 + gfj) * 8 + wave) * 128, tag);
  } else {
    __builtin_amdgcn_s_setprio(3);
    ADMA(1); ADMA(2); ADMA(3);
    WAIT_VM(0); BAR();
#pragma unroll
    for (int f = 0; f < 18; ++f) wdf[f] = ld128(wsr, lane * 16, wdo + (u32)((f & 1) * 112 + (f >> 1)) * 1024);
    asm volatile("s_barrier" ::: "memory");
#pragma unroll
    for (int i = 0; i < 7; ++i) PARKD(i);
    asm volatile("s_barrier" ::: "memory");
#pragma unroll
    for (int i = 7; i < 10; ++i) PARKD(i);
    if (dact) {

      lds_wait(lb + L_SYNC + 68, 7);

      wait_flags(ws + OFF_HF + ((dblk * 16 + (lane & 15)) * 8 + 7) * 128, tag);
    }
    WAIT_VM(0);
    __builtin_amdgcn_s_setprio(0);
  }
  STAMPL(23, "h flagged");
  if (dact) {
    BAR();
    constexpr int HR = 9;
    u32x4 hf[HR];
    const int dcol = 64 * dc + 32 * dn + 16 * dgp + 8 * hi;
#define LDH(i) hf[(i) % HR] = ld128(wsr, lane * 16 + (((i) % 14) & 3) * 1024, OFF_HB + (u32)((drt + 2 * ((i) / 14)) * 112 + 14 * wave + (((i) % 14) & ~3)) * 1024)
#pragma unroll
    for (int i = 0; i < 4; ++i) { LDH(i); SB(); }
#pragma unroll
    for (int f = 18; f < 28; ++f) wdf[f] = lds128(PARKA(f - 18) + lane * 16);
    BAR();
#pragma unroll
    for (int i = 4; i < HR - 1; ++i) { LDH(i); SB(); }
    STAMPL(24, "down go");
    f32x16 c0, c1; u32 late0;
    u32 pwb = lb + wave * 16384 + lane * 16; asm volatile("" : "+v"(pwb));
    static_for<0, 28>([&](auto II) __attribute__((always_inline)) {
      constexpr int i = II.value, m = i / 14, j = i % 14;
      if constexpr (i == LATE_PRE) late0 = ld32(wsr, ((lane >> 3) & 1) * 1024 + min(lane & 7, 6) * 128, OFF_HF + (u32)(dblk * 16 + 2 * wave) * 1024);
      if constexpr (i + HR - 1 == 14) {
        if (!__all(late0 == tag)) wait_flags(ws + OFF_HF + ((dblk * 16 + 2 * wave + ((lane >> 3) & 1)) * 8 + min(lane & 7, 6)) * 128, tag);
        CFENCE();
        STAMPL(27, "late seen");
      }
      if constexpr (i + HR - 1 < 28) LDH(i + HR - 1);
      if constexpr (i == 20) {
        gbA = ld128(wsr, dcol * 4, OFF_MODG + 1120 * 8); gbB = ld128(wsr, dcol * 4 + 16, OFF_MODG + 1120 * 8);
        x2r = ld128(wsr, lane * 16, OFF_X2 + (u32)((drt + 2 * dm) * 28 + 4 * dc + 2 * dn + dgp) * 1024);
      }
      SB();
      if constexpr (j == 0) { c0 = mfma32(wdf[0], hf[i % HR], (f32x16){}); c1 = mfma32(wdf[1], hf[i % HR], (f32x16){}); }
      else { c0 = mfma32(wdf[2 * j], hf[i % HR], c0); c1 = mfma32(wdf[2 * j + 1], hf[i % HR], c1); }
      SB();
      if constexpr (j == 13) {
#pragma unroll
        for (int g = 0; g < 4; ++g) {
          *(LAS f32x4*)(pwb + ((2 * m) * 4 + g) * 1024) = (f32x4){c0[4 * g], c0[4 * g + 1], c0[4 * g + 2], c0[4 * g + 3]};
          *(LAS f32x4*)(pwb + ((2 * m + 1) * 4 + g) * 1024) = (f32x4){c1[4 * g], c1[4 * g + 1], c1[4 * g + 2], c1[4 * g + 3]};
        }
      }
    });
    STAMPL(25, "down mfma done");
    BAR();
    f32x4 pa[8], pb[8];
    {
      u32 base = lb + (2 * dm + dn) * 4096 + dgp * 2048 + lane * 16, base2 = base + 65536;
      asm volatile("" : "+v"(base), "+v"(base2));
#pragma unroll
      for (int w = 0; w < 4; ++w) {
        pa[w] = lds128f(base + w * 16384); pb[w] = lds128f(base + w * 16384 + 1024);
        pa[4 + w] = lds128f(base2 + w * 16384); pb[4 + w] = lds128f(base2 + w * 16384 + 1024);
      }
      SB();
    }
    f32x4 a0 = ((pa[0] + pa[4]) + (pa[1] + pa[5])) + ((pa[2] + pa[6]) + (pa[3] + pa[7])), a1 = ((pb[0] + pb[4]) + (pb[1] + pb[5])) + ((pb[2] + pb[6]) + (pb[3] + pb[7]));
    asm volatile("" : "+v"(a0), "+v"(a1));
    float yv[8];
#pragma unroll
    for (int r = 0; r < 4; ++r) { auto sw = __builtin_amdgcn_permlane32_swap(__float_as_uint(a0[r]), __float_as_uint(a1[r]), false, false); yv[r] = __uint_as_float(sw[0]); yv[4 + r] = __uint_as_float(sw[1]); }
    const u32 gw[4] = {gbA[0], gbA[2], gbB[0], gbB[2]};
    u32x4 ov;
#pragma unroll
    for (int j = 0; j < 4; ++j) ov[j] = pk2(fmaf(bflo(gw[j]), yv[2 * j], bflo(x2r[j])), fmaf(bfhi(gw[j]), yv[2 * j + 1], bfhi(x2r[j])));
    *(u32x4*)(out + (size_t)(32 * (drt + 2 * dm) + l31) * 448 + dcol) = ov;
    if (b == 0 && tid == 0) *(GAS u32*)ws = tag;
  }
  STAMPL(26, "end");
}

extern "C" void kernel_launch(void* const* d_in, const int* in_sizes, int n_in, void* d_out, int out_size, void* d_ws, size_t ws_size, hipStream_t stream) {
  fused_block<<<256, 512, 0, stream>>>((const u16*)d_in[0], (const u16*)d_in[1], (const u16*)d_in[2], (const u16*)d_in[4], (const u16*)d_in[5],
                                       (const u16*)d_in[6], (const u16*)d_in[7], (const u16*)d_in[8], (u16*)d_out, (char*)d_ws);
}
```

```cpp
#define PROBE 26
#define PROBE_MUL 0
#define PROBE_W 0
#include <hip/hip_runtime.h>
#include <stdint.h>

typedef unsigned short u16;
typedef unsigned u32;
typedef unsigned long long u64;
typedef __attribute__((ext_vector_type(2))) unsigned u32x2;
typedef __attribute__((ext_vector_type(4))) unsigned u32x4;
typedef __attribute__((ext_vector_type(2))) float f32x2;
typedef __attribute__((ext_vector_type(4))) float f32x4;
typedef __attribute__((ext_vector_type(16))) float f32x16;
typedef __attribute__((ext_vector_type(2))) __bf16 bf16x2;
typedef __attribute__((ext_vector_type(8))) __bf16 bf16x8;
typedef __attribute__((ext_vector_type(4))) short s16x4;
#define DEV __device__ __forceinline__
#define GAS __attribute__((address_space(1)))
#define LAS __attribute__((address_space(3)))
#define RLX_AGENT __ATOMIC_RELAXED, __HIP_MEMORY_SCOPE_AGENT

constexpr u32 OFF_MODG0 = 4096, MODG_REP = 11264;
constexpr u32 OFF_X2G = 131072, OFF_UV = 13467648  , OFF_WQT = 524288, OFF_WOT = 1900544, OFF_WGU = 2359296, OFF_WDT = 5570560;
constexpr u32 OFF_KV = 7176192, OFF_OB = 11370496, OFF_X2 = 15302656, OFF_HB = 17137664, OFF_XH = 24477696;
constexpr u32 OFF_XF = 26312704, OFF_WQF = OFF_XF + 512 * 128, OFF_KVG = OFF_WQF + 1024 * 128, OFF_OF = OFF_KVG + 1024 * 128, OFF_HF = OFF_OF + 2048 * 128, OFF_PREPF = OFF_HF + 2048 * 128, WS_USED = OFF_PREPF + 256 * 128;
constexpr u32 OFF_STAMP = 29360128;
constexpr u32 L_STG = 90112  , L_Q = 131072, L_GAM = 139264, L_QN = 142848;
constexpr u32 L_PARK = 64512 + 1024  ;
constexpr u32 L_SP2 = 151552  , L_MODP = L_SP2, L_MX = L_SP2, L_LP = L_SP2, L_UV = 155648  , L_ST = 157696  , L_G2 = 159488  ;
constexpr u32 L_SYNC = 160000;
constexpr u32 LDS_BYTES = 163840;
constexpr int CODE_BYTES = 42000;
constexpr int SPIN_CAP = 1 << 15;
#define FIRST_POLL 265
#define KV_SLEEP 1
#define MOD_POLL 250
#define NREP 8
#define GATE 0
#define XP0 8
#define PRETOUCH 2
#define NOCODEPF 0
#define TOUCH_AT 0
#define LATE_PRE 5
#define GU_RING 6
#define PREP_START 130
#define POLL_T 0
#define POLL_SLEEP 3

DEV float bflo(u32 w) { return __uint_as_float(w << 16); }
DEV float bfhi(u32 w) { return __uint_as_float(w & 0xffff0000u); }
DEV u32 pk2(float a, float b) { f32x2 v = {a, b}; return __builtin_bit_cast(u32, __builtin_convertvector(v, bf16x2)); }
DEV float rbf(float a) { return bflo(pk2(a, 0.f)); }
DEV float fma1(float a, float b, float c) { float r; asm("v_fma_f32 %0, %1, %2, %3" : "=v"(r) : "v"(a), "v"(b), "v"(c)); return r; }
DEV float dot2(u32 a, u32 b, float c) { return __builtin_amdgcn_fdot2_f32_bf16(__builtin_bit_cast(bf16x2, a), __builtin_bit_cast(bf16x2, b), c, false); }
DEV f32x16 mfma32(u32x4 a, u32x4 b, f32x16 c) { return __builtin_amdgcn_mfma_f32_32x32x16_bf16(__builtin_bit_cast(bf16x8, a), __builtin_bit_cast(bf16x8, b), c, 0, 0, 0); }
DEV f32x4 mfma16(u32x4 a, u32x4 b, f32x4 c) { return __builtin_amdgcn_mfma_f32_16x16x32_bf16(__builtin_bit_cast(bf16x8, a), __builtin_bit_cast(bf16x8, b), c, 0, 0, 0); }
DEV float xhalf_sum(float v) { auto r = __builtin_amdgcn_permlane32_swap(__float_as_uint(v), __float_as_uint(v), false, false); return __uint_as_float(r[0]) + __uint_as_float(r[1]); }
DEV float xhalf_max(float v) { auto r = __builtin_amdgcn_permlane32_swap(__float_as_uint(v), __float_as_uint(v), false, false); return fmaxf(__uint_as_float(r[0]), __uint_as_float(r[1])); }
#define DPPF(v, ctrl) __uint_as_float(__builtin_amdgcn_update_dpp(0u, __float_as_uint(v), ctrl, 0xf, 0xf, false))
DEV float wave_max(float v) {
  v = fmaxf(v, DPPF(v, 0x121)); v = fmaxf(v, DPPF(v, 0x122)); v = fmaxf(v, DPPF(v, 0x124)); v = fmaxf(v, DPPF(v, 0x128));
  auto r = __builtin_amdgcn_permlane16_swap(__float_as_uint(v), __float_as_uint(v), false, false);
  return xhalf_max(fmaxf(__uint_as_float(r[0]), __uint_as_float(r[1])));
}
template <int I> struct IC { static constexpr int value = I; };
template <int I, int N, class F> DEV void static_for(F&& f) { if constexpr (I < N) { f(IC<I>{}); static_for<I + 1, N>(f); } }
#define WAIT_VM(n) asm volatile("s_waitcnt vmcnt(" #n ")" ::: "memory")
#define BAR() asm volatile("s_waitcnt lgkmcnt(0)\n\ts_barrier" ::: "memory")
#define SB() __builtin_amdgcn_sched_barrier(0)
#define CFENCE() asm volatile("" ::: "memory")
template <bool SC1> DEV void dma16(const void* sbase, u32 voff, u32 lds_dst) {
  u32 keep;
  if (SC1) asm volatile("s_mov_b32 %0, m0\n\ts_mov_b32 m0, %3\n\ts_nop 2\n\tglobal_load_lds_dwordx4 %1, %2 sc1\n\ts_mov_b32 m0, %0" : "=&s"(keep) : "v"(voff), "s"(sbase), "s"(lds_dst) : "memory");
  else asm volatile("s_mov_b32 %0, m0\n\ts_mov_b32 m0, %3\n\ts_nop 2\n\tglobal_load_lds_dwordx4 %1, %2\n\ts_mov_b32 m0, %0" : "=&s"(keep) : "v"(voff), "s"(sbase), "s"(lds_dst) : "memory");
}
DEV void dma16x4(const void* sbase, u32 voff, u32 lds_dst) {
  u32 keep;
  asm volatile("s_mov_b32 %0, m0\n\ts_mov_b32 m0, %3\n\ts_nop 2\n\tglobal_load_lds_dwordx4 %1, %2 sc1\n\tglobal_load_lds_dwordx4 %1, %2 offset:1024 sc1\n\t"
               "global_load_lds_dwordx4 %1, %2 offset:2048 sc1\n\tglobal_load_lds_dwordx4 %1, %2 offset:3072 sc1\n\ts_mov_b32 m0, %0" : "=&s"(keep) : "v"(voff), "s"(sbase), "s"(lds_dst) : "memory");
}
DEV void dma4_touch(const void* sbase, u32 voff, u32 lds_dst) {
  u32 keep;
  asm volatile("s_mov_b32 %0, m0\n\ts_mov_b32 m0, %3\n\ts_nop 2\n\tglobal_load_lds_dword %1, %2 sc1\n\ts_mov_b32 m0, %0" : "=&s"(keep) : "v"(voff), "s"(sbase), "s"(lds_dst) : "memory");
}
DEV void dma4_plain(const void* sbase, u32 voff, u32 lds_dst) {
  u32 keep;
  asm volatile("s_mov_b32 %0, m0\n\ts_mov_b32 m0, %3\n\ts_nop 2\n\tglobal_load_lds_dword %1, %2\n\ts_mov_b32 m0, %0" : "=&s"(keep) : "v"(voff), "s"(sbase), "s"(lds_dst) : "memory");
}
DEV u32x4 lds128(u32 a) { return *(const LAS u32x4*)a; }
DEV f32x4 lds128f(u32 a) { return *(const LAS f32x4*)a; }
DEV float lds32f(u32 a) { return *(const LAS float*)a; }
DEV u32x2 ldstr(u32 a) { return __builtin_bit_cast(u32x2, __builtin_amdgcn_ds_read_tr16_b64_v4i16((LAS s16x4*)a)); }
DEV u32x4 cat(u32x2 a, u32x2 b) { return (u32x4){a.x, a.y, b.x, b.y}; }
typedef __amdgpu_buffer_rsrc_t rsrc_t;
DEV u32x4 ld128(rsrc_t r, u32 voff, u32 soff) { return __builtin_amdgcn_raw_buffer_load_b128(r, voff, soff, 16); }
DEV u32 ld32(rsrc_t r, u32 voff, u32 soff) { return __builtin_amdgcn_raw_buffer_load_b32(r, voff, soff, 16); }
DEV void st128(rsrc_t r, u32 voff, u32 soff, u32x4 v) {
  __builtin_amdgcn_raw_buffer_store_b128(v, r, voff, soff, 16);
  asm volatile("s_nop 1" :: "v"(v) : "memory");
}
DEV void st32(rsrc_t r, u32 voff, u32 soff, u32 v) { __builtin_amdgcn_raw_buffer_store_b32(v, r, voff, soff, 16); }
DEV u32 poll32(const char* p) { return __hip_atomic_load((GAS u32*)p, RLX_AGENT); }
DEV u64 poll64(const char* p) { return __hip_atomic_load((GAS u64*)p, RLX_AGENT); }
DEV void flag32(char* p, u32 v) { __hip_atomic_store((GAS u32*)p, v, RLX_AGENT); }
DEV void flag64(char* p, u32 val, u32 tag) { __hip_atomic_store((GAS u64*)p, ((u64)tag << 32) | val, RLX_AGENT); }
DEV void wait_flags(const char* p, u32 tag) {
#pragma nounroll
  for (int i = 0; i < SPIN_CAP; ++i) { if (__all(poll32(p) == tag)) break; __builtin_amdgcn_s_sleep(POLL_SLEEP); }
  CFENCE();
}
DEV u32 wait_gran(const char* p, u32 tag) {
  u64 g = 0;
#pragma nounroll
  for (int i = 0; i < SPIN_CAP; ++i) { g = poll64(p); if (__all((u32)(g >> 32) == tag)) break; __builtin_amdgcn_s_sleep(POLL_SLEEP); }
  CFENCE();
  return (u32)g;
}
DEV u32 lds_ld(u32 a) { return __hip_atomic_load((LAS u32*)a, __ATOMIC_RELAXED, __HIP_MEMORY_SCOPE_WORKGROUP); }
DEV void lds_st(u32 a, u32 v) { __hip_atomic_store((LAS u32*)a, v, __ATOMIC_RELAXED, __HIP_MEMORY_SCOPE_WORKGROUP); }
DEV u32 lds_inc(u32 a) { return __builtin_amdgcn_readfirstlane(__hip_atomic_fetch_add((LAS u32*)a, 1u, __ATOMIC_RELAXED, __HIP_MEMORY_SCOPE_WORKGROUP)); }
DEV void lds_wait(u32 a, u32 want) {
#pragma nounroll
  for (int i = 0; i < (SPIN_CAP << 6); ++i) { if (__builtin_amdgcn_readfirstlane(lds_ld(a)) >= want) break; __builtin_amdgcn_s_sleep(1); }
  CFENCE();
}
#define WAIT_LGKM() asm volatile("s_waitcnt lgkmcnt(0)" ::: "memory")
#define STAMP(i) do {} while (0)
#undef STAMP
#define STAMP(i) do { if (((i) == 0 || (i) == PROBE) && wave == PROBE_W && lane == 0) __hip_atomic_store((GAS u64*)(ws + OFF_STAMP) + b * 2 + ((i) != 0), (u64)__builtin_amdgcn_s_memrealtime(), RLX_AGENT); } while (0)
__global__ void probe_spin(char* ws, int mul) {
  const u64* p = (const u64*)(ws + OFF_STAMP);
  u64 a = ~0ull, c = 0;
  for (int i = 0; i < 4; ++i) { const u64 a2 = p[(threadIdx.x + 64 * i) * 2], c2 = p[(threadIdx.x + 64 * i) * 2 + 1]; a = a2 < a ? a2 : a; if (c2 - a2 < 100000ull) c = c2 > c ? c2 : c; }
  for (int o = 32; o; o >>= 1) { const u64 a2 = __shfl_xor(a, o), c2 = __shfl_xor(c, o); a = a2 < a ? a2 : a; c = c2 > c ? c2 : c; }
  u64 d = (c - a) * mul; if (d > 20000) d = 20000;
  const u64 t0 = __builtin_amdgcn_s_memrealtime();
  while (__builtin_amdgcn_s_memrealtime() - t0 < d) __builtin_amdgcn_s_sleep(1);
}
#define STAMPL(i, s) STAMP(i)

__global__ __launch_bounds__(512, 2) void fused_block(const u16* __restrict__ x, const u16* __restrict__ wqkv, const u16* __restrict__ wo,
                                                      const u16* __restrict__ wg, const u16* __restrict__ wu, const u16* __restrict__ wd,
                                                      const u16* __restrict__ cvec, const u16* __restrict__ wmod, u16* __restrict__ out, char* ws) {
  __shared__ __attribute__((aligned(16))) char smem[LDS_BYTES];
  const int b = blockIdx.x, tid = threadIdx.x, lane = tid & 63;
  const int wave = __builtin_amdgcn_readfirstlane(tid >> 6);
  const int l31 = lane & 31, hi = lane >> 5;
  const int xg = b & 7, slot = b >> 3;
  const int ai = 4 * (xg >> 1) + (slot & 3), ah = 8 * (xg & 1) + (slot >> 2);
  const u32 lb = (u32)(uintptr_t)(LAS char*)smem;
  const rsrc_t wsr = __builtin_amdgcn_make_buffer_rsrc(ws, 0, WS_USED, 0x00020000);
  STAMP(0);
  {
    u64 pc; asm volatile("s_getpc_b64 %0" : "=s"(pc));
    if (wave == 0 && !NOCODEPF) {
      const u32 off = min((u32)(slot * 1408 + min(lane, 10) * 128), (u32)CODE_BYTES);
      u32 keep;
      asm volatile("s_mov_b32 %0, m0\n\ts_mov_b32 m0, %3\n\ts_nop 2\n\tglobal_load_lds_dword %1, %2\n\ts_mov_b32 m0, %0" : "=&s"(keep) : "v"(off), "s"(pc), "s"(lb + L_ST) : "memory");
    }
  }
  asm volatile("" :: "s"(x), "s"(wqkv), "s"(wo), "s"(wg), "s"(wu), "s"(wd), "s"(cvec), "s"(wmod), "s"(out), "s"(ws) : "memory");
  SB();
  if (PRETOUCH >= 1 && wave == 1)
    dma4_plain(ws, lane < 16 || PRETOUCH < 2 ? min(lane, 12) << 21 : min(slot * 14 + min(lane - 16, 13), 415) << 16, lb + L_ST + 256);
  if (PRETOUCH >= 2 && wave == 3) dma4_plain(out, min(lane, 27) << 16, lb + L_ST + 512);
  if (wave == 2 && lane < 32) lds_st(lb + L_SYNC + lane * 4, 0u);
  const u64 t_entry = __builtin_amdgcn_s_memrealtime();
  const u32 epoch_v = poll32(ws);
  u32 tag;

  f32x16 accQ, accK, accV;
  const u32 OFF_MODG = OFF_MODG0 + (NREP == 8 ? xg : 0) * MODG_REP;
  const int tq4 = (lane & 15) >> 2, tp4 = lane & 3, grp = lane >> 4;
  if (b < 42) {
    u32x4 wv[7];
    const u32 cv = cvec[56 * wave + min(lane, 55)];
#pragma unroll
    for (int n = 0; n < 7; ++n) wv[n] = *(const u32x4*)((const char*)wmod + (size_t)(56 * wave + 8 * n + (lane >> 3)) * 5376 + 128 * b + 16 * (lane & 7));
    BAR();
    const float cf = bflo(cv);
    const float sl = rbf(cf * __builtin_amdgcn_rcpf(1.f + __builtin_amdgcn_exp2f(-1.4426950408889634f * cf)));
    float acc[8] = {0.f, 0.f, 0.f, 0.f, 0.f, 0.f, 0.f, 0.f};
#pragma unroll
    for (int n = 0; n < 7; ++n) {
      const float sn = __uint_as_float(__builtin_amdgcn_ds_bpermute((8 * n + (lane >> 3)) * 4, __float_as_uint(sl)));
#pragma unroll
      for (int j = 0; j < 4; ++j) { acc[2 * j] = fmaf(sn, bflo(wv[n][j]), acc[2 * j]); acc[2 * j + 1] = fmaf(sn, bfhi(wv[n][j]), acc[2 * j + 1]); }
    }

#pragma unroll
    for (int j = 0; j < 8; ++j) acc[j] += DPPF(acc[j], 0x128);
#pragma unroll
    for (int j = 0; j < 4; ++j) { auto r = __builtin_amdgcn_permlane16_swap(__float_as_uint(acc[j]), __float_as_uint(acc[j + 4]), false, false); acc[j] = __uint_as_float(r[0]) + __uint_as_float(r[1]); }
#pragma unroll
    for (int j = 0; j < 2; ++j) { auto r = __builtin_amdgcn_permlane32_swap(__float_as_uint(acc[j]), __float_as_uint(acc[j + 2]), false, false); acc[j] = __uint_as_float(r[0]) + __uint_as_float(r[1]); }
    if (!(lane & 8)) *(LAS f32x2*)(lb + L_MODP + (wave * 64 + 8 * (lane & 7) + 2 * hi + 4 * (grp & 1)) * 4) = (f32x2){acc[0], acc[1]};
    WAIT_LGKM();
    u32 arrived = 0;
    if (lane == 0) arrived = __hip_atomic_fetch_add((LAS u32*)(lb + L_SYNC + 12), 1u, __ATOMIC_RELAXED, __HIP_MEMORY_SCOPE_WORKGROUP);
    if (__builtin_amdgcn_readfirstlane(arrived) == 7) {
      CFENCE();
      f32x2 t = {0.f, 0.f};
#pragma unroll
      for (int p = 0; p < 8; ++p) t += *(const LAS f32x2*)(lb + L_MODP + (p * 64 + 2 * l31) * 4);
      const u32 pv = pk2(t[0], t[1]);
      const u32 g0 = __builtin_amdgcn_ds_bpermute((2 * (lane & 15)) * 4, pv), g1 = __builtin_amdgcn_ds_bpermute((2 * (lane & 15) + 1) * 4, pv);
#pragma unroll
      for (int r = 0; r < NREP / 4; ++r) st128(wsr, (u32)(32 * b + 2 * (lane & 15)) * 8 + (4 * r + grp) * MODG_REP, OFF_MODG0, (u32x4){g0, epoch_v + 1u, g1, epoch_v + 1u});
      lds_st(lb + L_SYNC + 20, 1u);
      WAIT_VM(0);
      lds_st(lb + L_SYNC + 32, 1u);
    }
  } else if (wave < 3 && 3 * (b - 42) + wave < 512) {
    const int xgp = 3 * (b - 42) + wave, row = 4 * xgp + (lane & 3), c0i = lane >> 2;
    u32x4 xr[4];
#pragma unroll
    for (int n = 0; n < 4; ++n) xr[n] = *(const u32x4*)((const char*)x + (size_t)row * 896 + min(c0i + 16 * n, 55) * 16);
    BAR();
    if (c0i >= 8) xr[3] = (u32x4){0u, 0u, 0u, 0u};
    float s1[2] = {0.f, 0.f}, s2[2] = {0.f, 0.f};
#pragma unroll
    for (int n = 0; n < 4; ++n)
#pragma unroll
      for (int j = 0; j < 4; ++j) { s1[j & 1] = dot2(xr[n][j], 0x3F803F80u, s1[j & 1]); s2[j & 1] = dot2(xr[n][j], xr[n][j], s2[j & 1]); }
    float t1 = s1[0] + s1[1], t2 = s2[0] + s2[1];
    t1 += DPPF(t1, 0x124); t2 += DPPF(t2, 0x124); t1 += DPPF(t1, 0x128); t2 += DPPF(t2, 0x128);
    { auto r = __builtin_amdgcn_permlane16_swap(__float_as_uint(t1), __float_as_uint(t2), false, false); t1 = __uint_as_float(r[0]) + __uint_as_float(r[1]); }
    t1 = xhalf_sum(t1);
    { auto r = __builtin_amdgcn_permlane16_swap(__float_as_uint(t1), __float_as_uint(t1), false, false); t1 = __uint_as_float(r[0]); t2 = __uint_as_float(r[1]); }
    const float mean = t1 * (1.f / 448.f), ex2 = t2 * (1.f / 448.f);
    const float rstd = __builtin_amdgcn_rsqf(fmaxf(ex2 - mean * mean, 0.f) + 1e-5f), c0 = -mean * rstd;
    asm volatile("" :: "v"(c0));

#pragma unroll
    for (int n = 0; n < 4; ++n) {
      u32x4 o;
#pragma unroll
      for (int j = 0; j < 4; ++j) asm("v_cvt_pk_f16_f32 %0, %1, %2" : "=v"(o[j]) : "v"(fmaf(bflo(xr[n][j]), rstd, c0)), "v"(fmaf(bfhi(xr[n][j]), rstd, c0)));
      const u32 c = c0i + 16 * n;
      st128(wsr, c < 56 ? (c >> 1) * 1024 + ((row & 31) + 32 * (c & 1)) * 16 + (u32)(row >> 5) * 28672 : 0x80000000u, OFF_XH, o);
    }
    WAIT_VM(0);
    if (lane == 0) flag32(ws + OFF_XF + xgp * 128, epoch_v + 1u);
  } else if ((wave == 4 || wave == 5) && b < 210) {
    const int u = b - 42, part = u / 56, rem = u - part * 56, hp = rem / 7, kg = rem - hp * 7, s0 = 4 * kg + 2 * (wave - 4);
    const char* src = (const char*)wqkv + (size_t)(16 * s0) * 2688 + part * 896 + hp * 112;
    const u32 stg = lb + L_Q + (wave - 4) * 4096;
#pragma unroll
    for (int i = 0; i < 4; ++i) { const u32 c = lane & 7; dma16<false>(src + i * 8 * 2688, (lane >> 3) * 2688 + 16 * (c == 7 ? 6 : c), stg + i * 1024); }
    BAR();
    WAIT_VM(0);

#pragma unroll
    for (int e = 0; e < 4; ++e) {
      const u32 a = stg + (16 * (e >> 1) + 8 * (grp >> 1) + tq4) * 128 + (e & 1) * 56 + (grp & 1) * 32 + tp4 * 8;
      u32x4 f = cat(ldstr(a), ldstr(a + 512));
      if (l31 >= 28) f = (u32x4){0u, 0u, 0u, 0u};
      st128(wsr, lane * 16, OFF_WQT + (u32)((2 * hp + (e & 1)) * 84 + 3 * (s0 + (e >> 1)) + part) * 1024, f);
    }
    WAIT_VM(0);
    if (lane < 2) flag32(ws + OFF_WQF + ((2 * hp + lane) * 64 + 3 * (s0 >> 1) + part) * 128, epoch_v + 1u);
  } else {
    BAR();
  }
  tag = __builtin_amdgcn_readfirstlane(epoch_v) + 1u;
  STAMPL(7, "unit done");
  const int cg = b / 7, kg = b - cg * 7;
  if (wave >= 6) {
#pragma nounroll
    for (int i = 0; i < 4096 && __builtin_amdgcn_s_memrealtime() - t_entry < PREP_START; ++i) __builtin_amdgcn_s_sleep(1);
    if (b < 42) lds_wait(lb + L_SYNC + 20, 1);
#pragma unroll
    for (int ph = 0; ph < 2; ++ph) {
      const int pw = wave & 1, tw = 2 * ph + pw, p = 4 * b + tw;
      const u32 stg = lb + L_STG + tw * 6144;
      if (b < 196) {
#pragma unroll
        for (int i = 0; i < 2; ++i) {
          const u32 vo = (u32)(64 * kg + 32 * ph + 16 * i + (lane >> 2)) * 3584 + (64 * cg + 32 * pw) * 2 + 16 * (lane & 3);
          dma16<false>(wg, vo, stg + (2 * i) * 1024); dma16<false>(wu, vo, stg + (2 * i + 1) * 1024);
        }
      }
      if (p < 784) {
        const int ds_ = p / 7, np = p - ds_ * 7;
#pragma unroll
        for (int i = 0; i < 2; ++i) dma16<false>(wd, (u32)(16 * ds_ + (lane >> 2)) * 896 + np * 128 + i * 64 + 16 * (lane & 3), stg + (4 + i) * 1024);
      } else if (p < 1008) {
        const int q = p - 784, h = q / 14, nq = q - h * 14;
#pragma unroll
        for (int i = 0; i < 2; ++i) dma16<false>(wo, (u32)min(28 * h + 16 * i + (lane >> 2), 447) * 896 + nq * 64 + 16 * (lane & 3), stg + (4 + i) * 1024);
      }
    }
    STAMPL(6, "prep issued");
    WAIT_VM(0);
    if (lane == 0) __hip_atomic_fetch_add((LAS u32*)(lb + L_SYNC + 52), 1u, __ATOMIC_RELAXED, __HIP_MEMORY_SCOPE_WORKGROUP);
    STAMPL(5, "prep landed");
  } else {
#pragma nounroll
    for (int i = 0; i < 4096 && __builtin_amdgcn_s_memrealtime() - t_entry < (wave == 4 ? MOD_POLL : wave >= 3 ? FIRST_POLL : TOUCH_AT); ++i) __builtin_amdgcn_s_sleep(1);
    if (TOUCH_AT && wave < 3) {
      dma4_touch(ws + OFF_WQT + (size_t)ah * 86016 + (slot & 3) * 21504, min(wave * 64 + lane, 167) * 128, lb + L_ST + wave * 256);
      if (wave < 2) dma4_touch(ws + OFF_XH + (size_t)ai * 114688 + (slot >> 2) * 14336, min(wave * 64 + lane, 111) * 128, lb + L_ST + 768 + wave * 256);
    }
  }

  if (wave < 4) {
    u32x4 xf[28], pf[6]; u32 pfo[6], uvo[2]; float uvv[2];
    const int pw = wave & 1, ph = (wave >> 1) & 1;
    auto tile_read = [&]() __attribute__((always_inline)) {
      const int p = 4 * b + wave;
      const u32 stg = lb + L_STG + wave * 6144;
      const bool isd = p < 784;
      const int ds_ = p / 7, np = p - ds_ * 7, q = p - 784, h = q / 14, nq = q - h * 14;
      lds_wait(lb + L_SYNC + 52, 2);
      const int pi = ((tp4 & 1) << 1) | (tp4 >> 1);
#pragma unroll
      for (int i = 0; i < 2; ++i)
#pragma unroll
        for (int e = 0; e < 2; ++e) {
          const u32 a = stg + (2 * i + (grp & 1)) * 1024 + (8 * (grp >> 1) + tq4) * 64 + e * 32 + pi * 8;
          pf[2 * i + e] = cat(ldstr(a), ldstr(a + 256));
          pfo[2 * i + e] = lane * 16 + (b < 196 ? OFF_WGU + (u32)((4 * cg + 2 * pw + e) * 28 + 4 * kg + 2 * ph + i) * 1024 : 0x80000000u);
        }
#pragma unroll
      for (int i = 0; i < 2; ++i) {
        const u32 a = isd ? stg + (4 + i) * 1024 + (8 * (grp >> 1) + tq4) * 64 + (grp & 1) * 32 + tp4 * 8
                          : stg + 4096 + (16 * (grp >> 1) + 4 * (grp & 1) + tq4) * 64 + i * 32 + tp4 * 8;
        u32x4 f = cat(ldstr(a), ldstr(a + (isd ? 256 : 512)));
        if (!isd && grp == 3) { f[2] = 0u; f[3] = 0u; }
        pf[4 + i] = f;
        pfo[4 + i] = lane * 16 + (isd ? OFF_WDT + (u32)((2 * np + i) * 112 + ds_) * 1024 : p < 1008 ? OFF_WOT + (u32)((2 * nq + i) * 16 + h) * 1024 : 0x80000000u);
        uvo[i] = lane * 4 + (b < 196 ? OFF_UV + (u32)((2 * kg + ph) * 112 + 4 * cg + 2 * pw + i) * 256 : 0x80000000u);
      }
      asm volatile("" : "+v"(pf[0]), "+v"(pf[1]), "+v"(pf[2]), "+v"(pf[3]), "+v"(pf[4]), "+v"(pf[5]));
    };
    if (wave == 3) { wait_flags(ws + OFF_XF + (32 * ai + l31) * 128, tag); STAMPL(1, "xhat seen"); if (GATE) lds_wait(lb + L_SYNC + 36, 1); lds_st(lb + L_SYNC, 1u); }
    else { tile_read(); lds_wait(lb + L_SYNC, 1); }
    STAMPL(3, "xhat go");
    constexpr int XPRE = 12;
#define LDX(s) xf[s] = ld128(wsr, lane * 16 + ((s) & 3) * 1024, OFF_XH + (u32)((4 * ai + wave) * 28 + ((s) & ~3)) * 1024)
#pragma unroll
    for (int s = 0; s < XP0; ++s) { LDX(s); SB(); }
    if (wave == 3) tile_read();
    if (!GATE) lds_wait(lb + L_SYNC + 36, 1);
    {
      float uu[2] = {0.f, 0.f}, vv[2] = {0.f, 0.f};
#pragma unroll
      for (int i = 0; i < 2; ++i) {
        const u32x4 be = lds128(lb + L_G2 + (2 * ph + i) * 32 + hi * 16), ga = lds128(lb + L_G2 + 128 + (2 * ph + i) * 32 + hi * 16);
#pragma unroll
        for (int e = 0; e < 2; ++e)
#pragma unroll
          for (int j = 0; j < 4; ++j) {
            const u32 w = pf[2 * i + e][j];
            vv[e] = dot2(w, be[j], vv[e]);
            pf[2 * i + e][j] = pk2(bflo(w) * bflo(ga[j]), bfhi(w) * bfhi(ga[j]));
            uu[e] = dot2(pf[2 * i + e][j], 0x3F803F80u, uu[e]);
          }
      }
#pragma unroll
      for (int e = 0; e < 2; ++e) { const float us = xhalf_sum(uu[e]), vs = xhalf_sum(vv[e]); uvv[e] = hi ? vs : us; }
      asm volatile("" : "+v"(pf[0]), "+v"(pf[1]), "+v"(pf[2]), "+v"(pf[3]), "+v"(uvv[0]), "+v"(uvv[1]));
      STAMPL(10, "prep tiled");
    }
#pragma unroll
    for (int s = XP0; s < XPRE; ++s) { LDX(s); SB(); }
    lds_wait(lb + L_SYNC + 4, 4);

    STAMPL(8, "W1 ready");
    accQ = (f32x16){}; accK = (f32x16){}; accV = (f32x16){};
    {
      u32 gb = lb + L_GAM + hi * 16; asm volatile("" : "+v"(gb));
      u32 wb = lb + lane * 16, wb2 = wb + 43008; asm volatile("" : "+v"(wb), "+v"(wb2));
      u32x4 wf[3][3], hf[2], gs[3], be[3]; float ht[8];
#define RDG(s) gs[(s) % 3] = lds128(gb + (s) * 32)
#define RDB(s) be[(s) % 3] = lds128(gb + 896 + (s) * 32)
#define RDW1(s, p) wf[(s) % 3][p] = lds128(((s) < 14 ? wb : wb2) + (3 * ((s) % 14) + (p)) * 1024)
#define RDW(s) do { RDW1(s, 0); RDW1(s, 1); RDW1(s, 2); } while (0)
#define DOTS(s, h) do { _Pragma("unroll") for (int j = 2 * (h); j < 2 * (h) + 2; ++j) { \
        asm volatile("v_fma_mix_f32 %0, %1, %2, %3 op_sel_hi:[1,1,1]" : "=v"(ht[2 * j]) : "v"(xf[s][j]), "v"(gs[(s) % 3][j]), "v"(be[(s) % 3][j])); \
        asm volatile("v_fma_mix_f32 %0, %1, %2, %3 op_sel:[1,1,1] op_sel_hi:[1,1,1]" : "=v"(ht[2 * j + 1]) : "v"(xf[s][j]), "v"(gs[(s) % 3][j]), "v"(be[(s) % 3][j])); } } while (0)
#define CVTS(s) do { hf[(s) & 1] = (u32x4){pk2(ht[0], ht[1]), pk2(ht[2], ht[3]), pk2(ht[4], ht[5]), pk2(ht[6], ht[7])}; asm volatile("" : "+v"(hf[(s) & 1])); } while (0)
      RDG(0); RDB(0); RDW(0); RDG(1); RDB(1); RDW(1); RDG(2); RDB(2); RDW(2); DOTS(0, 0); DOTS(0, 1); CVTS(0); SB();
#pragma unroll
      for (int s = 0; s < 28; ++s) {
        if (s == 1) lds_wait(lb + L_SYNC + 8, 3);
        if (s == 13) lds_wait(lb + L_SYNC + 24, 3);
        SB(); accQ = mfma32(wf[s % 3][0], hf[s & 1], accQ); SB();
        if (s < 25) RDG(s + 3);
        if (s < 25) RDW1(s + 3, 0);
        if (s < 27) DOTS(s + 1, 0);
        SB(); accK = mfma32(wf[s % 3][1], hf[s & 1], accK); SB();
        if (s < 25) RDB(s + 3);
        if (s < 25) RDW1(s + 3, 1);
        if (s < 27) DOTS(s + 1, 1);
        SB(); accV = mfma32(hf[s & 1], wf[s % 3][2], accV); SB();
        if (s < 25) RDW1(s + 3, 2);
        if (s + XPRE < 28) LDX(s + XPRE);
        else if (s < 22) st128(wsr, pfo[s - 16], 0, pf[s - 16]);
        else if (s < 24) st32(wsr, uvo[s - 22], 0, __float_as_uint(uvv[s - 22]));
        if (s < 27) CVTS(s + 1);
        SB();
      }
    }
    STAMPL(5, "qkv mfma done");
    const float unscale = __uint_as_float(__builtin_amdgcn_readfirstlane(lds_ld(lb + L_SYNC + 28)));
    if (unscale != 1.f) { accQ *= unscale; accK *= unscale; accV *= unscale; }
    u32x4 k0, k1, v0, v1, q0, q1;
#pragma unroll
    for (int j = 0; j < 4; ++j) {
      k0[j] = pk2(accK[2 * j], accK[2 * j + 1]); k1[j] = pk2(accK[8 + 2 * j], accK[9 + 2 * j]);
      v0[j] = pk2(accV[2 * j], accV[2 * j + 1]); v1[j] = pk2(accV[8 + 2 * j], accV[9 + 2 * j]);
    }
    if (hi) { k1[2] = 0x3F803F80u; k1[3] = 0x00003F80u; }
    if (l31 == 28) { v0 = (u32x4){0x3F803F80u, 0x3F803F80u, 0x3F803F80u, 0x3F803F80u}; v1 = v0; }
    const u32 kvo = OFF_KV + (u32)(ah * 64 + ai * 4 + wave) * 4096;
    st128(wsr, lane * 16, kvo, k0); st128(wsr, lane * 16 + 1024, kvo, k1); st128(wsr, lane * 16 + 2048, kvo, v0); st128(wsr, lane * 16 + 3072, kvo, v1);
    SB();
    float kn = 0.f, qn = 0.f;
    constexpr float QS = 0.18898223650461362f * 1.4426950408889634f;
#pragma unroll
    for (int r = 0; r < 16; ++r) { kn = fmaf(accK[r], accK[r], kn); accQ[r] *= QS; qn = fmaf(accQ[r], accQ[r], qn); }
#pragma unroll
    for (int j = 0; j < 4; ++j) { q0[j] = pk2(accQ[2 * j], accQ[2 * j + 1]); q1[j] = pk2(accQ[8 + 2 * j], accQ[9 + 2 * j]); }
    *(LAS u32x4*)(lb + L_Q + wave * 2048 + lane * 16) = q0; *(LAS u32x4*)(lb + L_Q + wave * 2048 + 1024 + lane * 16) = q1;
    qn = xhalf_sum(qn); kn = wave_max(xhalf_sum(kn));
    if (!hi) *(LAS float*)(lb + L_QN + (wave * 32 + l31) * 4) = qn;
    WAIT_VM(0);
    if (lane == 0) flag64(ws + OFF_KVG + (ah * 64 + ai * 4 + wave) * 128, __float_as_uint(kn), tag);
    if (b < 42) lds_wait(lb + L_SYNC + 32, 1);
    if (lane == 0 && __hip_atomic_fetch_add((LAS u32*)(lb + L_SYNC + 44), 1u, __ATOMIC_RELAXED, __HIP_MEMORY_SCOPE_WORKGROUP) == 3u) flag32(ws + OFF_PREPF + b * 128, tag);
    STAMPL(6, "kv flagged");
  } else {
    if (wave == 4) {
      u64 gv[8];
      __builtin_amdgcn_s_setprio(3);
#pragma nounroll
      for (int i = 0; i < SPIN_CAP; ++i) {
        bool ok = true;
#pragma unroll
        for (int n = 0; n < 7; ++n) gv[n] = poll64(ws + OFF_MODG + (64 * n + lane) * 8);
        gv[7] = poll64(ws + OFF_MODG + (672 + 224 * hi + 32 * kg + l31) * 8);
#pragma unroll
        for (int n = 0; n < 8; ++n) ok = ok && (u32)(gv[n] >> 32) == tag;
        if (__all(ok)) break;
        __builtin_amdgcn_s_sleep(POLL_SLEEP);
      }
      CFENCE();
      __builtin_amdgcn_s_setprio(0);
      STAMPL(2, "mod seen");
      *(LAS u32*)(lb + L_G2 + lane * 4) = hi ? pk2(1.f + bflo((u32)gv[7]), 1.f + bfhi((u32)gv[7])) : (u32)gv[7];
      WAIT_LGKM();
      lds_st(lb + L_SYNC + 36, 1u);

      float lo[7], hi2[7], mxv = 0.f;
#pragma unroll
      for (int n = 0; n < 7; ++n) {
        u32 v = (u32)gv[n];
        if (64 * n + lane >= 224) v = pk2(1.f + bflo(v), 1.f + bfhi(v));
        lo[n] = bflo(v); hi2[n] = bfhi(v); mxv = fmaxf(mxv, fmaxf(fabsf(lo[n]), fabsf(hi2[n])));
      }
      const u32 ex = max((int)(__float_as_uint(wave_max(mxv)) >> 23) - (127 + 14), 0);
      const float scl = __uint_as_float((127u - min(ex, 114u)) << 23);
#pragma unroll
      for (int n = 0; n < 7; ++n) {
        u32 o; asm("v_cvt_pk_f16_f32 %0, %1, %2" : "=v"(o) : "v"(lo[n] * scl), "v"(hi2[n] * scl));
        const int g = 64 * n + lane;
        *(LAS u32*)(lb + L_GAM + (g >= 224 ? g - 224 : g + 224) * 4) = o;
      }
      lds_st(lb + L_SYNC + 28, (127u + min(ex, 114u)) << 23);
      WAIT_LGKM();
      if (lane == 0) __hip_atomic_fetch_add((LAS u32*)(lb + L_SYNC + 4), 1u, __ATOMIC_RELAXED, __HIP_MEMORY_SCOPE_WORKGROUP);
    } else {
      if (wave == 5) { wait_flags(ws + OFF_WQF + (ah * 64 + min(lane, 41)) * 128, tag); STAMPL(4, "W seen"); if (GATE) lds_wait(lb + L_SYNC + 36, 1); lds_st(lb + L_SYNC + 48, 1u); }
      else lds_wait(lb + L_SYNC + 48, 1);

      const char* wsrc = ws + OFF_WQT + (size_t)ah * 84 * 1024 + (wave - 5) * 4096;
#define WDMA(n) dma16x4(wsrc + (n) * 12288, lane * 16, lb + (wave - 5) * 4096 + (n) * 12288)
      WDMA(0); WDMA(1);
      WAIT_VM(4);
      if (lane == 0) __hip_atomic_fetch_add((LAS u32*)(lb + L_SYNC + 4), 1u, __ATOMIC_RELAXED, __HIP_MEMORY_SCOPE_WORKGROUP);
      WDMA(2); WDMA(3); WDMA(4);
      WAIT_VM(4);
      if (lane == 0) __hip_atomic_fetch_add((LAS u32*)(lb + L_SYNC + 8), 1u, __ATOMIC_RELAXED, __HIP_MEMORY_SCOPE_WORKGROUP);
      WDMA(5); WDMA(6);
      WAIT_VM(0);
      if (lane == 0) __hip_atomic_fetch_add((LAS u32*)(lb + L_SYNC + 24), 1u, __ATOMIC_RELAXED, __HIP_MEMORY_SCOPE_WORKGROUP);
      STAMPL(9, "W landed");
    }
  }
  BAR();
  STAMPL(11, "B3");

  const int ort = 8 * xg + (slot >> 2), ocq = slot & 3;
  const int gi = ai, gfj = ah;
  u32x4 wo_f[16], wgu[28], xres, gaA, gaB;
  {
    u32x4 qf[4][2];
#pragma unroll
    for (int t = 0; t < 4; ++t) { qf[t][0] = lds128(lb + L_Q + t * 2048 + lane * 16); qf[t][1] = lds128(lb + L_Q + t * 2048 + 1024 + lane * 16); }
    float qn2[4];
#pragma unroll
    for (int t = 0; t < 4; ++t) qn2[t] = lds32f(lb + L_QN + (t * 32 + l31) * 4);
    WAIT_LGKM();
    if (lane == 0) __hip_atomic_fetch_add((LAS u32*)(lb + L_SYNC + 40), 1u, __ATOMIC_RELAXED, __HIP_MEMORY_SCOPE_WORKGROUP);
    if (wave == 3) {
      __builtin_amdgcn_s_sleep(KV_SLEEP);
      const float km = wave_max(__uint_as_float(wait_gran(ws + OFF_KVG + (ah * 64 + lane) * 128, tag)));
      lds_st(lb + L_SYNC + 60, __float_as_uint(km));
      WAIT_LGKM();
      lds_st(lb + L_SYNC + 56, 1u);
    } else lds_wait(lb + L_SYNC + 56, 1);
    const float kmax2 = __uint_as_float(lds_ld(lb + L_SYNC + 60));
    STAMPL(12, "kv flags seen");
    const u32 kvb = OFF_KV + (u32)(ah * 64 + 8 * wave) * 4096;
    u32x4 kf[2][2], vf[2][2];
    const u32 kvl = lane * 16;
#define LDKV(D, kt, off) asm volatile("buffer_load_dwordx4 %0, %2, %3, %4 offen offset:" #off " sc1\n\tbuffer_load_dwordx4 %1, %2, %3, %4 offen offset:" #off "+1024 sc1" \
      : "=&v"(D[(kt) & 1][0]), "=&v"(D[(kt) & 1][1]) : "v"(kvl), "s"(wsr), "s"(kvb + (kt) * 4096) : "memory")
#define WAITN(n) asm volatile("s_waitcnt vmcnt(%0)" :: "i"(n) : "memory")
#define PINKV(D, kt) asm volatile("" : "+v"(D[(kt) & 1][0]), "+v"(D[(kt) & 1][1]))
#define WAITKV(D, kt, n) do { WAITN(n); SB(); PINKV(D, kt); } while (0)
#define WAITKV2(D, kt, n0, n1) do { if (wave < 4) WAITN(n0); else WAITN(n1); SB(); PINKV(D, kt); } while (0)
    asm volatile("s_nop 4");
    LDKV(kf, 0, 0); LDKV(vf, 0, 2048); LDKV(kf, 1, 0);
    float sh[4]; bool safe = true;
#pragma unroll
    for (int t = 0; t < 4; ++t) { sh[t] = 1.02f * __builtin_amdgcn_sqrtf(qn2[t] * kmax2); safe = safe && (sh[t] <= 48.f); }
    if (!__all(safe)) {
      BAR();
      float mx[4] = {-INFINITY, -INFINITY, -INFINITY, -INFINITY};
#pragma unroll 1
      for (int kt = 0; kt < 8; ++kt) {
        const u32x4 ka = ld128(wsr, lane * 16, kvb + kt * 4096), kb = ld128(wsr, lane * 16 + 1024, kvb + kt * 4096);
#pragma unroll
        for (int t = 0; t < 4; ++t) {
          f32x16 s = mfma32(ka, qf[t][0], (f32x16){}); s = mfma32(kb, qf[t][1], s);
#pragma unroll
          for (int r = 0; r < 16; ++r) mx[t] = fmaxf(mx[t], s[r]);
        }
      }
#pragma unroll
      for (int t = 0; t < 4; ++t) { const float m = xhalf_max(mx[t]); if (!hi) *(LAS float*)(lb + L_MX + (wave * 128 + t * 32 + l31) * 4) = m; }
      BAR();
#pragma unroll
      for (int t = 0; t < 4; ++t) {
        float m = -INFINITY;
#pragma unroll
        for (int w = 0; w < 8; ++w) m = fmaxf(m, lds32f(lb + L_MX + (w * 128 + t * 32 + l31) * 4));
        sh[t] = m;
      }
    }
    if (hi) {
#pragma unroll
      for (int t = 0; t < 4; ++t) {
        const float a = rbf(-sh[t]), r1 = -sh[t] - a, c = rbf(r1);
        qf[t][1][2] = pk2(a, c); qf[t][1][3] = pk2(r1 - c, 0.f);
      }
    }
    f32x16 o[4] = {(f32x16){}, (f32x16){}, (f32x16){}, (f32x16){}};
    WAITKV(kf, 0, 4);
    f32x16 sc = mfma32(kf[0][0], qf[0][0], (f32x16){}); sc = mfma32(kf[0][1], qf[0][1], sc);
    u32x4 p0, p1;
#define EXP2(v) __builtin_amdgcn_exp2f(v)
    float ec, ed;
#define GRP(r, PREV, MF, CUR) do { float ea = EXP2(sc[r]), eb = EXP2(sc[(r) + 1]); asm volatile("" : "+v"(ea), "+v"(eb)); PREV; SB(); MF; SB(); \
      ec = EXP2(sc[(r) + 2]); ed = EXP2(sc[(r) + 3]); asm volatile("" : "+v"(ec), "+v"(ed)); { u32 pa = pk2(ea, eb); asm volatile("" : "+v"(pa)); CUR = pa; } SB(); } while (0)
#define PKP(D) do { u32 pb = pk2(ec, ed); asm volatile("" : "+v"(pb)); D = pb; } while (0)
    u32 pwb = lb + wave * 8192 + lane * 16, lwb = lb + L_LP + wave * 512 + l31 * 4; asm volatile("" : "+v"(pwb), "+v"(lwb));
#define WRP(t) do { _Pragma("unroll") for (int g = 0; g < 2; ++g) *(LAS u32x4*)(pwb + ((t) * 2 + g) * 1024) = (u32x4){pk2(o[t][8 * g], o[t][8 * g + 1]), pk2(o[t][8 * g + 2], o[t][8 * g + 3]), pk2(o[t][8 * g + 4], o[t][8 * g + 5]), pk2(o[t][8 * g + 6], o[t][8 * g + 7])}; \
      if (hi) *(LAS float*)(lwb + (t) * 128) = o[t][12]; } while (0)
    const int w6 = min(wave, 6);
    const u32 wo_base = OFF_WOT + (u32)(7 * ocq + w6) * 16384, wg_base = OFF_WGU + (u32)(7 * gfj + w6) * 28672;
    auto nslot = [](int kt, int t, bool) constexpr { const int sl = 4 * (kt - 2) + t;
      return (t == 0 && kt < 7 ? 2 : 0) + (t == 2 && kt < 6 ? 2 : 0) + (kt >= 2 ? (sl == 0) + (sl < 16) + (sl >= 6 && sl < 18) + (sl == 18) : 0); };
    auto nafter = [&](int k0, int t0, int k1, int t1, bool young) constexpr { int n = -2; for (int i = 4 * k0 + t0; i <= 4 * k1 + t1; ++i) n += nslot(i >> 2, i & 3, young); return n; };
#pragma unroll
    for (int kt = 0; kt < 8; ++kt) {
      if (kt == 0) __builtin_amdgcn_s_setprio(3);
      if (kt == 2) __builtin_amdgcn_s_setprio(2);
      if (kt == 4) __builtin_amdgcn_s_setprio(1);
      if (kt == 6 && wave < 4) __builtin_amdgcn_s_setprio(0);
      if (kt == 7) __builtin_amdgcn_s_setprio(0);
#pragma unroll
      for (int t = 0; t < 4; ++t) {
        f32x16 sn;
        const bool more = t < 3 || kt < 7, first = t == 0 && kt == 0;
        const int kn = t < 3 ? (kt & 1) : ((kt + 1) & 1), tn = (t + 1) & 3;
        if (t == 3 && kt < 7) {
          if (kt == 0) WAITKV(kf, 1, nafter(0, 0, 0, 2, false) + 2);
          else if (kt == 1) WAITKV(kf, 2, nafter(0, 2, 1, 2, false));
          else WAITKV(kf, kt + 1, nafter(kt - 1, 2, kt, 2, false));
        }
        GRP(0, if (!first) PKP(p1[3]), if (more) sn = mfma32(kf[kn][0], qf[tn][0], (f32x16){}), p0[0]);
        GRP(4, PKP(p0[1]), if (more) sn = mfma32(kf[kn][1], qf[tn][1], sn), p0[2]);
        GRP(8, PKP(p0[3]), if (t > 0) o[t - 1] = mfma32(vf[kt & 1][1], p1, o[t - 1]); else if (kt > 0) o[3] = mfma32(vf[(kt + 1) & 1][1], p1, o[3]), p1[0]);
        if (t == 0 && kt < 7) LDKV(vf, kt + 1, 2048);
        if (t == 2 && kt < 6) LDKV(kf, kt + 2, 0);
        if (kt == 1 && t == 0 && wave == 3) {
#pragma nounroll
          for (int i = 0; i < SPIN_CAP; ++i) {
            const u32 a = poll32(ws + OFF_PREPF + lane * 128), c = poll32(ws + OFF_PREPF + (64 + lane) * 128), d = poll32(ws + OFF_PREPF + (128 + lane) * 128), e = poll32(ws + OFF_PREPF + (192 + lane) * 128);
            if (__all(a == tag && c == tag && d == tag && e == tag)) break;
            __builtin_amdgcn_s_sleep(8);
          }
          CFENCE();
          if (lane == 0) __hip_atomic_fetch_add((LAS u32*)(lb + L_SYNC + 40), 1u, __ATOMIC_RELAXED, __HIP_MEMORY_SCOPE_WORKGROUP);
        }
        if (t == 0) {
        }
        if (kt >= 2) {
          const int sl = 4 * (kt - 2) + t;
          if (sl == 0) {
            lds_wait(lb + L_SYNC + 40, 9);
            dma4_touch(ws + wg_base + 12288, ((slot & 3) * 32 + l31) * 128 + hi * 64, lb + L_ST + w6 * 256);
          }
          if (sl == 18) dma4_touch(ws + OFF_WDT + (size_t)((slot & 15) % 7) * 229376 + ((slot >> 4) * 2 + ((slot & 15) >= 7)) * 57344, (w6 * 64 + lane) * 128, lb + L_ST + w6 * 256);
          if (sl < 10) wo_f[sl] = ld128(wsr, lane * 16, wo_base + sl * 1024);
          else if (sl < 16) wgu[sl - 10] = ld128(wsr, lane * 16, wg_base + (sl - 10) * 1024);
          if (sl >= 6 && sl < 18) dma16<true>(ws + (sl < 12 ? wo_base + (4 + sl) * 1024 : wg_base + (sl - 6) * 1024), lane * 16, lb + L_PARK + w6 * 12288 + (sl - 6) * 1024);
        }
        if (t == 0) {
          if (kt == 0) WAITKV(vf, 0, nafter(0, 0, 0, 0, false) + 4);
          else if (kt == 1) WAITKV(vf, 1, nafter(0, 0, 1, 0, false));
          else WAITKV(vf, kt, nafter(kt - 1, 0, kt, 0, false));
        }
        SB();
        GRP(12, PKP(p1[1]), o[t] = mfma32(vf[kt & 1][0], p0, o[t]), p1[2]);
        if (kt == 7 && t > 0) { WRP(t - 1); SB(); }
        if (more) sc = sn;
      }
    }
    PKP(p1[3]);
    o[3] = mfma32(vf[1][1], p1, o[3]);
    WRP(3);
    STAMPL(13, "attn done");
    BAR();
    const int qt = wave >> 1, sf = wave & 1;
    u32x4 pa[8]; float pl[8];
    {
      u32 base = lb + qt * 2048 + sf * 1024 + lane * 16, lbase = lb + L_LP + qt * 128 + l31 * 4;
      asm volatile("" : "+v"(base), "+v"(lbase));
#pragma unroll
      for (int w = 0; w < 8; ++w) { pa[w] = lds128(base + w * 8192); pl[w] = lds32f(lbase + w * 512); }
      SB();
    }
    float a8[8]; u32 sel0 = 0x00003F80u, sel1 = 0x3F800000u; asm volatile("" : "+v"(sel0), "+v"(sel1));
#pragma unroll
    for (int j = 0; j < 4; ++j) {
      a8[2 * j] = bflo(pa[0][j]); a8[2 * j + 1] = bfhi(pa[0][j]);
#pragma unroll
      for (int w = 1; w < 8; ++w) { a8[2 * j] = dot2(pa[w][j], sel0, a8[2 * j]); a8[2 * j + 1] = dot2(pa[w][j], sel1, a8[2 * j + 1]); }
    }
    const float l = ((pl[0] + pl[4]) + (pl[1] + pl[5])) + ((pl[2] + pl[6]) + (pl[3] + pl[7]));
    const float rl = __builtin_amdgcn_rcpf(l);
    const u32x4 of = {pk2(a8[0] * rl, a8[1] * rl), pk2(a8[2] * rl, a8[3] * rl), pk2(a8[4] * rl, a8[5] * rl), pk2(a8[6] * rl, a8[7] * rl)};
    st128(wsr, (l31 >> 4) * 1024 + ((l31 & 15) + 16 * (2 * sf + hi)) * 16, OFF_OB + (u32)((4 * ai + qt) * 16 + ah) * 2048, of);
    WAIT_VM(0);
    if (lane == 0) flag32(ws + OFF_OF + (((4 * ai + qt) * 16 + ah) * 2 + sf) * 128, tag);
    BAR();
    STAMPL(14, "O flagged");
  }

  u32 uvp[14];
  const int otok = 16 * ((lane >> 4) & 1) + (lane & 15), ofe = 112 * ocq + 16 * min(wave, 6) + 8 * hi;
  if (wave < 7) {
    xres = *(const u32x4*)(x + (size_t)(32 * ort + otok) * 448 + ofe);
    gaA = ld128(wsr, ofe * 4, OFF_MODG + 448 * 8); gaB = ld128(wsr, ofe * 4 + 16, OFF_MODG + 448 * 8);
#pragma unroll
    for (int k = 0; k < 14; ++k) uvp[k] = ld32(wsr, lane * 4, OFF_UV + (u32)(k * 112 + 7 * gfj + wave) * 256);
#pragma unroll
    for (int s = 12; s < 26; ++s) wgu[s] = ld128(wsr, lane * 16 + (s & 3) * 1024, OFF_WGU + (u32)((7 * gfj + wave) * 28 + (s & ~3)) * 1024);
#pragma unroll
    for (int j = 0; j < 6; ++j) wo_f[10 + j] = lds128(lb + L_PARK + wave * 12288 + j * 1024 + lane * 16);
#pragma unroll
    for (int j = 6; j < 12; ++j) wgu[j] = lds128(lb + L_PARK + wave * 12288 + j * 1024 + lane * 16);
    lds_wait(lb + L_SYNC + 16, 1);
  } else {
    wait_flags(ws + OFF_OF + (ort * 32 + l31) * 128, tag);
    lds_st(lb + L_SYNC + 16, 1u);
  }
  STAMPL(15, "O flags seen");
  {
    const char* osrc = ws + OFF_OB + (size_t)(ort * 16 + 2 * wave) * 2048;
    dma16x4(osrc, lane * 16, lb + 4 * wave * 1024);
  }
  if (wave < 7) {
    float uvs = 0.f;
#pragma unroll
    for (int k = 0; k < 14; ++k) uvs += __uint_as_float(uvp[k]);
    *(LAS float*)(lb + L_UV + wave * 256 + lane * 4) = uvs;
  }
  __builtin_amdgcn_s_waitcnt(0x0F70);
  CFENCE();
  BAR();
  STAMPL(16, "O landed");
  if (wave < 7) {
    f32x4 y0 = {0.f, 0.f, 0.f, 0.f}, y1 = y0;
    u32x4 oa[10];
#pragma unroll
    for (int i = 0; i < 8; ++i) oa[i] = lds128(lb + i * 1024 + lane * 16);
#pragma unroll
    for (int h = 0; h < 16; ++h) {
      if (h < 12) { oa[(2 * h + 8) % 10] = lds128(lb + (2 * h + 8) * 1024 + lane * 16); oa[(2 * h + 9) % 10] = lds128(lb + (2 * h + 9) * 1024 + lane * 16); }
      SB(); y0 = mfma16(wo_f[h], oa[(2 * h) % 10], y0); y1 = mfma16(wo_f[h], oa[(2 * h + 1) % 10], y1); SB();
    }
    float yv[8];
#pragma unroll
    for (int r = 0; r < 4; ++r) { auto sw = __builtin_amdgcn_permlane16_swap(__float_as_uint(y0[r]), __float_as_uint(y1[r]), false, false); yv[r] = __uint_as_float(sw[0]); yv[4 + r] = __uint_as_float(sw[1]); }
    const u32 gw[4] = {gaA[0], gaA[2], gaB[0], gaB[2]};
    u32x4 x2p; float s1 = 0.f, s2 = 0.f;
#pragma unroll
    for (int j = 0; j < 4; ++j) {
      x2p[j] = pk2(fmaf(bflo(gw[j]), yv[2 * j], bflo(xres[j])), fmaf(bfhi(gw[j]), yv[2 * j + 1], bfhi(xres[j])));
      s1 = dot2(x2p[j], 0x3F803F80u, s1); s2 = dot2(x2p[j], x2p[j], s2);
    }
    const u32 fo = (u32)(ort * 28 + 7 * ocq + wave) * 1024;
    st128(wsr, (otok + 32 * hi) * 16, OFF_X2 + fo, x2p);
    s1 = xhalf_sum(s1); s2 = xhalf_sum(s2);
    *(LAS float*)(lb + L_ST + wave * 256 + (hi * 32 + otok) * 4) = hi ? s2 : s1;
    WAIT_VM(0);
  }
  BAR();
  if (wave == 7) {
    float t = 0.f;
#pragma unroll
    for (int w = 0; w < 7; ++w) t += lds32f(lb + L_ST + w * 256 + lane * 4);
    const u32 nv = __builtin_amdgcn_update_dpp(0u, __float_as_uint(t), 0xB1, 0xf, 0xf, false);
    st128(wsr, !(lane & 1) ? (u32)lane * 8 : 0x80000000u, OFF_X2G + (u32)(ort * 4 + ocq) * 512, (u32x4){__float_as_uint(t), tag, nv, tag});
  }
  if (wave < 7) {
#pragma unroll
    for (int s = 26; s < 28; ++s) wgu[s] = ld128(wsr, lane * 16 + (s & 3) * 1024, OFF_WGU + (u32)((7 * gfj + wave) * 28 + (s & ~3)) * 1024);
  }
  STAMPL(17, "x2 flagged");

  const int dsl = slot & 15, dblk = 2 * xg + (slot >> 4), drt = 4 * dblk + dsl / 7, dc = dsl % 7;
  const bool dact = dsl < 14;
  u32x4 wdf[28], x2r, gbA, gbB;
  if (wave == 7) {
    u64 g[16]; const char* gp = ws + OFF_X2G + (16 * gi * 64 + lane) * 8;
#pragma nounroll
    for (int i = 0; i < SPIN_CAP; ++i) {
      bool ok = true;
#pragma unroll
      for (int p = 0; p < 16; ++p) g[p] = poll64(gp + p * 512);
#pragma unroll
      for (int p = 0; p < 16; ++p) ok = ok && (u32)(g[p] >> 32) == tag;
      if (__all(ok)) break;
      __builtin_amdgcn_s_sleep(1);
    }
    CFENCE();
#pragma unroll
    for (int p = 0; p < 16; ++p) *(LAS u32*)(lb + L_SP2 + p * 256 + lane * 4) = (u32)g[p];
  }
  BAR();
  STAMPL(18, "x2 flags seen");
  const char* asrc = ws + OFF_X2 + (size_t)(4 * gi) * 28 * 1024;
#define ADMA(n) do { const int g_ = min(wave + 8 * (n), 27); dma16x4(asrc + g_ * 4096, lane * 16, lb + g_ * 4096); } while (0)
  ADMA(0);
  WAIT_VM(0);
  BAR();
  STAMPL(19, "A0 landed");
  const int dm = wave >> 2, dn = (wave >> 1) & 1, dgp = wave & 1;
  const u32 wdo = OFF_WDT + (u32)(2 * dc * 112 + 14 * wave) * 1024;
  const u32 pk_free = lb + 114688 + wave * 4096, pk_a = lb + wave * 3072;
#define PARKA(i) ((i) < 4 ? pk_free + (i) * 1024 : pk_a + ((i) < 7 ? (i) - 4 : 28 + (i) - 7) * 1024)
#define PARKD(i) dma16<true>(ws + wdo + (((i) & 1) * 112 + 9 + ((i) >> 1)) * 1024, lane * 16, PARKA(i))
  if (wave < 7) {
    u32 sb = lb + L_SP2 + l31 * 4, ub = lb + L_UV + wave * 256 + hi * 16; asm volatile("" : "+v"(sb), "+v"(ub));
    u32 ab = lb + lane * 16, ab2 = ab + 57344; asm volatile("" : "+v"(ab), "+v"(ab2));
    constexpr int RD = GU_RING;
    f32x16 acc[2]; u32x4 af[RD + 1]; f32x4 uv[4]; float st[8], rs = 0.f, nm = 0.f, hv[8];
#define EP_RD(T) do { _Pragma("unroll") for (int c = 0; c < 4; ++c) { st[c] = lds32f(sb + ((T) * 4 + c) * 256); st[4 + c] = lds32f(sb + ((T) * 4 + c) * 256 + 128); } } while (0)
#define EP_UV(g) do { uv[0] = lds128f(ub + 32 * (g)); uv[1] = lds128f(ub + 32 * (g) + 64); uv[2] = lds128f(ub + 32 * (g) + 128); uv[3] = lds128f(ub + 32 * (g) + 192); } while (0)
#define EP_ST() do { float m = ((st[0] + st[1]) + (st[2] + st[3])) * (1.f / 448.f); asm volatile("" : "+v"(m)); const float e2 = (((st[4] + st[5]) + st[6]) + st[7]) * (1.f / 448.f); \
      rs = __builtin_amdgcn_rsqf(fmaxf(e2 - m * m, 0.f) + 1e-5f); nm = -m * rs; } while (0)
    constexpr float K1 = -2.f * 0.7978845608028654f * 1.4426950408889634f, K3 = K1 * 0.044715f;
    float ea[8], eb[8], k1v = K1; asm volatile("" : "+v"(k1v));
#define PIN1(v) asm volatile("" : "+v"(v))
#define EP_STAGE(A, e, j) do { \
      if ((j) == 0) { ea[e] = fma1(nm, uv[0][(e) & 3], uv[2][(e) & 3]); eb[e] = fma1(nm, uv[1][(e) & 3], uv[3][(e) & 3]); PIN1(ea[e]); PIN1(eb[e]); } \
      if ((j) == 1) { ea[e] = fma1(A[e], rs, ea[e]); eb[e] = fma1(A[8 + (e)], rs, eb[e]); PIN1(ea[e]); PIN1(eb[e]); } \
      if ((j) == 2) { hv[e] = ea[e] * ea[e]; PIN1(hv[e]); eb[e] *= ea[e]; PIN1(eb[e]); } \
      if ((j) == 3) { hv[e] = fmaf(hv[e], K3, k1v); PIN1(hv[e]); } \
      if ((j) == 4) { hv[e] *= ea[e]; PIN1(hv[e]); } \
      if ((j) == 5) { hv[e] = __builtin_amdgcn_exp2f(hv[e]); PIN1(hv[e]); } \
      if ((j) == 6) { hv[e] += 1.f; PIN1(hv[e]); } \
      if ((j) == 7) { hv[e] = __builtin_amdgcn_rcpf(hv[e]); PIN1(hv[e]); } \
      if ((j) == 8) { hv[e] *= eb[e]; PIN1(hv[e]); } } while (0)
#define EP_OUT(T) st128(wsr, lane * 16, OFF_HB + (u32)((4 * gi + (T)) * 112 + 7 * gfj + wave) * 1024, (u32x4){pk2(hv[0], hv[1]), pk2(hv[2], hv[3]), pk2(hv[4], hv[5]), pk2(hv[6], hv[7])})
#pragma unroll
    for (int i = 0; i < RD; ++i) af[i] = lds128(ab + i * 1024);
    static_for<0, 112>([&](auto NN) __attribute__((always_inline)) {
      {
        constexpr int n = NN.value, t = n / 28, s = n % 28;
        if (n == 28 - RD + 4) { STAMPL(28, "gu n25"); WAIT_VM(0); BAR();  }
        if constexpr (n + RD < 112) af[(n + RD) % (RD + 1)] = (n + RD < 56) ? lds128(ab + (n + RD) * 1024) : lds128(ab2 + (n + RD - 56) * 1024);
        if (s == 0) __builtin_amdgcn_s_setprio(3 - t);
        if constexpr (n == 0 || n == 3 || n == 6) ADMA(1 + n / 3);
        SB();
        if (s == 0) acc[t & 1] = mfma32(wgu[s], af[n % (RD + 1)], (f32x16){}); else acc[t & 1] = mfma32(wgu[s], af[n % (RD + 1)], acc[t & 1]);
        SB();
        if constexpr (t > 0) {
          if (s == 0) { EP_RD(t - 1); EP_UV(0); }
          if (s == 2) EP_ST();
          static_for<0, 8>([&](auto EE) __attribute__((always_inline)) { constexpr int e = EE.value, j = s - 3 - 2 * e; if constexpr (j >= 0 && j < 9) EP_STAGE(acc[(t - 1) & 1], e, j); });
          if (s == 9) { SB(); EP_UV(1); }
          if (s == 26) EP_OUT(t - 1);
          if (s == 2 && t < 3) asm volatile("s_barrier" ::: "memory");
          if constexpr (t == 1 && s % 3 == 1 && s >= 4 && s < 25) PARKD(s / 3 - 1);
          if constexpr (t == 2 && s % 6 == 4 && s < 18) PARKD(7 + s / 6);
          if (s == 12 && t == 3) {
            WAIT_VM(6);
            if (lane == 0 && __hip_atomic_fetch_add((LAS u32*)(lb + L_SYNC + 68), 1u, __ATOMIC_RELAXED, __HIP_MEMORY_SCOPE_WORKGROUP) == 6u) flag32(ws + OFF_HF + ((gi * 16 + gfj) * 8 + 7) * 128, tag);
          }
          if (s == 27 && t == 1) STAMPL(20, "gu tile1");
          if (s == 27 && t == 2) STAMPL(21, "gu tile2");
          if (s == 27 && t == 3) STAMPL(22, "gu tile3");
          if constexpr (t < 3 ? (s % 6 == 1 && s < 24) : (s % 2 == 1 && s < 20)) { constexpr int f = t < 3 ? 4 * (t - 1) + s / 6 : 8 + s / 2; wdf[f] = ld128(wsr, lane * 16, wdo + (u32)((f & 1) * 112 + (f >> 1)) * 1024); }
          SB();
        }
      }
    });
    asm volatile("s_nop 11" : "+v"(acc[1]));
    EP_RD(3); EP_UV(0); EP_ST();
    {
      f32x4 uw[4];
      uw[0] = lds128f(ub + 32); uw[1] = lds128f(ub + 96); uw[2] = lds128f(ub + 160); uw[3] = lds128f(ub + 224);
      float g8[8], u8[8];
#pragma unroll
      for (int e = 0; e < 8; ++e) {
        const f32x4* W = e < 4 ? uv : uw;
        g8[e] = fma1(acc[1][e], rs, fma1(nm, W[0][e & 3], W[2][e & 3])); u8[e] = fma1(acc[1][8 + e], rs, fma1(nm, W[1][e & 3], W[3][e & 3]));
      }
#pragma unroll
      for (int e = 0; e < 8; ++e) hv[e] = (g8[e] * u8[e]) * __builtin_amdgcn_rcpf(1.f + __builtin_amdgcn_exp2f(g8[e] * fmaf(g8[e] * g8[e], K3, k1v)));
    }
    EP_OUT(3);
    WAIT_VM(0);
    if (lane == 0) flag32(ws + OFF_HF + ((gi * 16 + gfj) * 8 + wave) * 128, tag);
  } else {
    __builtin_amdgcn_s_setprio(3);
    ADMA(1); ADMA(2); ADMA(3);
    WAIT_VM(0); BAR();
#pragma unroll
    for (int f = 0; f < 18; ++f) wdf[f] = ld128(wsr, lane * 16, wdo + (u32)((f & 1) * 112 + (f >> 1)) * 1024);
    asm volatile("s_barrier" ::: "memory");
#pragma unroll
    for (int i = 0; i < 7; ++i) PARKD(i);
    asm volatile("s_barrier" ::: "memory");
#pragma unroll
    for (int i = 7; i < 10; ++i) PARKD(i);
    if (dact) {

      lds_wait(lb + L_SYNC + 68, 7);

      wait_flags(ws + OFF_HF + ((dblk * 16 + (lane & 15)) * 8 + 7) * 128, tag);
    }
    WAIT_VM(0);
    __builtin_amdgcn_s_setprio(0);
  }
  STAMPL(23, "h flagged");
  if (dact) {
    BAR();
    constexpr int HR = 9;
    u32x4 hf[HR];
    const int dcol = 64 * dc + 32 * dn + 16 * dgp + 8 * hi;
#define LDH(i) hf[(i) % HR] = ld128(wsr, lane * 16 + (((i) % 14) & 3) * 1024, OFF_HB + (u32)((drt + 2 * ((i) / 14)) * 112 + 14 * wave + (((i) % 14) & ~3)) * 1024)
#pragma unroll
    for (int i = 0; i < 4; ++i) { LDH(i); SB(); }
#pragma unroll
    for (int f = 18; f < 28; ++f) wdf[f] = lds128(PARKA(f - 18) + lane * 16);
    BAR();
#pragma unroll
    for (int i = 4; i < HR - 1; ++i) { LDH(i); SB(); }
    STAMPL(24, "down go");
    f32x16 c0, c1; u32 late0;
    u32 pwb = lb + wave * 16384 + lane * 16; asm volatile("" : "+v"(pwb));
    static_for<0, 28>([&](auto II) __attribute__((always_inline)) {
      constexpr int i = II.value, m = i / 14, j = i % 14;
      if constexpr (i == LATE_PRE) late0 = ld32(wsr, ((lane >> 3) & 1) * 1024 + min(lane & 7, 6) * 128, OFF_HF + (u32)(dblk * 16 + 2 * wave) * 1024);
      if constexpr (i + HR - 1 == 14) {
        if (!__all(late0 == tag)) wait_flags(ws + OFF_HF + ((dblk * 16 + 2 * wave + ((lane >> 3) & 1)) * 8 + min(lane & 7, 6)) * 128, tag);
        CFENCE();
        STAMPL(27, "late seen");
      }
      if constexpr (i + HR - 1 < 28) LDH(i + HR - 1);
      if constexpr (i == 20) {
        gbA = ld128(wsr, dcol * 4, OFF_MODG + 1120 * 8); gbB = ld128(wsr, dcol * 4 + 16, OFF_MODG + 1120 * 8);
        x2r = ld128(wsr, lane * 16, OFF_X2 + (u32)((drt + 2 * dm) * 28 + 4 * dc + 2 * dn + dgp) * 1024);
      }
      SB();
      if constexpr (j == 0) { c0 = mfma32(wdf[0], hf[i % HR], (f32x16){}); c1 = mfma32(wdf[1], hf[i % HR], (f32x16){}); }
      else { c0 = mfma32(wdf[2 * j], hf[i % HR], c0); c1 = mfma32(wdf[2 * j + 1], hf[i % HR], c1); }
      SB();
      if constexpr (j == 13) {
#pragma unroll
        for (int g = 0; g < 4; ++g) {
          *(LAS f32x4*)(pwb + ((2 * m) * 4 + g) * 1024) = (f32x4){c0[4 * g], c0[4 * g + 1], c0[4 * g + 2], c0[4 * g + 3]};
          *(LAS f32x4*)(pwb + ((2 * m + 1) * 4 + g) * 1024) = (f32x4){c1[4 * g], c1[4 * g + 1], c1[4 * g + 2], c1[4 * g + 3]};
        }
      }
    });
    STAMPL(25, "down mfma done");
    BAR();
    f32x4 pa[8], pb[8];
    {
      u32 base = lb + (2 * dm + dn) * 4096 + dgp * 2048 + lane * 16, base2 = base + 65536;
      asm volatile("" : "+v"(base), "+v"(base2));
#pragma unroll
      for (int w = 0; w < 4; ++w) {
        pa[w] = lds128f(base + w * 16384); pb[w] = lds128f(base + w * 16384 + 1024);
        pa[4 + w] = lds128f(base2 + w * 16384); pb[4 + w] = lds128f(base2 + w * 16384 + 1024);
      }
      SB();
    }
    f32x4 a0 = ((pa[0] + pa[4]) + (pa[1] + pa[5])) + ((pa[2] + pa[6]) + (pa[3] + pa[7])), a1 = ((pb[0] + pb[4]) + (pb[1] + pb[5])) + ((pb[2] + pb[6]) + (pb[3] + pb[7]));
    asm volatile("" : "+v"(a0), "+v"(a1));
    float yv[8];
#pragma unroll
    for (int r = 0; r < 4; ++r) { auto sw = __builtin_amdgcn_permlane32_swap(__float_as_uint(a0[r]), __float_as_uint(a1[r]), false, false); yv[r] = __uint_as_float(sw[0]); yv[4 + r] = __uint_as_float(sw[1]); }
    const u32 gw[4] = {gbA[0], gbA[2], gbB[0], gbB[2]};
    u32x4 ov;
#pragma unroll
    for (int j = 0; j < 4; ++j) ov[j] = pk2(fmaf(bflo(gw[j]), yv[2 * j], bflo(x2r[j])), fmaf(bfhi(gw[j]), yv[2 * j + 1], bfhi(x2r[j])));
    *(u32x4*)(out + (size_t)(32 * (drt + 2 * dm) + l31) * 448 + dcol) = ov;
    if (b == 0 && tid == 0) *(GAS u32*)ws = tag;
  }
  STAMPL(26, "end");
}

extern "C" void kernel_launch(void* const* d_in, const int* in_sizes, int n_in, void* d_out, int out_size, void* d_ws, size_t ws_size, hipStream_t stream) {
  fused_block<<<256, 512, 0, stream>>>((const u16*)d_in[0], (const u16*)d_in[1], (const u16*)d_in[2], (const u16*)d_in[4], (const u16*)d_in[5],
                                       (const u16*)d_in[6], (const u16*)d_in[7], (const u16*)d_in[8], (u16*)d_out, (char*)d_ws);
  probe_spin<<<1, 64, 0, stream>>>((char*)d_ws, PROBE_MUL);
}
```

```cpp
#include <hip/hip_runtime.h>
#include <stdint.h>

typedef unsigned short u16;
typedef unsigned u32;
typedef unsigned long long u64;
typedef __attribute__((ext_vector_type(2))) unsigned u32x2;
typedef __attribute__((ext_vector_type(4))) unsigned u32x4;
typedef __attribute__((ext_vector_type(2))) float f32x2;
typedef __attribute__((ext_vector_type(4))) float f32x4;
typedef __attribute__((ext_vector_type(16))) float f32x16;
typedef __attribute__((ext_vector_type(2))) __bf16 bf16x2;
typedef __attribute__((ext_vector_type(8))) __bf16 bf16x8;
typedef __attribute__((ext_vector_type(4))) short s16x4;
#define DEV __device__ __forceinline__
#define GAS __attribute__((address_space(1)))
#define LAS __attribute__((address_space(3)))
#define RLX_AGENT __ATOMIC_RELAXED, __HIP_MEMORY_SCOPE_AGENT

constexpr u32 OFF_MODG0 = 4096, MODG_REP = 11264;
constexpr u32 OFF_X2G = 131072, OFF_UV = 13467648  , OFF_WQT = 524288, OFF_WOT = 1900544, OFF_WGU = 2359296, OFF_WDT = 5570560;
constexpr u32 OFF_KV = 7176192, OFF_OB = 11370496, OFF_X2 = 15302656, OFF_HB = 17137664, OFF_XH = 24477696;
constexpr u32 OFF_XF = 26312704, OFF_WQF = OFF_XF + 512 * 128, OFF_KVG = OFF_WQF + 1024 * 128, OFF_OF = OFF_KVG + 1024 * 128, OFF_HF = OFF_OF + 2048 * 128, OFF_PREPF = OFF_HF + 2048 * 128, WS_USED = OFF_PREPF + 256 * 128;
constexpr u32 OFF_STAMP = 29360128;
constexpr u32 L_STG = 90112  , L_Q = 131072, L_GAM = 139264, L_QN = 142848;
constexpr u32 L_PARK = 64512 + 1024  ;
constexpr u32 L_SP2 = 151552  , L_MODP = L_SP2, L_MX = L_SP2, L_LP = L_SP2, L_UV = 155648  , L_ST = 157696  , L_G2 = 159488  ;
constexpr u32 L_SYNC = 160000;
constexpr u32 LDS_BYTES = 163840;
constexpr int CODE_BYTES = 42000;
constexpr int SPIN_CAP = 1 << 15;
#define FIRST_POLL 265
#define KV_SLEEP 1
#define MOD_POLL 250
#define NREP 8
#define GATE 0
#define XP0 8
#define PRETOUCH 0
#define NOCODEPF 0
#define O_SLEEP 20
#define X_SLEEP 8
#define NBA 23
#define TOUCH_AT 0
#define LATE_PRE 5
#define GU_RING 6
#define PREP_START 130
#define POLL_T 0
#define POLL_SLEEP 3

DEV float bflo(u32 w) { return __uint_as_float(w << 16); }
DEV float bfhi(u32 w) { return __uint_as_float(w & 0xffff0000u); }
DEV u32 pk2(float a, float b) { f32x2 v = {a, b}; return __builtin_bit_cast(u32, __builtin_convertvector(v, bf16x2)); }
DEV float rbf(float a) { return bflo(pk2(a, 0.f)); }
DEV float fma1(float a, float b, float c) { float r; asm("v_fma_f32 %0, %1, %2, %3" : "=v"(r) : "v"(a), "v"(b), "v"(c)); return r; }
DEV float dot2(u32 a, u32 b, float c) { return __builtin_amdgcn_fdot2_f32_bf16(__builtin_bit_cast(bf16x2, a), __builtin_bit_cast(bf16x2, b), c, false); }
DEV f32x16 mfma32(u32x4 a, u32x4 b, f32x16 c) { return __builtin_amdgcn_mfma_f32_32x32x16_bf16(__builtin_bit_cast(bf16x8, a), __builtin_bit_cast(bf16x8, b), c, 0, 0, 0); }
DEV f32x4 mfma16(u32x4 a, u32x4 b, f32x4 c) { return __builtin_amdgcn_mfma_f32_16x16x32_bf16(__builtin_bit_cast(bf16x8, a), __builtin_bit_cast(bf16x8, b), c, 0, 0, 0); }
DEV float xhalf_sum(float v) { auto r = __builtin_amdgcn_permlane32_swap(__float_as_uint(v), __float_as_uint(v), false, false); return __uint_as_float(r[0]) + __uint_as_float(r[1]); }
DEV float xhalf_max(float v) { auto r = __builtin_amdgcn_permlane32_swap(__float_as_uint(v), __float_as_uint(v), false, false); return fmaxf(__uint_as_float(r[0]), __uint_as_float(r[1])); }
#define DPPF(v, ctrl) __uint_as_float(__builtin_amdgcn_update_dpp(0u, __float_as_uint(v), ctrl, 0xf, 0xf, false))
DEV float wave_max(float v) {
  v = fmaxf(v, DPPF(v, 0x121)); v = fmaxf(v, DPPF(v, 0x122)); v = fmaxf(v, DPPF(v, 0x124)); v = fmaxf(v, DPPF(v, 0x128));
  auto r = __builtin_amdgcn_permlane16_swap(__float_as_uint(v), __float_as_uint(v), false, false);
  return xhalf_max(fmaxf(__uint_as_float(r[0]), __uint_as_float(r[1])));
}
template <int I> struct IC { static constexpr int value = I; };
template <int I, int N, class F> DEV void static_for(F&& f) { if constexpr (I < N) { f(IC<I>{}); static_for<I + 1, N>(f); } }
#define WAIT_VM(n) asm volatile("s_waitcnt vmcnt(" #n ")" ::: "memory")
#define BAR() asm volatile("s_waitcnt lgkmcnt(0)\n\ts_barrier" ::: "memory")
#define SB() __builtin_amdgcn_sched_barrier(0)
#define CFENCE() asm volatile("" ::: "memory")
template <bool SC1> DEV void dma16(const void* sbase, u32 voff, u32 lds_dst) {
  u32 keep;
  if (SC1) asm volatile("s_mov_b32 %0, m0\n\ts_mov_b32 m0, %3\n\ts_nop 2\n\tglobal_load_lds_dwordx4 %1, %2 sc1\n\ts_mov_b32 m0, %0" : "=&s"(keep) : "v"(voff), "s"(sbase), "s"(lds_dst) : "memory");
  else asm volatile("s_mov_b32 %0, m0\n\ts_mov_b32 m0, %3\n\ts_nop 2\n\tglobal_load_lds_dwordx4 %1, %2\n\ts_mov_b32 m0, %0" : "=&s"(keep) : "v"(voff), "s"(sbase), "s"(lds_dst) : "memory");
}
DEV void dma16x4(const void* sbase, u32 voff, u32 lds_dst) {
  u32 keep;
  asm volatile("s_mov_b32 %0, m0\n\ts_mov_b32 m0, %3\n\ts_nop 2\n\tglobal_load_lds_dwordx4 %1, %2 sc1\n\tglobal_load_lds_dwordx4 %1, %2 offset:1024 sc1\n\t"
               "global_load_lds_dwordx4 %1, %2 offset:2048 sc1\n\tglobal_load_lds_dwordx4 %1, %2 offset:3072 sc1\n\ts_mov_b32 m0, %0" : "=&s"(keep) : "v"(voff), "s"(sbase), "s"(lds_dst) : "memory");
}
DEV void dma4_touch(const void* sbase, u32 voff, u32 lds_dst) {
  u32 keep;
  asm volatile("s_mov_b32 %0, m0\n\ts_mov_b32 m0, %3\n\ts_nop 2\n\tglobal_load_lds_dword %1, %2 sc1\n\ts_mov_b32 m0, %0" : "=&s"(keep) : "v"(voff), "s"(sbase), "s"(lds_dst) : "memory");
}
DEV void dma4_plain(const void* sbase, u32 voff, u32 lds_dst) {
  u32 keep;
  asm volatile("s_mov_b32 %0, m0\n\ts_mov_b32 m0, %3\n\ts_nop 2\n\tglobal_load_lds_dword %1, %2\n\ts_mov_b32 m0, %0" : "=&s"(keep) : "v"(voff), "s"(sbase), "s"(lds_dst) : "memory");
}
DEV u32x4 lds128(u32 a) { return *(const LAS u32x4*)a; }
DEV f32x4 lds128f(u32 a) { return *(const LAS f32x4*)a; }
DEV float lds32f(u32 a) { return *(const LAS float*)a; }
DEV u32x2 ldstr(u32 a) { return __builtin_bit_cast(u32x2, __builtin_amdgcn_ds_read_tr16_b64_v4i16((LAS s16x4*)a)); }
DEV u32x4 cat(u32x2 a, u32x2 b) { return (u32x4){a.x, a.y, b.x, b.y}; }
typedef __amdgpu_buffer_rsrc_t rsrc_t;
DEV u32x4 ld128(rsrc_t r, u32 voff, u32 soff) { return __builtin_amdgcn_raw_buffer_load_b128(r, voff, soff, 16); }
DEV u32 ld32(rsrc_t r, u32 voff, u32 soff) { return __builtin_amdgcn_raw_buffer_load_b32(r, voff, soff, 16); }
DEV void st128(rsrc_t r, u32 voff, u32 soff, u32x4 v) {
  __builtin_amdgcn_raw_buffer_store_b128(v, r, voff, soff, 16);
  asm volatile("s_nop 1" :: "v"(v) : "memory");
}
DEV void st32(rsrc_t r, u32 voff, u32 soff, u32 v) { __builtin_amdgcn_raw_buffer_store_b32(v, r, voff, soff, 16); }
constexpr u32 POISON = 0xFFFFFFFFu;
DEV u32x4 nopoison(u32x4 v) { return (u32x4){min(v[0], POISON - 1u), min(v[1], POISON - 1u), min(v[2], POISON - 1u), min(v[3], POISON - 1u)}; }
DEV u32 umax4(u32x4 v) { return max(max(v[0], v[1]), max(v[2], v[3])); }
DEV u32 poll32(const char* p) { return __hip_atomic_load((GAS u32*)p, RLX_AGENT); }
DEV u64 poll64(const char* p) { return __hip_atomic_load((GAS u64*)p, RLX_AGENT); }
DEV void flag32(char* p, u32 v) { __hip_atomic_store((GAS u32*)p, v, RLX_AGENT); }
DEV void flag64(char* p, u32 val, u32 tag) { __hip_atomic_store((GAS u64*)p, ((u64)tag << 32) | val, RLX_AGENT); }
DEV void wait_flags(const char* p, u32 tag) {
#pragma nounroll
  for (int i = 0; i < SPIN_CAP; ++i) { if (__all(poll32(p) == tag)) break; __builtin_amdgcn_s_sleep(POLL_SLEEP); }
  CFENCE();
}
DEV u32 wait_gran(const char* p, u32 tag) {
  u64 g = 0;
#pragma nounroll
  for (int i = 0; i < SPIN_CAP; ++i) { g = poll64(p); if (__all((u32)(g >> 32) == tag)) break; __builtin_amdgcn_s_sleep(POLL_SLEEP); }
  CFENCE();
  return (u32)g;
}
DEV u32 lds_ld(u32 a) { return __hip_atomic_load((LAS u32*)a, __ATOMIC_RELAXED, __HIP_MEMORY_SCOPE_WORKGROUP); }
DEV void lds_st(u32 a, u32 v) { __hip_atomic_store((LAS u32*)a, v, __ATOMIC_RELAXED, __HIP_MEMORY_SCOPE_WORKGROUP); }
DEV u32 lds_inc(u32 a) { return __builtin_amdgcn_readfirstlane(__hip_atomic_fetch_add((LAS u32*)a, 1u, __ATOMIC_RELAXED, __HIP_MEMORY_SCOPE_WORKGROUP)); }
DEV void lds_wait(u32 a, u32 want) {
#pragma nounroll
  for (int i = 0; i < (SPIN_CAP << 6); ++i) { if (__builtin_amdgcn_readfirstlane(lds_ld(a)) >= want) break; __builtin_amdgcn_s_sleep(1); }
  CFENCE();
}
#define WAIT_LGKM() asm volatile("s_waitcnt lgkmcnt(0)" ::: "memory")
#define STAMP(i) do {} while (0)
#define STAMPL(i, s) STAMP(i)

__global__ __launch_bounds__(512, 2) void fused_block(const u16* __restrict__ x, const u16* __restrict__ wqkv, const u16* __restrict__ wo,
                                                      const u16* __restrict__ wg, const u16* __restrict__ wu, const u16* __restrict__ wd,
                                                      const u16* __restrict__ cvec, const u16* __restrict__ wmod, u16* __restrict__ out, char* ws) {
  __shared__ __attribute__((aligned(16))) char smem[LDS_BYTES];
  const int b = blockIdx.x, tid = threadIdx.x, lane = tid & 63;
  const int wave = __builtin_amdgcn_readfirstlane(tid >> 6);
  const int l31 = lane & 31, hi = lane >> 5;
  const int xg = b & 7, slot = b >> 3;
  const int ai = 4 * (xg >> 1) + (slot & 3), ah = 8 * (xg & 1) + (slot >> 2);
  const u32 lb = (u32)(uintptr_t)(LAS char*)smem;
  const rsrc_t wsr = __builtin_amdgcn_make_buffer_rsrc(ws, 0, WS_USED, 0x00020000);
  STAMP(0);
  {
    u64 pc; asm volatile("s_getpc_b64 %0" : "=s"(pc));
    if (wave == 0 && !NOCODEPF) {
      const u32 off = min((u32)(slot * 1408 + min(lane, 10) * 128), (u32)CODE_BYTES);
      u32 keep;
      asm volatile("s_mov_b32 %0, m0\n\ts_mov_b32 m0, %3\n\ts_nop 2\n\tglobal_load_lds_dword %1, %2\n\ts_mov_b32 m0, %0" : "=&s"(keep) : "v"(off), "s"(pc), "s"(lb + L_ST) : "memory");
    }
  }
  asm volatile("" :: "s"(x), "s"(wqkv), "s"(wo), "s"(wg), "s"(wu), "s"(wd), "s"(cvec), "s"(wmod), "s"(out), "s"(ws) : "memory");
  SB();
  if (PRETOUCH >= 1 && wave == 1)
    dma4_plain(ws, lane < 16 || PRETOUCH < 2 ? min(lane, 12) << 21 : min(slot * 14 + min(lane - 16, 13), 415) << 16, lb + L_ST + 256);
  if (PRETOUCH >= 2 && wave == 3) dma4_plain(out, min(lane, 27) << 16, lb + L_ST + 512);
  if (wave == 2 && lane < 32) lds_st(lb + L_SYNC + lane * 4, 0u);
  const u64 t_entry = __builtin_amdgcn_s_memrealtime();
  const u32 epoch_v = poll32(ws);
  u32 tag;

  f32x16 accQ, accK, accV;
  const int ort = 8 * xg + (slot >> 2), ocq = slot & 3;
  const u32 OFF_MODG = OFF_MODG0 + (NREP == 8 ? xg : 0) * MODG_REP;
  const int tq4 = (lane & 15) >> 2, tp4 = lane & 3, grp = lane >> 4;
  if (b < 42) {
    u32x4 wv[7];
    const u32 cv = cvec[56 * wave + min(lane, 55)];
#pragma unroll
    for (int n = 0; n < 7; ++n) wv[n] = *(const u32x4*)((const char*)wmod + (size_t)(56 * wave + 8 * n + (lane >> 3)) * 5376 + 128 * b + 16 * (lane & 7));
    BAR();
    const float cf = bflo(cv);
    const float sl = rbf(cf * __builtin_amdgcn_rcpf(1.f + __builtin_amdgcn_exp2f(-1.4426950408889634f * cf)));
    float acc[8] = {0.f, 0.f, 0.f, 0.f, 0.f, 0.f, 0.f, 0.f};
#pragma unroll
    for (int n = 0; n < 7; ++n) {
      const float sn = __uint_as_float(__builtin_amdgcn_ds_bpermute((8 * n + (lane >> 3)) * 4, __float_as_uint(sl)));
#pragma unroll
      for (int j = 0; j < 4; ++j) { acc[2 * j] = fmaf(sn, bflo(wv[n][j]), acc[2 * j]); acc[2 * j + 1] = fmaf(sn, bfhi(wv[n][j]), acc[2 * j + 1]); }
    }

#pragma unroll
    for (int j = 0; j < 8; ++j) acc[j] += DPPF(acc[j], 0x128);
#pragma unroll
    for (int j = 0; j < 4; ++j) { auto r = __builtin_amdgcn_permlane16_swap(__float_as_uint(acc[j]), __float_as_uint(acc[j + 4]), false, false); acc[j] = __uint_as_float(r[0]) + __uint_as_float(r[1]); }
#pragma unroll
    for (int j = 0; j < 2; ++j) { auto r = __builtin_amdgcn_permlane32_swap(__float_as_uint(acc[j]), __float_as_uint(acc[j + 2]), false, false); acc[j] = __uint_as_float(r[0]) + __uint_as_float(r[1]); }
    if (!(lane & 8)) *(LAS f32x2*)(lb + L_MODP + (wave * 64 + 8 * (lane & 7) + 2 * hi + 4 * (grp & 1)) * 4) = (f32x2){acc[0], acc[1]};
    WAIT_LGKM();
    u32 arrived = 0;
    if (lane == 0) arrived = __hip_atomic_fetch_add((LAS u32*)(lb + L_SYNC + 12), 1u, __ATOMIC_RELAXED, __HIP_MEMORY_SCOPE_WORKGROUP);
    if (__builtin_amdgcn_readfirstlane(arrived) == 7) {
      CFENCE();
      f32x2 t = {0.f, 0.f};
#pragma unroll
      for (int p = 0; p < 8; ++p) t += *(const LAS f32x2*)(lb + L_MODP + (p * 64 + 2 * l31) * 4);
      const u32 pv = pk2(t[0], t[1]);
      const u32 g0 = __builtin_amdgcn_ds_bpermute((2 * (lane & 15)) * 4, pv), g1 = __builtin_amdgcn_ds_bpermute((2 * (lane & 15) + 1) * 4, pv);
#pragma unroll
      for (int r = 0; r < NREP / 4; ++r) st128(wsr, (u32)(32 * b + 2 * (lane & 15)) * 8 + (4 * r + grp) * MODG_REP, OFF_MODG0, (u32x4){g0, epoch_v + 1u, g1, epoch_v + 1u});
      lds_st(lb + L_SYNC + 20, 1u);
      WAIT_VM(0);
      lds_st(lb + L_SYNC + 32, 1u);
    }
  } else if (wave < 3 && 3 * (b - 42) + wave < 512) {
    const int xgp = 3 * (b - 42) + wave, row = 4 * xgp + (lane & 3), c0i = lane >> 2;
    u32x4 xr[4];
#pragma unroll
    for (int n = 0; n < 4; ++n) xr[n] = *(const u32x4*)((const char*)x + (size_t)row * 896 + min(c0i + 16 * n, 55) * 16);
    BAR();
    if (c0i >= 8) xr[3] = (u32x4){0u, 0u, 0u, 0u};
    float s1[2] = {0.f, 0.f}, s2[2] = {0.f, 0.f};
#pragma unroll
    for (int n = 0; n < 4; ++n)
#pragma unroll
      for (int j = 0; j < 4; ++j) { s1[j & 1] = dot2(xr[n][j], 0x3F803F80u, s1[j & 1]); s2[j & 1] = dot2(xr[n][j], xr[n][j], s2[j & 1]); }
    float t1 = s1[0] + s1[1], t2 = s2[0] + s2[1];
    t1 += DPPF(t1, 0x124); t2 += DPPF(t2, 0x124); t1 += DPPF(t1, 0x128); t2 += DPPF(t2, 0x128);
    { auto r = __builtin_amdgcn_permlane16_swap(__float_as_uint(t1), __float_as_uint(t2), false, false); t1 = __uint_as_float(r[0]) + __uint_as_float(r[1]); }
    t1 = xhalf_sum(t1);
    { auto r = __builtin_amdgcn_permlane16_swap(__float_as_uint(t1), __float_as_uint(t1), false, false); t1 = __uint_as_float(r[0]); t2 = __uint_as_float(r[1]); }
    const float mean = t1 * (1.f / 448.f), ex2 = t2 * (1.f / 448.f);
    const float rstd = __builtin_amdgcn_rsqf(fmaxf(ex2 - mean * mean, 0.f) + 1e-5f), c0 = -mean * rstd;
    asm volatile("" :: "v"(c0));

#pragma unroll
    for (int n = 0; n < 4; ++n) {
      u32x4 o;
#pragma unroll
      for (int j = 0; j < 4; ++j) asm("v_cvt_pk_f16_f32 %0, %1, %2" : "=v"(o[j]) : "v"(fmaf(bflo(xr[n][j]), rstd, c0)), "v"(fmaf(bfhi(xr[n][j]), rstd, c0)));
      const u32 c = c0i + 16 * n;
      st128(wsr, c < 56 ? (c >> 1) * 1024 + ((row & 31) + 32 * (c & 1)) * 16 + (u32)(row >> 5) * 28672 : 0x80000000u, OFF_XH, o);
    }
    WAIT_VM(0);
    if (lane == 0) flag32(ws + OFF_XF + xgp * 128, epoch_v + 1u);
  } else if ((wave == 4 || wave == 5) && b < 210) {
    const int u = b - 42, part = u / 56, rem = u - part * 56, hp = rem / 7, kg = rem - hp * 7, s0 = 4 * kg + 2 * (wave - 4);
    const char* src = (const char*)wqkv + (size_t)(16 * s0) * 2688 + part * 896 + hp * 112;
    const u32 stg = lb + L_Q + (wave - 4) * 4096;
#pragma unroll
    for (int i = 0; i < 4; ++i) { const u32 c = lane & 7; dma16<false>(src + i * 8 * 2688, (lane >> 3) * 2688 + 16 * (c == 7 ? 6 : c), stg + i * 1024); }
    BAR();
    WAIT_VM(0);

#pragma unroll
    for (int e = 0; e < 4; ++e) {
      const u32 a = stg + (16 * (e >> 1) + 8 * (grp >> 1) + tq4) * 128 + (e & 1) * 56 + (grp & 1) * 32 + tp4 * 8;
      u32x4 f = cat(ldstr(a), ldstr(a + 512));
      if (l31 >= 28) f = (u32x4){0u, 0u, 0u, 0u};
      st128(wsr, lane * 16, OFF_WQT + (u32)((2 * hp + (e & 1)) * 84 + 3 * (s0 + (e >> 1)) + part) * 1024, f);
    }
    WAIT_VM(0);
    if (lane < 2) flag32(ws + OFF_WQF + ((2 * hp + lane) * 64 + 3 * (s0 >> 1) + part) * 128, epoch_v + 1u);
  } else {
    BAR();
  }
  tag = __builtin_amdgcn_readfirstlane(epoch_v) + 1u;
  STAMPL(7, "unit done");
  const int cg = b / 7, kg = b - cg * 7;
  if (wave >= 6) {
#pragma nounroll
    for (int i = 0; i < 4096 && __builtin_amdgcn_s_memrealtime() - t_entry < PREP_START; ++i) __builtin_amdgcn_s_sleep(1);
    if (b < 42) lds_wait(lb + L_SYNC + 20, 1);
#pragma unroll
    for (int ph = 0; ph < 2; ++ph) {
      const int pw = wave & 1, tw = 2 * ph + pw, p = 4 * b + tw;
      const u32 stg = lb + L_STG + tw * 6144;
      if (b < 196) {
#pragma unroll
        for (int i = 0; i < 2; ++i) {
          const u32 vo = (u32)(64 * kg + 32 * ph + 16 * i + (lane >> 2)) * 3584 + (64 * cg + 32 * pw) * 2 + 16 * (lane & 3);
          dma16<false>(wg, vo, stg + (2 * i) * 1024); dma16<false>(wu, vo, stg + (2 * i + 1) * 1024);
        }
      }
      if (p < 784) {
        const int ds_ = p / 7, np = p - ds_ * 7;
#pragma unroll
        for (int i = 0; i < 2; ++i) dma16<false>(wd, (u32)(16 * ds_ + (lane >> 2)) * 896 + np * 128 + i * 64 + 16 * (lane & 3), stg + (4 + i) * 1024);
      } else if (p < 1008) {
        const int q = p - 784, h = q / 14, nq = q - h * 14;
#pragma unroll
        for (int i = 0; i < 2; ++i) dma16<false>(wo, (u32)min(28 * h + 16 * i + (lane >> 2), 447) * 896 + nq * 64 + 16 * (lane & 3), stg + (4 + i) * 1024);
      }
    }
    STAMPL(6, "prep issued");
    WAIT_VM(0);
    if (lane == 0) __hip_atomic_fetch_add((LAS u32*)(lb + L_SYNC + 52), 1u, __ATOMIC_RELAXED, __HIP_MEMORY_SCOPE_WORKGROUP);
    STAMPL(5, "prep landed");
  } else {
#pragma nounroll
    for (int i = 0; i < 4096 && __builtin_amdgcn_s_memrealtime() - t_entry < (wave == 4 ? MOD_POLL : wave >= 3 ? FIRST_POLL : TOUCH_AT); ++i) __builtin_amdgcn_s_sleep(1);
    if (TOUCH_AT && wave < 3) {
      dma4_touch(ws + OFF_WQT + (size_t)ah * 86016 + (slot & 3) * 21504, min(wave * 64 + lane, 167) * 128, lb + L_ST + wave * 256);
      if (wave < 2) dma4_touch(ws + OFF_XH + (size_t)ai * 114688 + (slot >> 2) * 14336, min(wave * 64 + lane, 111) * 128, lb + L_ST + 768 + wave * 256);
    }
  }

  if (wave < 4) {
    u32x4 xf[28], pf[6]; u32 pfo[6], uvo[2]; float uvv[2];
    const int pw = wave & 1, ph = (wave >> 1) & 1;
    auto tile_read = [&]() __attribute__((always_inline)) {
      const int p = 4 * b + wave;
      const u32 stg = lb + L_STG + wave * 6144;
      const bool isd = p < 784;
      const int ds_ = p / 7, np = p - ds_ * 7, q = p - 784, h = q / 14, nq = q - h * 14;
      lds_wait(lb + L_SYNC + 52, 2);
      const int pi = ((tp4 & 1) << 1) | (tp4 >> 1);
#pragma unroll
      for (int i = 0; i < 2; ++i)
#pragma unroll
        for (int e = 0; e < 2; ++e) {
          const u32 a = stg + (2 * i + (grp & 1)) * 1024 + (8 * (grp >> 1) + tq4) * 64 + e * 32 + pi * 8;
          pf[2 * i + e] = cat(ldstr(a), ldstr(a + 256));
          pfo[2 * i + e] = lane * 16 + (b < 196 ? OFF_WGU + (u32)((4 * cg + 2 * pw + e) * 28 + 4 * kg + 2 * ph + i) * 1024 : 0x80000000u);
        }
#pragma unroll
      for (int i = 0; i < 2; ++i) {
        const u32 a = isd ? stg + (4 + i) * 1024 + (8 * (grp >> 1) + tq4) * 64 + (grp & 1) * 32 + tp4 * 8
                          : stg + 4096 + (16 * (grp >> 1) + 4 * (grp & 1) + tq4) * 64 + i * 32 + tp4 * 8;
        u32x4 f = cat(ldstr(a), ldstr(a + (isd ? 256 : 512)));
        if (!isd && grp == 3) { f[2] = 0u; f[3] = 0u; }
        pf[4 + i] = f;
        pfo[4 + i] = lane * 16 + (isd ? OFF_WDT + (u32)((2 * np + i) * 112 + ds_) * 1024 : p < 1008 ? OFF_WOT + (u32)((2 * nq + i) * 16 + h) * 1024 : 0x80000000u);
        uvo[i] = lane * 4 + (b < 196 ? OFF_UV + (u32)((2 * kg + ph) * 112 + 4 * cg + 2 * pw + i) * 256 : 0x80000000u);
      }
      asm volatile("" : "+v"(pf[0]), "+v"(pf[1]), "+v"(pf[2]), "+v"(pf[3]), "+v"(pf[4]), "+v"(pf[5]));
    };
    if (wave == 3) { wait_flags(ws + OFF_XF + (32 * ai + l31) * 128, tag); STAMPL(1, "xhat seen"); if (GATE) lds_wait(lb + L_SYNC + 36, 1); lds_st(lb + L_SYNC, 1u); }
    else { tile_read(); lds_wait(lb + L_SYNC, 1); }
    STAMPL(3, "xhat go");
    constexpr int XPRE = 12;
#define LDX(s) xf[s] = ld128(wsr, lane * 16 + ((s) & 3) * 1024, OFF_XH + (u32)((4 * ai + wave) * 28 + ((s) & ~3)) * 1024)
#pragma unroll
    for (int s = 0; s < XP0; ++s) { LDX(s); SB(); }
    if (wave == 3) tile_read();
    if (!GATE) lds_wait(lb + L_SYNC + 36, 1);
    {
      float uu[2] = {0.f, 0.f}, vv[2] = {0.f, 0.f};
#pragma unroll
      for (int i = 0; i < 2; ++i) {
        const u32x4 be = lds128(lb + L_G2 + (2 * ph + i) * 32 + hi * 16), ga = lds128(lb + L_G2 + 128 + (2 * ph + i) * 32 + hi * 16);
#pragma unroll
        for (int e = 0; e < 2; ++e)
#pragma unroll
          for (int j = 0; j < 4; ++j) {
            const u32 w = pf[2 * i + e][j];
            vv[e] = dot2(w, be[j], vv[e]);
            pf[2 * i + e][j] = pk2(bflo(w) * bflo(ga[j]), bfhi(w) * bfhi(ga[j]));
            uu[e] = dot2(pf[2 * i + e][j], 0x3F803F80u, uu[e]);
          }
      }
#pragma unroll
      for (int e = 0; e < 2; ++e) { const float us = xhalf_sum(uu[e]), vs = xhalf_sum(vv[e]); uvv[e] = hi ? vs : us; }
      asm volatile("" : "+v"(pf[0]), "+v"(pf[1]), "+v"(pf[2]), "+v"(pf[3]), "+v"(uvv[0]), "+v"(uvv[1]));
      STAMPL(10, "prep tiled");
    }
#pragma unroll
    for (int s = XP0; s < XPRE; ++s) { LDX(s); SB(); }
    lds_wait(lb + L_SYNC + 4, 4);

    STAMPL(8, "W1 ready");
    accQ = (f32x16){}; accK = (f32x16){}; accV = (f32x16){};
    {
      u32 gb = lb + L_GAM + hi * 16; asm volatile("" : "+v"(gb));
      u32 wb = lb + lane * 16, wb2 = wb + 43008; asm volatile("" : "+v"(wb), "+v"(wb2));
      u32x4 wf[3][3], hf[2], gs[3], be[3]; float ht[8];
#define RDG(s) gs[(s) % 3] = lds128(gb + (s) * 32)
#define RDB(s) be[(s) % 3] = lds128(gb + 896 + (s) * 32)
#define RDW1(s, p) wf[(s) % 3][p] = lds128(((s) < 14 ? wb : wb2) + (3 * ((s) % 14) + (p)) * 1024)
#define RDW(s) do { RDW1(s, 0); RDW1(s, 1); RDW1(s, 2); } while (0)
#define DOTS(s, h) do { _Pragma("unroll") for (int j = 2 * (h); j < 2 * (h) + 2; ++j) { \
        asm volatile("v_fma_mix_f32 %0, %1, %2, %3 op_sel_hi:[1,1,1]" : "=v"(ht[2 * j]) : "v"(xf[s][j]), "v"(gs[(s) % 3][j]), "v"(be[(s) % 3][j])); \
        asm volatile("v_fma_mix_f32 %0, %1, %2, %3 op_sel:[1,1,1] op_sel_hi:[1,1,1]" : "=v"(ht[2 * j + 1]) : "v"(xf[s][j]), "v"(gs[(s) % 3][j]), "v"(be[(s) % 3][j])); } } while (0)
#define CVTS(s) do { hf[(s) & 1] = (u32x4){pk2(ht[0], ht[1]), pk2(ht[2], ht[3]), pk2(ht[4], ht[5]), pk2(ht[6], ht[7])}; asm volatile("" : "+v"(hf[(s) & 1])); } while (0)
      RDG(0); RDB(0); RDW(0); RDG(1); RDB(1); RDW(1); RDG(2); RDB(2); RDW(2); DOTS(0, 0); DOTS(0, 1); CVTS(0); SB();
#pragma unroll
      for (int s = 0; s < 28; ++s) {
        if (s == 1) lds_wait(lb + L_SYNC + 8, 3);
        if (s == 13) lds_wait(lb + L_SYNC + 24, 3);
        SB(); accQ = mfma32(wf[s % 3][0], hf[s & 1], accQ); SB();
        if (s < 25) RDG(s + 3);
        if (s < 25) RDW1(s + 3, 0);
        if (s < 27) DOTS(s + 1, 0);
        SB(); accK = mfma32(wf[s % 3][1], hf[s & 1], accK); SB();
        if (s < 25) RDB(s + 3);
        if (s < 25) RDW1(s + 3, 1);
        if (s < 27) DOTS(s + 1, 1);
        SB(); accV = mfma32(hf[s & 1], wf[s % 3][2], accV); SB();
        if (s < 25) RDW1(s + 3, 2);
        if (s + XPRE < 28) LDX(s + XPRE);
        else if (s < 22) st128(wsr, pfo[s - 16], 0, pf[s - 16]);
        else if (s < 24) st32(wsr, uvo[s - 22], 0, __float_as_uint(uvv[s - 22]));
        if (s < 27) CVTS(s + 1);
        SB();
      }
    }
    STAMPL(5, "qkv mfma done");
    const float unscale = __uint_as_float(__builtin_amdgcn_readfirstlane(lds_ld(lb + L_SYNC + 28)));
    if (unscale != 1.f) { accQ *= unscale; accK *= unscale; accV *= unscale; }
    u32x4 k0, k1, v0, v1, q0, q1;
#pragma unroll
    for (int j = 0; j < 4; ++j) {
      k0[j] = pk2(accK[2 * j], accK[2 * j + 1]); k1[j] = pk2(accK[8 + 2 * j], accK[9 + 2 * j]);
      v0[j] = pk2(accV[2 * j], accV[2 * j + 1]); v1[j] = pk2(accV[8 + 2 * j], accV[9 + 2 * j]);
    }
    if (hi) { k1[2] = 0x3F803F80u; k1[3] = 0x00003F80u; }
    if (l31 == 28) { v0 = (u32x4){0x3F803F80u, 0x3F803F80u, 0x3F803F80u, 0x3F803F80u}; v1 = v0; }
    const u32 kvo = OFF_KV + (u32)(ah * 64 + ai * 4 + wave) * 4096;
    st128(wsr, lane * 16, kvo, k0); st128(wsr, lane * 16 + 1024, kvo, k1); st128(wsr, lane * 16 + 2048, kvo, v0); st128(wsr, lane * 16 + 3072, kvo, v1);
    SB();
    float kn = 0.f, qn = 0.f;
    constexpr float QS = 0.18898223650461362f * 1.4426950408889634f;
#pragma unroll
    for (int r = 0; r < 16; ++r) { kn = fmaf(accK[r], accK[r], kn); accQ[r] *= QS; qn = fmaf(accQ[r], accQ[r], qn); }
#pragma unroll
    for (int j = 0; j < 4; ++j) { q0[j] = pk2(accQ[2 * j], accQ[2 * j + 1]); q1[j] = pk2(accQ[8 + 2 * j], accQ[9 + 2 * j]); }
    *(LAS u32x4*)(lb + L_Q + wave * 2048 + lane * 16) = q0; *(LAS u32x4*)(lb + L_Q + wave * 2048 + 1024 + lane * 16) = q1;
    qn = xhalf_sum(qn); kn = wave_max(xhalf_sum(kn));
    if (!hi) *(LAS float*)(lb + L_QN + (wave * 32 + l31) * 4) = qn;
    WAIT_VM(0);
    if (lane == 0) flag64(ws + OFF_KVG + (ah * 64 + ai * 4 + wave) * 128, __float_as_uint(kn), tag);
    if (b < 42) lds_wait(lb + L_SYNC + 32, 1);
    if (lane == 0 && __hip_atomic_fetch_add((LAS u32*)(lb + L_SYNC + 44), 1u, __ATOMIC_RELAXED, __HIP_MEMORY_SCOPE_WORKGROUP) == 6u) flag32(ws + OFF_PREPF + b * 128, tag);
    STAMPL(6, "kv flagged");
  } else {
    if (wave == 4) {
      u64 gv[8];
      __builtin_amdgcn_s_setprio(3);
#pragma nounroll
      for (int i = 0; i < SPIN_CAP; ++i) {
        bool ok = true;
#pragma unroll
        for (int n = 0; n < 7; ++n) gv[n] = poll64(ws + OFF_MODG + (64 * n + lane) * 8);
        gv[7] = poll64(ws + OFF_MODG + (672 + 224 * hi + 32 * kg + l31) * 8);
#pragma unroll
        for (int n = 0; n < 8; ++n) ok = ok && (u32)(gv[n] >> 32) == tag;
        if (__all(ok)) break;
        __builtin_amdgcn_s_sleep(POLL_SLEEP);
      }
      CFENCE();
      __builtin_amdgcn_s_setprio(0);
      STAMPL(2, "mod seen");
      *(LAS u32*)(lb + L_G2 + lane * 4) = hi ? pk2(1.f + bflo((u32)gv[7]), 1.f + bfhi((u32)gv[7])) : (u32)gv[7];
      WAIT_LGKM();
      lds_st(lb + L_SYNC + 36, 1u);

      float lo[7], hi2[7], mxv = 0.f;
#pragma unroll
      for (int n = 0; n < 7; ++n) {
        u32 v = (u32)gv[n];
        if (64 * n + lane >= 224) v = pk2(1.f + bflo(v), 1.f + bfhi(v));
        lo[n] = bflo(v); hi2[n] = bfhi(v); mxv = fmaxf(mxv, fmaxf(fabsf(lo[n]), fabsf(hi2[n])));
      }
      const u32 ex = max((int)(__float_as_uint(wave_max(mxv)) >> 23) - (127 + 14), 0);
      const float scl = __uint_as_float((127u - min(ex, 114u)) << 23);
#pragma unroll
      for (int n = 0; n < 7; ++n) {
        u32 o; asm("v_cvt_pk_f16_f32 %0, %1, %2" : "=v"(o) : "v"(lo[n] * scl), "v"(hi2[n] * scl));
        const int g = 64 * n + lane;
        *(LAS u32*)(lb + L_GAM + (g >= 224 ? g - 224 : g + 224) * 4) = o;
      }
      lds_st(lb + L_SYNC + 28, (127u + min(ex, 114u)) << 23);
      WAIT_LGKM();
      if (lane == 0) __hip_atomic_fetch_add((LAS u32*)(lb + L_SYNC + 4), 1u, __ATOMIC_RELAXED, __HIP_MEMORY_SCOPE_WORKGROUP);
    } else {
      if (wave == 5) { wait_flags(ws + OFF_WQF + (ah * 64 + min(lane, 41)) * 128, tag); STAMPL(4, "W seen"); if (GATE) lds_wait(lb + L_SYNC + 36, 1); lds_st(lb + L_SYNC + 48, 1u); }
      else lds_wait(lb + L_SYNC + 48, 1);

      const char* wsrc = ws + OFF_WQT + (size_t)ah * 84 * 1024 + (wave - 5) * 4096;
#define WDMA(n) dma16x4(wsrc + (n) * 12288, lane * 16, lb + (wave - 5) * 4096 + (n) * 12288)
      WDMA(0); WDMA(1);
      WAIT_VM(4);
      if (lane == 0) __hip_atomic_fetch_add((LAS u32*)(lb + L_SYNC + 4), 1u, __ATOMIC_RELAXED, __HIP_MEMORY_SCOPE_WORKGROUP);
      WDMA(2); WDMA(3); WDMA(4);
      WAIT_VM(4);
      if (lane == 0) __hip_atomic_fetch_add((LAS u32*)(lb + L_SYNC + 8), 1u, __ATOMIC_RELAXED, __HIP_MEMORY_SCOPE_WORKGROUP);
      WDMA(5); WDMA(6);
      WAIT_VM(0);
      if (lane == 0) __hip_atomic_fetch_add((LAS u32*)(lb + L_SYNC + 24), 1u, __ATOMIC_RELAXED, __HIP_MEMORY_SCOPE_WORKGROUP);
      STAMPL(9, "W landed");
      const u32x4 pz = {POISON, POISON, POISON, POISON};
#pragma unroll
      for (int i = 0; i < 5; ++i) {
        const int k = 5 * (wave - 5) + i;
        st128(wsr, lane * 16, k < 8 ? OFF_OB + (u32)((4 * ai + (k >> 1)) * 16 + ah) * 2048 + (k & 1) * 1024 : OFF_X2 + (u32)(ort * 28 + 7 * ocq + k - 8) * 1024, pz);
      }
      WAIT_VM(0);
      if (lane == 0 && __hip_atomic_fetch_add((LAS u32*)(lb + L_SYNC + 44), 1u, __ATOMIC_RELAXED, __HIP_MEMORY_SCOPE_WORKGROUP) == 6u) flag32(ws + OFF_PREPF + b * 128, tag);
    }
  }
  BAR();
  STAMPL(11, "B3");

  const int gi = ai, gfj = ah;
  u32x4 wo_f[16], wgu[28], xres, gaA, gaB;
  {
    u32x4 qf[4][2];
#pragma unroll
    for (int t = 0; t < 4; ++t) { qf[t][0] = lds128(lb + L_Q + t * 2048 + lane * 16); qf[t][1] = lds128(lb + L_Q + t * 2048 + 1024 + lane * 16); }
    float qn2[4];
#pragma unroll
    for (int t = 0; t < 4; ++t) qn2[t] = lds32f(lb + L_QN + (t * 32 + l31) * 4);
    WAIT_LGKM();
    if (lane == 0) __hip_atomic_fetch_add((LAS u32*)(lb + L_SYNC + 40), 1u, __ATOMIC_RELAXED, __HIP_MEMORY_SCOPE_WORKGROUP);
    if (wave == 3) {
      __builtin_amdgcn_s_sleep(KV_SLEEP);
      const float km = wave_max(__uint_as_float(wait_gran(ws + OFF_KVG + (ah * 64 + lane) * 128, tag)));
      lds_st(lb + L_SYNC + 60, __float_as_uint(km));
      WAIT_LGKM();
      lds_st(lb + L_SYNC + 56, 1u);
    } else lds_wait(lb + L_SYNC + 56, 1);
    const float kmax2 = __uint_as_float(lds_ld(lb + L_SYNC + 60));
    STAMPL(12, "kv flags seen");
    const u32 kvb = OFF_KV + (u32)(ah * 64 + 8 * wave) * 4096;
    u32x4 kf[2][2], vf[2][2];
    const u32 kvl = lane * 16;
#define LDKV(D, kt, off) asm volatile("buffer_load_dwordx4 %0, %2, %3, %4 offen offset:" #off " sc1\n\tbuffer_load_dwordx4 %1, %2, %3, %4 offen offset:" #off "+1024 sc1" \
      : "=&v"(D[(kt) & 1][0]), "=&v"(D[(kt) & 1][1]) : "v"(kvl), "s"(wsr), "s"(kvb + (kt) * 4096) : "memory")
#define WAITN(n) asm volatile("s_waitcnt vmcnt(%0)" :: "i"(n) : "memory")
#define PINKV(D, kt) asm volatile("" : "+v"(D[(kt) & 1][0]), "+v"(D[(kt) & 1][1]))
#define WAITKV(D, kt, n) do { WAITN(n); SB(); PINKV(D, kt); } while (0)
#define WAITKV2(D, kt, n0, n1) do { if (wave < 4) WAITN(n0); else WAITN(n1); SB(); PINKV(D, kt); } while (0)
    asm volatile("s_nop 4");
    LDKV(kf, 0, 0); LDKV(vf, 0, 2048); LDKV(kf, 1, 0);
    float sh[4]; bool safe = true;
#pragma unroll
    for (int t = 0; t < 4; ++t) { sh[t] = 1.02f * __builtin_amdgcn_sqrtf(qn2[t] * kmax2); safe = safe && (sh[t] <= 48.f); }
    if (!__all(safe)) {
      BAR();
      float mx[4] = {-INFINITY, -INFINITY, -INFINITY, -INFINITY};
#pragma unroll 1
      for (int kt = 0; kt < 8; ++kt) {
        const u32x4 ka = ld128(wsr, lane * 16, kvb + kt * 4096), kb = ld128(wsr, lane * 16 + 1024, kvb + kt * 4096);
#pragma unroll
        for (int t = 0; t < 4; ++t) {
          f32x16 s = mfma32(ka, qf[t][0], (f32x16){}); s = mfma32(kb, qf[t][1], s);
#pragma unroll
          for (int r = 0; r < 16; ++r) mx[t] = fmaxf(mx[t], s[r]);
        }
      }
#pragma unroll
      for (int t = 0; t < 4; ++t) { const float m = xhalf_max(mx[t]); if (!hi) *(LAS float*)(lb + L_MX + (wave * 128 + t * 32 + l31) * 4) = m; }
      BAR();
#pragma unroll
      for (int t = 0; t < 4; ++t) {
        float m = -INFINITY;
#pragma unroll
        for (int w = 0; w < 8; ++w) m = fmaxf(m, lds32f(lb + L_MX + (w * 128 + t * 32 + l31) * 4));
        sh[t] = m;
      }
    }
    if (hi) {
#pragma unroll
      for (int t = 0; t < 4; ++t) {
        const float a = rbf(-sh[t]), r1 = -sh[t] - a, c = rbf(r1);
        qf[t][1][2] = pk2(a, c); qf[t][1][3] = pk2(r1 - c, 0.f);
      }
    }
    f32x16 o[4] = {(f32x16){}, (f32x16){}, (f32x16){}, (f32x16){}};
    WAITKV(kf, 0, 4);
    f32x16 sc = mfma32(kf[0][0], qf[0][0], (f32x16){}); sc = mfma32(kf[0][1], qf[0][1], sc);
    u32x4 p0, p1;
#define EXP2(v) __builtin_amdgcn_exp2f(v)
    float ec, ed;
#define GRP(r, PREV, MF, CUR) do { float ea = EXP2(sc[r]), eb = EXP2(sc[(r) + 1]); asm volatile("" : "+v"(ea), "+v"(eb)); PREV; SB(); MF; SB(); \
      ec = EXP2(sc[(r) + 2]); ed = EXP2(sc[(r) + 3]); asm volatile("" : "+v"(ec), "+v"(ed)); { u32 pa = pk2(ea, eb); asm volatile("" : "+v"(pa)); CUR = pa; } SB(); } while (0)
#define PKP(D) do { u32 pb = pk2(ec, ed); asm volatile("" : "+v"(pb)); D = pb; } while (0)
    u32 pwb = lb + wave * 8192 + lane * 16, lwb = lb + L_LP + wave * 512 + l31 * 4; asm volatile("" : "+v"(pwb), "+v"(lwb));
#define WRP(t) do { _Pragma("unroll") for (int g = 0; g < 2; ++g) *(LAS u32x4*)(pwb + ((t) * 2 + g) * 1024) = (u32x4){pk2(o[t][8 * g], o[t][8 * g + 1]), pk2(o[t][8 * g + 2], o[t][8 * g + 3]), pk2(o[t][8 * g + 4], o[t][8 * g + 5]), pk2(o[t][8 * g + 6], o[t][8 * g + 7])}; \
      if (hi) *(LAS float*)(lwb + (t) * 128) = o[t][12]; } while (0)
    const int w6 = min(wave, 6);
    const u32 wo_base = OFF_WOT + (u32)(7 * ocq + w6) * 16384, wg_base = OFF_WGU + (u32)(7 * gfj + w6) * 28672;
    auto nslot = [](int kt, int t, bool) constexpr { const int sl = 4 * (kt - 2) + t;
      return (t == 0 && kt < 7 ? 2 : 0) + (t == 2 && kt < 6 ? 2 : 0) + (kt >= 2 ? (sl < 16) + (sl >= 6 && sl < 18) : 0); };
    auto nafter = [&](int k0, int t0, int k1, int t1, bool young) constexpr { int n = -2; for (int i = 4 * k0 + t0; i <= 4 * k1 + t1; ++i) n += nslot(i >> 2, i & 3, young); return n; };
#pragma unroll
    for (int kt = 0; kt < 8; ++kt) {
      if (kt == 0) __builtin_amdgcn_s_setprio(3);
      if (kt == 2) __builtin_amdgcn_s_setprio(2);
      if (kt == 4) __builtin_amdgcn_s_setprio(1);
      if (kt == 6 && wave < 4) __builtin_amdgcn_s_setprio(0);
      if (kt == 7) __builtin_amdgcn_s_setprio(0);
#pragma unroll
      for (int t = 0; t < 4; ++t) {
        f32x16 sn;
        const bool more = t < 3 || kt < 7, first = t == 0 && kt == 0;
        const int kn = t < 3 ? (kt & 1) : ((kt + 1) & 1), tn = (t + 1) & 3;
        if (t == 3 && kt < 7) {
          if (kt == 0) WAITKV(kf, 1, nafter(0, 0, 0, 2, false) + 2);
          else if (kt == 1) WAITKV(kf, 2, nafter(0, 2, 1, 2, false));
          else WAITKV(kf, kt + 1, nafter(kt - 1, 2, kt, 2, false));
        }
        GRP(0, if (!first) PKP(p1[3]), if (more) sn = mfma32(kf[kn][0], qf[tn][0], (f32x16){}), p0[0]);
        GRP(4, PKP(p0[1]), if (more) sn = mfma32(kf[kn][1], qf[tn][1], sn), p0[2]);
        GRP(8, PKP(p0[3]), if (t > 0) o[t - 1] = mfma32(vf[kt & 1][1], p1, o[t - 1]); else if (kt > 0) o[3] = mfma32(vf[(kt + 1) & 1][1], p1, o[3]), p1[0]);
        if (t == 0 && kt < 7) LDKV(vf, kt + 1, 2048);
        if (t == 2 && kt < 6) LDKV(kf, kt + 2, 0);
        if (kt == 1 && t == 0 && wave == 3) {
#pragma nounroll
          for (int i = 0; i < SPIN_CAP; ++i) {
            const u32 a = poll32(ws + OFF_PREPF + lane * 128), c = poll32(ws + OFF_PREPF + (64 + lane) * 128), d = poll32(ws + OFF_PREPF + (128 + lane) * 128), e = poll32(ws + OFF_PREPF + (192 + lane) * 128);
            if (__all(a == tag && c == tag && d == tag && e == tag)) break;
            __builtin_amdgcn_s_sleep(8);
          }
          CFENCE();
          if (lane == 0) __hip_atomic_fetch_add((LAS u32*)(lb + L_SYNC + 40), 1u, __ATOMIC_RELAXED, __HIP_MEMORY_SCOPE_WORKGROUP);
        }
        if (t == 0) {
        }
        if (kt >= 2) {
          const int sl = 4 * (kt - 2) + t;
          if (sl == 0) lds_wait(lb + L_SYNC + 40, 9);
          if (sl < 10) wo_f[sl] = ld128(wsr, lane * 16, wo_base + sl * 1024);
          else if (sl < 16) wgu[sl - 10] = ld128(wsr, lane * 16, wg_base + (sl - 10) * 1024);
          if (sl >= 6 && sl < 18) dma16<true>(ws + (sl < 12 ? wo_base + (4 + sl) * 1024 : wg_base + (sl - 6) * 1024), lane * 16, lb + L_PARK + w6 * 12288 + (sl - 6) * 1024);
        }
        if (t == 0) {
          if (kt == 0) WAITKV(vf, 0, nafter(0, 0, 0, 0, false) + 4);
          else if (kt == 1) WAITKV(vf, 1, nafter(0, 0, 1, 0, false));
          else WAITKV(vf, kt, nafter(kt - 1, 0, kt, 0, false));
        }
        SB();
        GRP(12, PKP(p1[1]), o[t] = mfma32(vf[kt & 1][0], p0, o[t]), p1[2]);
        if (kt == 7 && t > 0) { WRP(t - 1); SB(); }
        if (more) sc = sn;
      }
    }
    PKP(p1[3]);
    o[3] = mfma32(vf[1][1], p1, o[3]);
    WRP(3);
    STAMPL(13, "attn done");
    BAR();
    const int qt = wave >> 1, sf = wave & 1;
    u32x4 pa[8]; float pl[8];
    {
      u32 base = lb + qt * 2048 + sf * 1024 + lane * 16, lbase = lb + L_LP + qt * 128 + l31 * 4;
      asm volatile("" : "+v"(base), "+v"(lbase));
#pragma unroll
      for (int w = 0; w < 8; ++w) { pa[w] = lds128(base + w * 8192); pl[w] = lds32f(lbase + w * 512); }
      SB();
    }
    float a8[8]; u32 sel0 = 0x00003F80u, sel1 = 0x3F800000u; asm volatile("" : "+v"(sel0), "+v"(sel1));
#pragma unroll
    for (int j = 0; j < 4; ++j) {
      a8[2 * j] = bflo(pa[0][j]); a8[2 * j + 1] = bfhi(pa[0][j]);
#pragma unroll
      for (int w = 1; w < 8; ++w) { a8[2 * j] = dot2(pa[w][j], sel0, a8[2 * j]); a8[2 * j + 1] = dot2(pa[w][j], sel1, a8[2 * j + 1]); }
    }
    const float l = ((pl[0] + pl[4]) + (pl[1] + pl[5])) + ((pl[2] + pl[6]) + (pl[3] + pl[7]));
    const float rl = __builtin_amdgcn_rcpf(l);
    const u32x4 of = {pk2(a8[0] * rl, a8[1] * rl), pk2(a8[2] * rl, a8[3] * rl), pk2(a8[4] * rl, a8[5] * rl), pk2(a8[6] * rl, a8[7] * rl)};
    st128(wsr, (l31 >> 4) * 1024 + ((l31 & 15) + 16 * (2 * sf + hi)) * 16, OFF_OB + (u32)((4 * ai + qt) * 16 + ah) * 2048, nopoison(of));
    WAIT_VM(1);
    BAR();
    STAMPL(14, "O flagged");
  }

  u32 uvp[14];
  const int otok = 16 * ((lane >> 4) & 1) + (lane & 15), ofe = 112 * ocq + 16 * min(wave, 6) + 8 * hi;
  if (wave < 7) {
#pragma unroll
    for (int s = 12; s < NBA; ++s) wgu[s] = ld128(wsr, lane * 16 + (s & 3) * 1024, OFF_WGU + (u32)((7 * gfj + wave) * 28 + (s & ~3)) * 1024);
#pragma unroll
    for (int j = 0; j < 6; ++j) wo_f[10 + j] = lds128(lb + L_PARK + wave * 12288 + j * 1024 + lane * 16);
#pragma unroll
    for (int j = 6; j < 12; ++j) wgu[j] = lds128(lb + L_PARK + wave * 12288 + j * 1024 + lane * 16);
  } else __builtin_amdgcn_s_sleep(O_SLEEP);
  STAMPL(15, "O first try");
  {
    u32x4 o4[4];
#pragma nounroll
    for (int i = 0; i < SPIN_CAP; ++i) {
      CFENCE();
#pragma unroll
      for (int k = 0; k < 4; ++k) o4[k] = ld128(wsr, lane * 16 + k * 1024, OFF_OB + (u32)(ort * 16 + 2 * wave) * 2048);
      if (!__any(max(max(umax4(o4[0]), umax4(o4[1])), max(umax4(o4[2]), umax4(o4[3]))) == POISON)) break;
      __builtin_amdgcn_s_sleep(2);
    }
#pragma unroll
    for (int k = 0; k < 4; ++k) *(LAS u32x4*)(lb + (4 * wave + k) * 1024 + lane * 16) = o4[k];
    xres = *(const u32x4*)(x + (size_t)(32 * ort + otok) * 448 + ofe);
    gaA = ld128(wsr, ofe * 4, OFF_MODG + 448 * 8); gaB = ld128(wsr, ofe * 4 + 16, OFF_MODG + 448 * 8);
  }
  BAR();
  STAMPL(16, "O landed");
  if (wave < 7) {
    f32x4 y0 = {0.f, 0.f, 0.f, 0.f}, y1 = y0;
    u32x4 oa[10];
#pragma unroll
    for (int i = 0; i < 8; ++i) oa[i] = lds128(lb + i * 1024 + lane * 16);
#pragma unroll
    for (int h = 0; h < 16; ++h) {
      if (h < 12) { oa[(2 * h + 8) % 10] = lds128(lb + (2 * h + 8) * 1024 + lane * 16); oa[(2 * h + 9) % 10] = lds128(lb + (2 * h + 9) * 1024 + lane * 16); }
      SB(); y0 = mfma16(wo_f[h], oa[(2 * h) % 10], y0); y1 = mfma16(wo_f[h], oa[(2 * h + 1) % 10], y1); SB();
    }
    float yv[8];
#pragma unroll
    for (int r = 0; r < 4; ++r) { auto sw = __builtin_amdgcn_permlane16_swap(__float_as_uint(y0[r]), __float_as_uint(y1[r]), false, false); yv[r] = __uint_as_float(sw[0]); yv[4 + r] = __uint_as_float(sw[1]); }
    const u32 gw[4] = {gaA[0], gaA[2], gaB[0], gaB[2]};
    u32x4 x2p; float s1 = 0.f, s2 = 0.f;
#pragma unroll
    for (int j = 0; j < 4; ++j) {
      x2p[j] = pk2(fmaf(bflo(gw[j]), yv[2 * j], bflo(xres[j])), fmaf(bfhi(gw[j]), yv[2 * j + 1], bfhi(xres[j])));
      s1 = dot2(x2p[j], 0x3F803F80u, s1); s2 = dot2(x2p[j], x2p[j], s2);
    }
    const u32 fo = (u32)(ort * 28 + 7 * ocq + wave) * 1024;
    x2p = nopoison(x2p);
    st128(wsr, (otok + 32 * hi) * 16, OFF_X2 + fo, x2p);
    s1 = xhalf_sum(s1); s2 = xhalf_sum(s2);
    *(LAS float*)(lb + L_ST + wave * 256 + (hi * 32 + otok) * 4) = hi ? s2 : s1;
    WAIT_VM(0);
  }
  BAR();
  if (wave == 7) {
    float t = 0.f;
#pragma unroll
    for (int w = 0; w < 7; ++w) t += lds32f(lb + L_ST + w * 256 + lane * 4);
    const u32 nv = __builtin_amdgcn_update_dpp(0u, __float_as_uint(t), 0xB1, 0xf, 0xf, false);
    st128(wsr, !(lane & 1) ? (u32)lane * 8 : 0x80000000u, OFF_X2G + (u32)(ort * 4 + ocq) * 512, (u32x4){__float_as_uint(t), tag, nv, tag});
  }
  if (wave < 7) {
#pragma unroll
    for (int s = NBA; s < 28; ++s) wgu[s] = ld128(wsr, lane * 16 + (s & 3) * 1024, OFF_WGU + (u32)((7 * gfj + wave) * 28 + (s & ~3)) * 1024);
  } else __builtin_amdgcn_s_sleep(X_SLEEP);
  STAMPL(17, "x2 flagged");

  const int dsl = slot & 15, dblk = 2 * xg + (slot >> 4), drt = 4 * dblk + dsl / 7, dc = dsl % 7;
  const bool dact = dsl < 14;
  u32x4 wdf[28], x2r, gbA, gbB;
  const u32 xa0 = OFF_X2 + (u32)(4 * gi) * 28672;
  u32x4 a1[3];
  {
    u32x4 a0[4];
#pragma nounroll
    for (int i = 0; i < SPIN_CAP; ++i) {
      CFENCE();
#pragma unroll
      for (int k = 0; k < 4; ++k) a0[k] = ld128(wsr, lane * 16 + k * 1024, xa0 + wave * 4096);
      if (!__any(max(max(umax4(a0[0]), umax4(a0[1])), max(umax4(a0[2]), umax4(a0[3]))) == POISON)) break;
      __builtin_amdgcn_s_sleep(2);
    }
    CFENCE();
#pragma unroll
    for (int k = 0; k < 4; ++k) *(LAS u32x4*)(lb + (4 * wave + k) * 1024 + lane * 16) = a0[k];
#pragma unroll
    for (int k = 0; k < 3; ++k) a1[k] = ld128(wsr, lane * 16 + k * 1024, xa0 + 32768 + wave * 3072);
  }
  auto tile1 = [&]() __attribute__((always_inline)) {
#pragma nounroll
    for (int i = 0; i < SPIN_CAP && __any(max(max(umax4(a1[0]), umax4(a1[1])), umax4(a1[2])) == POISON); ++i) {
      CFENCE();
#pragma unroll
      for (int k = 0; k < 3; ++k) a1[k] = ld128(wsr, lane * 16 + k * 1024, xa0 + 32768 + wave * 3072);
    }
#pragma unroll
    for (int k = 0; k < 3; ++k) *(LAS u32x4*)(lb + 32768 + (3 * wave + k) * 1024 + lane * 16) = a1[k];
  };
#define ADMA(n) do { const int g_ = 14 + min(wave + 8 * (n), 13); dma16x4(ws + xa0 + g_ * 4096, lane * 16, lb + g_ * 4096); } while (0)
  BAR();
  STAMPL(19, "A0 landed");
  const int dm = wave >> 2, dn = (wave >> 1) & 1, dgp = wave & 1;
  const u32 wdo = OFF_WDT + (u32)(2 * dc * 112 + 14 * wave) * 1024;
  const u32 pk_free = lb + 114688 + wave * 4096, pk_a = lb + wave * 3072;
#define PARKA(i) ((i) < 4 ? pk_free + (i) * 1024 : pk_a + ((i) < 7 ? (i) - 4 : 28 + (i) - 7) * 1024)
#define PARKD(i) dma16<true>(ws + wdo + (((i) & 1) * 112 + 9 + ((i) >> 1)) * 1024, lane * 16, PARKA(i))
  if (wave < 7) {
    u32 sb = lb + L_SP2 + l31 * 4, ub = lb + L_UV + wave * 256 + hi * 16; asm volatile("" : "+v"(sb), "+v"(ub));
    u32 ab = lb + lane * 16, ab2 = ab + 57344; asm volatile("" : "+v"(ab), "+v"(ab2));
    constexpr int RD = GU_RING;
    static_assert(RD == 6, "the counted waits below assume it");
    f32x16 acc[2]; u32x4 af[RD + 1]; f32x4 uv[4]; float st[8], rs = 0.f, nm = 0.f, hv[8];
#define EP_RD(T) do { _Pragma("unroll") for (int c = 0; c < 4; ++c) { st[c] = lds32f(sb + ((T) * 4 + c) * 256); st[4 + c] = lds32f(sb + ((T) * 4 + c) * 256 + 128); } } while (0)
#define EP_UV(g) do { uv[0] = lds128f(ub + 32 * (g)); uv[1] = lds128f(ub + 32 * (g) + 64); uv[2] = lds128f(ub + 32 * (g) + 128); uv[3] = lds128f(ub + 32 * (g) + 192); } while (0)
#define EP_ST() do { float m = ((st[0] + st[1]) + (st[2] + st[3])) * (1.f / 448.f); asm volatile("" : "+v"(m)); const float e2 = (((st[4] + st[5]) + st[6]) + st[7]) * (1.f / 448.f); \
      rs = __builtin_amdgcn_rsqf(fmaxf(e2 - m * m, 0.f) + 1e-5f); nm = -m * rs; } while (0)
    constexpr float K1 = -2.f * 0.7978845608028654f * 1.4426950408889634f, K3 = K1 * 0.044715f;
    float ea[8], eb[8], k1v = K1; asm volatile("" : "+v"(k1v));
#define PIN1(v) asm volatile("" : "+v"(v))
#define EP_STAGE(A, e, j) do { \
      if ((j) == 0) { ea[e] = fma1(nm, uv[0][(e) & 3], uv[2][(e) & 3]); eb[e] = fma1(nm, uv[1][(e) & 3], uv[3][(e) & 3]); PIN1(ea[e]); PIN1(eb[e]); } \
      if ((j) == 1) { ea[e] = fma1(A[e], rs, ea[e]); eb[e] = fma1(A[8 + (e)], rs, eb[e]); PIN1(ea[e]); PIN1(eb[e]); } \
      if ((j) == 2) { hv[e] = ea[e] * ea[e]; PIN1(hv[e]); eb[e] *= ea[e]; PIN1(eb[e]); } \
      if ((j) == 3) { hv[e] = fmaf(hv[e], K3, k1v); PIN1(hv[e]); } \
      if ((j) == 4) { hv[e] *= ea[e]; PIN1(hv[e]); } \
      if ((j) == 5) { hv[e] = __builtin_amdgcn_exp2f(hv[e]); PIN1(hv[e]); } \
      if ((j) == 6) { hv[e] += 1.f; PIN1(hv[e]); } \
      if ((j) == 7) { hv[e] = __builtin_amdgcn_rcpf(hv[e]); PIN1(hv[e]); } \
      if ((j) == 8) { hv[e] *= eb[e]; PIN1(hv[e]); } } while (0)
#define EP_OUT(T) st128(wsr, lane * 16, OFF_HB + (u32)((4 * gi + (T)) * 112 + 7 * gfj + wave) * 1024, (u32x4){pk2(hv[0], hv[1]), pk2(hv[2], hv[3]), pk2(hv[4], hv[5]), pk2(hv[6], hv[7])})
#pragma unroll
    for (int i = 0; i < RD; ++i) af[i] = lds128(ab + i * 1024);
    static_for<0, 112>([&](auto NN) __attribute__((always_inline)) {
      {
        constexpr int n = NN.value, t = n / 28, s = n % 28;
        if (n == 8) tile1();
        if constexpr (n >= 11 && n < 18) { uvp[2 * (n - 11)] = ld32(wsr, lane * 4, OFF_UV + (u32)((2 * (n - 11)) * 112 + 7 * gfj + wave) * 256); uvp[2 * (n - 11) + 1] = ld32(wsr, lane * 4, OFF_UV + (u32)((2 * (n - 11) + 1) * 112 + 7 * gfj + wave) * 256); }
        if (n == 25) {
          float uvs = 0.f;
#pragma unroll
          for (int k = 0; k < 14; ++k) uvs += __uint_as_float(uvp[k]);
          *(LAS float*)(lb + L_UV + wave * 256 + lane * 4) = uvs;
        }
        if (n == 32 - RD) { STAMPL(28, "gu n25"); BAR(); STAMPL(18, "B7c"); ADMA(0); }
        if (n == 31) ADMA(1);
        if (n == 56 - RD) { WAIT_VM(4); asm volatile("s_barrier" ::: "memory"); }
        if constexpr (n + RD < 112) af[(n + RD) % (RD + 1)] = (n + RD < 56) ? lds128(ab + (n + RD) * 1024) : lds128(ab2 + (n + RD - 56) * 1024);
        if (s == 0) __builtin_amdgcn_s_setprio(3 - t);
        SB();
        if (s == 0) acc[t & 1] = mfma32(wgu[s], af[n % (RD + 1)], (f32x16){}); else acc[t & 1] = mfma32(wgu[s], af[n % (RD + 1)], acc[t & 1]);
        SB();
        if constexpr (t > 0) {
          if (s == 0) { EP_RD(t - 1); EP_UV(0); }
          if (s == 2) EP_ST();
          static_for<0, 8>([&](auto EE) __attribute__((always_inline)) { constexpr int e = EE.value, j = s - 3 - 2 * e; if constexpr (j >= 0 && j < 9) EP_STAGE(acc[(t - 1) & 1], e, j); });
          if (s == 9) { SB(); EP_UV(1); }
          if (s == 26) EP_OUT(t - 1);
          if (s == 2 && t == 2) asm volatile("s_barrier" ::: "memory");
          if constexpr (t == 1 && s % 6 == 1 && s >= 7) PARKD(s / 6 - 1);
          if constexpr (t == 2 && s % 4 == 0 && s >= 4 && s < 28) PARKD(3 + s / 4);
          if (s == 12 && t == 3) {
            WAIT_VM(6);
            if (lane == 0 && __hip_atomic_fetch_add((LAS u32*)(lb + L_SYNC + 68), 1u, __ATOMIC_RELAXED, __HIP_MEMORY_SCOPE_WORKGROUP) == 6u) flag32(ws + OFF_HF + ((gi * 16 + gfj) * 8 + 7) * 128, tag);
          }
          if (s == 27 && t == 1) STAMPL(20, "gu tile1");
          if (s == 27 && t == 2) STAMPL(21, "gu tile2");
          if (s == 27 && t == 3) STAMPL(22, "gu tile3");
          if constexpr (t < 3 ? (s % 12 == 1) : (s % 2 == 1 && s < 24)) { constexpr int f = t < 3 ? 3 * (t - 1) + s / 12 : 6 + s / 2; wdf[f] = ld128(wsr, lane * 16, wdo + (u32)((f & 1) * 112 + (f >> 1)) * 1024); }
          SB();
        }
      }
    });
    asm volatile("s_nop 11" : "+v"(acc[1]));
    EP_RD(3); EP_UV(0); EP_ST();
    {
      f32x4 uw[4];
      uw[0] = lds128f(ub + 32); uw[1] = lds128f(ub + 96); uw[2] = lds128f(ub + 160); uw[3] = lds128f(ub + 224);
      float g8[8], u8[8];
#pragma unroll
      for (int e = 0; e < 8; ++e) {
        const f32x4* W = e < 4 ? uv : uw;
        g8[e] = fma1(acc[1][e], rs, fma1(nm, W[0][e & 3], W[2][e & 3])); u8[e] = fma1(acc[1][8 + e], rs, fma1(nm, W[1][e & 3], W[3][e & 3]));
      }
#pragma unroll
      for (int e = 0; e < 8; ++e) hv[e] = (g8[e] * u8[e]) * __builtin_amdgcn_rcpf(1.f + __builtin_amdgcn_exp2f(g8[e] * fmaf(g8[e] * g8[e], K3, k1v)));
    }
    EP_OUT(3);
    WAIT_VM(0);
    if (lane == 0) flag32(ws + OFF_HF + ((gi * 16 + gfj) * 8 + wave) * 128, tag);
  } else {
    __builtin_amdgcn_s_setprio(3);
    tile1();
    {
    u64 g[16]; const char* gp = ws + OFF_X2G + (16 * gi * 64 + lane) * 8;
#pragma nounroll
    for (int i = 0; i < SPIN_CAP; ++i) {
      bool ok = true;
#pragma unroll
      for (int p = 0; p < 16; ++p) g[p] = poll64(gp + p * 512);
#pragma unroll
      for (int p = 0; p < 16; ++p) ok = ok && (u32)(g[p] >> 32) == tag;
      if (__all(ok)) break;
      __builtin_amdgcn_s_sleep(1);
    }
    CFENCE();
#pragma unroll
    for (int p = 0; p < 16; ++p) *(LAS u32*)(lb + L_SP2 + p * 256 + lane * 4) = (u32)g[p];
    }
    BAR();
    ADMA(0); ADMA(1);
#pragma unroll
    for (int f = 0; f < 18; ++f) wdf[f] = ld128(wsr, lane * 16, wdo + (u32)((f & 1) * 112 + (f >> 1)) * 1024);
#pragma unroll
    for (int i = 0; i < 4; ++i) PARKD(i);
    WAIT_VM(22);
    asm volatile("s_barrier" ::: "memory");
    asm volatile("s_barrier" ::: "memory");
#pragma unroll
    for (int i = 4; i < 10; ++i) PARKD(i);
    if (dact) {

      lds_wait(lb + L_SYNC + 68, 7);

      wait_flags(ws + OFF_HF + ((dblk * 16 + (lane & 15)) * 8 + 7) * 128, tag);
    }
    __builtin_amdgcn_s_waitcnt(0x0F70);
    CFENCE();
    __builtin_amdgcn_s_setprio(0);
  }
  STAMPL(23, "h flagged");
  if (dact) {
    BAR();
    constexpr int HR = 9;
    u32x4 hf[HR];
    const int dcol = 64 * dc + 32 * dn + 16 * dgp + 8 * hi;
#define LDH(i) hf[(i) % HR] = ld128(wsr, lane * 16 + (((i) % 14) & 3) * 1024, OFF_HB + (u32)((drt + 2 * ((i) / 14)) * 112 + 14 * wave + (((i) % 14) & ~3)) * 1024)
#pragma unroll
    for (int i = 0; i < 4; ++i) { LDH(i); SB(); }
#pragma unroll
    for (int f = 18; f < 28; ++f) wdf[f] = lds128(PARKA(f - 18) + lane * 16);
    BAR();
#pragma unroll
    for (int i = 4; i < HR - 1; ++i) { LDH(i); SB(); }
    STAMPL(24, "down go");
    f32x16 c0, c1; u32 late0;
    u32 pwb = lb + wave * 16384 + lane * 16; asm volatile("" : "+v"(pwb));
    static_for<0, 28>([&](auto II) __attribute__((always_inline)) {
      constexpr int i = II.value, m = i / 14, j = i % 14;
      if constexpr (i == LATE_PRE) late0 = ld32(wsr, ((lane >> 3) & 1) * 1024 + min(lane & 7, 6) * 128, OFF_HF + (u32)(dblk * 16 + 2 * wave) * 1024);
      if constexpr (i + HR - 1 == 14) {
        if (!__all(late0 == tag)) wait_flags(ws + OFF_HF + ((dblk * 16 + 2 * wave + ((lane >> 3) & 1)) * 8 + min(lane & 7, 6)) * 128, tag);
        CFENCE();
        STAMPL(27, "late seen");
      }
      if constexpr (i + HR - 1 < 28) LDH(i + HR - 1);
      if constexpr (i == 20) {
        gbA = ld128(wsr, dcol * 4, OFF_MODG + 1120 * 8); gbB = ld128(wsr, dcol * 4 + 16, OFF_MODG + 1120 * 8);
        x2r = ld128(wsr, lane * 16, OFF_X2 + (u32)((drt + 2 * dm) * 28 + 4 * dc + 2 * dn + dgp) * 1024);
      }
      SB();
      if constexpr (j == 0) { c0 = mfma32(wdf[0], hf[i % HR], (f32x16){}); c1 = mfma32(wdf[1], hf[i % HR], (f32x16){}); }
      else { c0 = mfma32(wdf[2 * j], hf[i % HR], c0); c1 = mfma32(wdf[2 * j + 1], hf[i % HR], c1); }
      SB();
      if constexpr (j == 13) {
#pragma unroll
        for (int g = 0; g < 4; ++g) {
          *(LAS f32x4*)(pwb + ((2 * m) * 4 + g) * 1024) = (f32x4){c0[4 * g], c0[4 * g + 1], c0[4 * g + 2], c0[4 * g + 3]};
          *(LAS f32x4*)(pwb + ((2 * m + 1) * 4 + g) * 1024) = (f32x4){c1[4 * g], c1[4 * g + 1], c1[4 * g + 2], c1[4 * g + 3]};
        }
      }
    });
    STAMPL(25, "down mfma done");
    BAR();
    f32x4 pa[8], pb[8];
    {
      u32 base = lb + (2 * dm + dn) * 4096 + dgp * 2048 + lane * 16, base2 = base + 65536;
      asm volatile("" : "+v"(base), "+v"(base2));
#pragma unroll
      for (int w = 0; w < 4; ++w) {
        pa[w] = lds128f(base + w * 16384); pb[w] = lds128f(base + w * 16384 + 1024);
        pa[4 + w] = lds128f(base2 + w * 16384); pb[4 + w] = lds128f(base2 + w * 16384 + 1024);
      }
      SB();
    }
    f32x4 a0 = ((pa[0] + pa[4]) + (pa[1] + pa[5])) + ((pa[2] + pa[6]) + (pa[3] + pa[7])), a1 = ((pb[0] + pb[4]) + (pb[1] + pb[5])) + ((pb[2] + pb[6]) + (pb[3] + pb[7]));
    asm volatile("" : "+v"(a0), "+v"(a1));
    float yv[8];
#pragma unroll
    for (int r = 0; r < 4; ++r) { auto sw = __builtin_amdgcn_permlane32_swap(__float_as_uint(a0[r]), __float_as_uint(a1[r]), false, false); yv[r] = __uint_as_float(sw[0]); yv[4 + r] = __uint_as_float(sw[1]); }
    const u32 gw[4] = {gbA[0], gbA[2], gbB[0], gbB[2]};
    u32x4 ov;
#pragma unroll
    for (int j = 0; j < 4; ++j) ov[j] = pk2(fmaf(bflo(gw[j]), yv[2 * j], bflo(x2r[j])), fmaf(bfhi(gw[j]), yv[2 * j + 1], bfhi(x2r[j])));
    *(u32x4*)(out + (size_t)(32 * (drt + 2 * dm) + l31) * 448 + dcol) = ov;
    if (b == 0 && tid == 0) *(GAS u32*)ws = tag;
  }
  STAMPL(26, "end");
}

extern "C" void kernel_launch(void* const* d_in, const int* in_sizes, int n_in, void* d_out, int out_size, void* d_ws, size_t ws_size, hipStream_t stream) {
  fused_block<<<256, 512, 0, stream>>>((const u16*)d_in[0], (const u16*)d_in[1], (const u16*)d_in[2], (const u16*)d_in[4], (const u16*)d_in[5],
                                       (const u16*)d_in[6], (const u16*)d_in[7], (const u16*)d_in[8], (u16*)d_out, (char*)d_ws);
}
```

```cpp
#include <hip/hip_runtime.h>
#include <stdint.h>

typedef unsigned short u16;
typedef unsigned u32;
typedef unsigned long long u64;
typedef __attribute__((ext_vector_type(2))) unsigned u32x2;
typedef __attribute__((ext_vector_type(4))) unsigned u32x4;
typedef __attribute__((ext_vector_type(2))) float f32x2;
typedef __attribute__((ext_vector_type(4))) float f32x4;
typedef __attribute__((ext_vector_type(16))) float f32x16;
typedef __attribute__((ext_vector_type(2))) __bf16 bf16x2;
typedef __attribute__((ext_vector_type(8))) __bf16 bf16x8;
typedef __attribute__((ext_vector_type(4))) short s16x4;
#define DEV __device__ __forceinline__
#define GAS __attribute__((address_space(1)))
#define LAS __attribute__((address_space(3)))
#define RLX_AGENT __ATOMIC_RELAXED, __HIP_MEMORY_SCOPE_AGENT

constexpr u32 OFF_MODG0 = 4096, MODG_REP = 11264;
constexpr u32 OFF_X2G = 131072, OFF_UV = 13467648  , OFF_WQT = 524288, OFF_WOT = 1900544, OFF_WGU = 2359296, OFF_WDT = 5570560;
constexpr u32 OFF_KV = 7176192, OFF_OB = 11370496, OFF_X2 = 15302656, OFF_HB = 17137664, OFF_XH = 24477696;
constexpr u32 OFF_XF = 26312704, OFF_WQF = OFF_XF + 512 * 128, OFF_KVG = OFF_WQF + 1024 * 128, OFF_OF = OFF_KVG + 1024 * 128, OFF_HF = OFF_OF + 2048 * 128, OFF_PREPF = OFF_HF + 2048 * 128, WS_USED = OFF_PREPF + 256 * 128;
constexpr u32 OFF_STAMP = 29360128;
constexpr u32 L_STG = 90112  , L_Q = 131072, L_GAM = 139264, L_QN = 142848;
constexpr u32 L_PARK = 64512 + 1024  ;
constexpr u32 L_SP2 = 151552  , L_MODP = L_SP2, L_MX = L_SP2, L_LP = L_SP2, L_UV = 155648  , L_ST = 157696  , L_G2 = 159488  ;
constexpr u32 L_SYNC = 160000;
constexpr u32 LDS_BYTES = 163840;
constexpr int CODE_BYTES = 44500;
constexpr int SPIN_CAP = 1 << 15;
#define FIRST_POLL 265
#define KV_SLEEP 1
#define MOD_POLL 250
#define NREP 8
#define GATE 0
#define XP0 8
#define PRETOUCH 0
#define NOCODEPF 0
#define O_SLEEP 20
#define X_SLEEP 8
#define KP_SLEEP 16
#define KV_SLEEP2 12
#define FORCE_REDO 0
#define PST 14
#define HPS 20
#define NBA 23
#define TOUCH_AT 0
#define LATE_PRE 5
#define GU_RING 6
#define PREP_START 130
#define POLL_T 0
#define POLL_SLEEP 3

DEV float bflo(u32 w) { return __uint_as_float(w << 16); }
DEV float bfhi(u32 w) { return __uint_as_float(w & 0xffff0000u); }
DEV u32 pk2(float a, float b) { f32x2 v = {a, b}; return __builtin_bit_cast(u32, __builtin_convertvector(v, bf16x2)); }
DEV float rbf(float a) { return bflo(pk2(a, 0.f)); }
DEV float fma1(float a, float b, float c) { float r; asm("v_fma_f32 %0, %1, %2, %3" : "=v"(r) : "v"(a), "v"(b), "v"(c)); return r; }
DEV float dot2(u32 a, u32 b, float c) { return __builtin_amdgcn_fdot2_f32_bf16(__builtin_bit_cast(bf16x2, a), __builtin_bit_cast(bf16x2, b), c, false); }
DEV f32x16 mfma32(u32x4 a, u32x4 b, f32x16 c) { return __builtin_amdgcn_mfma_f32_32x32x16_bf16(__builtin_bit_cast(bf16x8, a), __builtin_bit_cast(bf16x8, b), c, 0, 0, 0); }
DEV f32x4 mfma16(u32x4 a, u32x4 b, f32x4 c) { return __builtin_amdgcn_mfma_f32_16x16x32_bf16(__builtin_bit_cast(bf16x8, a), __builtin_bit_cast(bf16x8, b), c, 0, 0, 0); }
DEV float xhalf_sum(float v) { auto r = __builtin_amdgcn_permlane32_swap(__float_as_uint(v), __float_as_uint(v), false, false); return __uint_as_float(r[0]) + __uint_as_float(r[1]); }
DEV float xhalf_max(float v) { auto r = __builtin_amdgcn_permlane32_swap(__float_as_uint(v), __float_as_uint(v), false, false); return fmaxf(__uint_as_float(r[0]), __uint_as_float(r[1])); }
#define DPPF(v, ctrl) __uint_as_float(__builtin_amdgcn_update_dpp(0u, __float_as_uint(v), ctrl, 0xf, 0xf, false))
DEV float wave_max(float v) {
  v = fmaxf(v, DPPF(v, 0x121)); v = fmaxf(v, DPPF(v, 0x122)); v = fmaxf(v, DPPF(v, 0x124)); v = fmaxf(v, DPPF(v, 0x128));
  auto r = __builtin_amdgcn_permlane16_swap(__float_as_uint(v), __float_as_uint(v), false, false);
  return xhalf_max(fmaxf(__uint_as_float(r[0]), __uint_as_float(r[1])));
}
template <int I> struct IC { static constexpr int value = I; };
template <int I, int N, class F> DEV void static_for(F&& f) { if constexpr (I < N) { f(IC<I>{}); static_for<I + 1, N>(f); } }
#define WAIT_VM(n) asm volatile("s_waitcnt vmcnt(" #n ")" ::: "memory")
#define BAR() asm volatile("s_waitcnt lgkmcnt(0)\n\ts_barrier" ::: "memory")
#define SB() __builtin_amdgcn_sched_barrier(0)
#define CFENCE() asm volatile("" ::: "memory")
template <bool SC1> DEV void dma16(const void* sbase, u32 voff, u32 lds_dst) {
  u32 keep;
  if (SC1) asm volatile("s_mov_b32 %0, m0\n\ts_mov_b32 m0, %3\n\ts_nop 2\n\tglobal_load_lds_dwordx4 %1, %2 sc1\n\ts_mov_b32 m0, %0" : "=&s"(keep) : "v"(voff), "s"(sbase), "s"(lds_dst) : "memory");
  else asm volatile("s_mov_b32 %0, m0\n\ts_mov_b32 m0, %3\n\ts_nop 2\n\tglobal_load_lds_dwordx4 %1, %2\n\ts_mov_b32 m0, %0" : "=&s"(keep) : "v"(voff), "s"(sbase), "s"(lds_dst) : "memory");
}
DEV void dma16x4(const void* sbase, u32 voff, u32 lds_dst) {
  u32 keep;
  asm volatile("s_mov_b32 %0, m0\n\ts_mov_b32 m0, %3\n\ts_nop 2\n\tglobal_load_lds_dwordx4 %1, %2 sc1\n\tglobal_load_lds_dwordx4 %1, %2 offset:1024 sc1\n\t"
               "global_load_lds_dwordx4 %1, %2 offset:2048 sc1\n\tglobal_load_lds_dwordx4 %1, %2 offset:3072 sc1\n\ts_mov_b32 m0, %0" : "=&s"(keep) : "v"(voff), "s"(sbase), "s"(lds_dst) : "memory");
}
DEV void dma4_touch(const void* sbase, u32 voff, u32 lds_dst) {
  u32 keep;
  asm volatile("s_mov_b32 %0, m0\n\ts_mov_b32 m0, %3\n\ts_nop 2\n\tglobal_load_lds_dword %1, %2 sc1\n\ts_mov_b32 m0, %0" : "=&s"(keep) : "v"(voff), "s"(sbase), "s"(lds_dst) : "memory");
}
DEV void dma4_plain(const void* sbase, u32 voff, u32 lds_dst) {
  u32 keep;
  asm volatile("s_mov_b32 %0, m0\n\ts_mov_b32 m0, %3\n\ts_nop 2\n\tglobal_load_lds_dword %1, %2\n\ts_mov_b32 m0, %0" : "=&s"(keep) : "v"(voff), "s"(sbase), "s"(lds_dst) : "memory");
}
DEV u32x4 lds128(u32 a) { return *(const LAS u32x4*)a; }
DEV f32x4 lds128f(u32 a) { return *(const LAS f32x4*)a; }
DEV float lds32f(u32 a) { return *(const LAS float*)a; }
DEV u32x2 ldstr(u32 a) { return __builtin_bit_cast(u32x2, __builtin_amdgcn_ds_read_tr16_b64_v4i16((LAS s16x4*)a)); }
DEV u32x4 cat(u32x2 a, u32x2 b) { return (u32x4){a.x, a.y, b.x, b.y}; }
typedef __amdgpu_buffer_rsrc_t rsrc_t;
DEV u32x4 ld128(rsrc_t r, u32 voff, u32 soff) { return __builtin_amdgcn_raw_buffer_load_b128(r, voff, soff, 16); }
DEV u32 ld32(rsrc_t r, u32 voff, u32 soff) { return __builtin_amdgcn_raw_buffer_load_b32(r, voff, soff, 16); }
DEV void st128(rsrc_t r, u32 voff, u32 soff, u32x4 v) {
  __builtin_amdgcn_raw_buffer_store_b128(v, r, voff, soff, 16);
  asm volatile("s_nop 1" :: "v"(v) : "memory");
}
DEV void st32(rsrc_t r, u32 voff, u32 soff, u32 v) { __builtin_amdgcn_raw_buffer_store_b32(v, r, voff, soff, 16); }
constexpr u32 POISON = 0xFFFFFFFFu;
DEV u32x4 nopoison(u32x4 v) { return (u32x4){min(v[0], POISON - 1u), min(v[1], POISON - 1u), min(v[2], POISON - 1u), min(v[3], POISON - 1u)}; }
DEV u32 umax4(u32x4 v) { return max(max(v[0], v[1]), max(v[2], v[3])); }
DEV void pin32(u32& v) { asm volatile("" : "+v"(v)); }
DEV u32 poll32(const char* p) { return __hip_atomic_load((GAS u32*)p, RLX_AGENT); }
DEV u64 poll64(const char* p) { return __hip_atomic_load((GAS u64*)p, RLX_AGENT); }
DEV void flag32(char* p, u32 v) { __hip_atomic_store((GAS u32*)p, v, RLX_AGENT); }
DEV void flag64(char* p, u32 val, u32 tag) { __hip_atomic_store((GAS u64*)p, ((u64)tag << 32) | val, RLX_AGENT); }
DEV void wait_flags(const char* p, u32 tag) {
#pragma nounroll
  for (int i = 0; i < SPIN_CAP; ++i) { if (__all(poll32(p) == tag)) break; __builtin_amdgcn_s_sleep(POLL_SLEEP); }
  CFENCE();
}
DEV u32 wait_gran(const char* p, u32 tag) {
  u64 g = 0;
#pragma nounroll
  for (int i = 0; i < SPIN_CAP; ++i) { g = poll64(p); if (__all((u32)(g >> 32) == tag)) break; __builtin_amdgcn_s_sleep(POLL_SLEEP); }
  CFENCE();
  return (u32)g;
}
DEV u32 lds_ld(u32 a) { return __hip_atomic_load((LAS u32*)a, __ATOMIC_RELAXED, __HIP_MEMORY_SCOPE_WORKGROUP); }
DEV void lds_st(u32 a, u32 v) { __hip_atomic_store((LAS u32*)a, v, __ATOMIC_RELAXED, __HIP_MEMORY_SCOPE_WORKGROUP); }
DEV u32 lds_inc(u32 a) { return __builtin_amdgcn_readfirstlane(__hip_atomic_fetch_add((LAS u32*)a, 1u, __ATOMIC_RELAXED, __HIP_MEMORY_SCOPE_WORKGROUP)); }
DEV void lds_wait(u32 a, u32 want) {
#pragma nounroll
  for (int i = 0; i < (SPIN_CAP << 6); ++i) { if (__builtin_amdgcn_readfirstlane(lds_ld(a)) >= want) break; __builtin_amdgcn_s_sleep(1); }
  CFENCE();
}
#define WAIT_LGKM() asm volatile("s_waitcnt lgkmcnt(0)" ::: "memory")
#define STAMP(i) do {} while (0)
#define STAMPL(i, s) STAMP(i)

__global__ __launch_bounds__(512, 2) void fused_block(const u16* __restrict__ x, const u16* __restrict__ wqkv, const u16* __restrict__ wo,
                                                      const u16* __restrict__ wg, const u16* __restrict__ wu, const u16* __restrict__ wd,
                                                      const u16* __restrict__ cvec, const u16* __restrict__ wmod, u16* __restrict__ out, char* ws) {
  __shared__ __attribute__((aligned(16))) char smem[LDS_BYTES];
  const int b = blockIdx.x, tid = threadIdx.x, lane = tid & 63;
  const int wave = __builtin_amdgcn_readfirstlane(tid >> 6);
  const int l31 = lane & 31, hi = lane >> 5;
  const int xg = b & 7, slot = b >> 3;
  const int ai = 4 * (xg >> 1) + (slot & 3), ah = 8 * (xg & 1) + (slot >> 2);
  const u32 lb = (u32)(uintptr_t)(LAS char*)smem;
  const rsrc_t wsr = __builtin_amdgcn_make_buffer_rsrc(ws, 0, WS_USED, 0x00020000);
  STAMP(0);
  {
    u64 pc; asm volatile("s_getpc_b64 %0" : "=s"(pc));
    if (wave == 0 && !NOCODEPF) {
      const u32 off = min((u32)(slot * 1408 + min(lane, 10) * 128), (u32)CODE_BYTES);
      u32 keep;
      asm volatile("s_mov_b32 %0, m0\n\ts_mov_b32 m0, %3\n\ts_nop 2\n\tglobal_load_lds_dword %1, %2\n\ts_mov_b32 m0, %0" : "=&s"(keep) : "v"(off), "s"(pc), "s"(lb + L_ST) : "memory");
    }
  }
  asm volatile("" :: "s"(x), "s"(wqkv), "s"(wo), "s"(wg), "s"(wu), "s"(wd), "s"(cvec), "s"(wmod), "s"(out), "s"(ws) : "memory");
  SB();
  if (PRETOUCH >= 1 && wave == 1)
    dma4_plain(ws, lane < 16 || PRETOUCH < 2 ? min(lane, 12) << 21 : min(slot * 14 + min(lane - 16, 13), 415) << 16, lb + L_ST + 256);
  if (PRETOUCH >= 2 && wave == 3) dma4_plain(out, min(lane, 27) << 16, lb + L_ST + 512);
  if (wave == 2 && lane < 32) lds_st(lb + L_SYNC + lane * 4, 0u);
  const u64 t_entry = __builtin_amdgcn_s_memrealtime();
  const u32 epoch_v = poll32(ws);
  u32 tag;

  f32x16 accQ, accK, accV;
  const int ort = 8 * xg + (slot >> 2), ocq = slot & 3;
  const u32 OFF_MODG = OFF_MODG0 + (NREP == 8 ? xg : 0) * MODG_REP;
  const int tq4 = (lane & 15) >> 2, tp4 = lane & 3, grp = lane >> 4;
  if (b < 42) {
    u32x4 wv[7];
    const u32 cv = cvec[56 * wave + min(lane, 55)];
#pragma unroll
    for (int n = 0; n < 7; ++n) wv[n] = *(const u32x4*)((const char*)wmod + (size_t)(56 * wave + 8 * n + (lane >> 3)) * 5376 + 128 * b + 16 * (lane & 7));
    BAR();
    const float cf = bflo(cv);
    const float sl = rbf(cf * __builtin_amdgcn_rcpf(1.f + __builtin_amdgcn_exp2f(-1.4426950408889634f * cf)));
    float acc[8] = {0.f, 0.f, 0.f, 0.f, 0.f, 0.f, 0.f, 0.f};
#pragma unroll
    for (int n = 0; n < 7; ++n) {
      const float sn = __uint_as_float(__builtin_amdgcn_ds_bpermute((8 * n + (lane >> 3)) * 4, __float_as_uint(sl)));
#pragma unroll
      for (int j = 0; j < 4; ++j) { acc[2 * j] = fmaf(sn, bflo(wv[n][j]), acc[2 * j]); acc[2 * j + 1] = fmaf(sn, bfhi(wv[n][j]), acc[2 * j + 1]); }
    }

#pragma unroll
    for (int j = 0; j < 8; ++j) acc[j] += DPPF(acc[j], 0x128);
#pragma unroll
    for (int j = 0; j < 4; ++j) { auto r = __builtin_amdgcn_permlane16_swap(__float_as_uint(acc[j]), __float_as_uint(acc[j + 4]), false, false); acc[j] = __uint_as_float(r[0]) + __uint_as_float(r[1]); }
#pragma unroll
    for (int j = 0; j < 2; ++j) { auto r = __builtin_amdgcn_permlane32_swap(__float_as_uint(acc[j]), __float_as_uint(acc[j + 2]), false, false); acc[j] = __uint_as_float(r[0]) + __uint_as_float(r[1]); }
    if (!(lane & 8)) *(LAS f32x2*)(lb + L_MODP + (wave * 64 + 8 * (lane & 7) + 2 * hi + 4 * (grp & 1)) * 4) = (f32x2){acc[0], acc[1]};
    WAIT_LGKM();
    u32 arrived = 0;
    if (lane == 0) arrived = __hip_atomic_fetch_add((LAS u32*)(lb + L_SYNC + 12), 1u, __ATOMIC_RELAXED, __HIP_MEMORY_SCOPE_WORKGROUP);
    if (__builtin_amdgcn_readfirstlane(arrived) == 7) {
      CFENCE();
      f32x2 t = {0.f, 0.f};
#pragma unroll
      for (int p = 0; p < 8; ++p) t += *(const LAS f32x2*)(lb + L_MODP + (p * 64 + 2 * l31) * 4);
      const u32 pv = pk2(t[0], t[1]);
      const u32 g0 = __builtin_amdgcn_ds_bpermute((2 * (lane & 15)) * 4, pv), g1 = __builtin_amdgcn_ds_bpermute((2 * (lane & 15) + 1) * 4, pv);
#pragma unroll
      for (int r = 0; r < NREP / 4; ++r) st128(wsr, (u32)(32 * b + 2 * (lane & 15)) * 8 + (4 * r + grp) * MODG_REP, OFF_MODG0, (u32x4){g0, epoch_v + 1u, g1, epoch_v + 1u});
      WAIT_VM(0);
      lds_st(lb + L_SYNC + 32, 1u);
    }
  } else if (wave < 3 && 3 * (b - 42) + wave < 512) {
    const int xgp = 3 * (b - 42) + wave, row = 4 * xgp + (lane & 3), c0i = lane >> 2;
    u32x4 xr[4];
#pragma unroll
    for (int n = 0; n < 4; ++n) xr[n] = *(const u32x4*)((const char*)x + (size_t)row * 896 + min(c0i + 16 * n, 55) * 16);
    BAR();
    if (c0i >= 8) xr[3] = (u32x4){0u, 0u, 0u, 0u};
    float s1[2] = {0.f, 0.f}, s2[2] = {0.f, 0.f};
#pragma unroll
    for (int n = 0; n < 4; ++n)
#pragma unroll
      for (int j = 0; j < 4; ++j) { s1[j & 1] = dot2(xr[n][j], 0x3F803F80u, s1[j & 1]); s2[j & 1] = dot2(xr[n][j], xr[n][j], s2[j & 1]); }
    float t1 = s1[0] + s1[1], t2 = s2[0] + s2[1];
    t1 += DPPF(t1, 0x124); t2 += DPPF(t2, 0x124); t1 += DPPF(t1, 0x128); t2 += DPPF(t2, 0x128);
    { auto r = __builtin_amdgcn_permlane16_swap(__float_as_uint(t1), __float_as_uint(t2), false, false); t1 = __uint_as_float(r[0]) + __uint_as_float(r[1]); }
    t1 = xhalf_sum(t1);
    { auto r = __builtin_amdgcn_permlane16_swap(__float_as_uint(t1), __float_as_uint(t1), false, false); t1 = __uint_as_float(r[0]); t2 = __uint_as_float(r[1]); }
    const float mean = t1 * (1.f / 448.f), ex2 = t2 * (1.f / 448.f);
    const float rstd = __builtin_amdgcn_rsqf(fmaxf(ex2 - mean * mean, 0.f) + 1e-5f), c0 = -mean * rstd;
    asm volatile("" :: "v"(c0));

#pragma unroll
    for (int n = 0; n < 4; ++n) {
      u32x4 o;
#pragma unroll
      for (int j = 0; j < 4; ++j) asm("v_cvt_pk_f16_f32 %0, %1, %2" : "=v"(o[j]) : "v"(fmaf(bflo(xr[n][j]), rstd, c0)), "v"(fmaf(bfhi(xr[n][j]), rstd, c0)));
      const u32 c = c0i + 16 * n;
      st128(wsr, c < 56 ? (c >> 1) * 1024 + ((row & 31) + 32 * (c & 1)) * 16 + (u32)(row >> 5) * 28672 : 0x80000000u, OFF_XH, o);
    }
    WAIT_VM(0);
    if (lane == 0) flag32(ws + OFF_XF + xgp * 128, epoch_v + 1u);
  } else if ((wave == 4 || wave == 5) && b < 210) {
    const int u = b - 42, part = u / 56, rem = u - part * 56, hp = rem / 7, kg = rem - hp * 7, s0 = 4 * kg + 2 * (wave - 4);
    const char* src = (const char*)wqkv + (size_t)(16 * s0) * 2688 + part * 896 + hp * 112;
    const u32 stg = lb + L_Q + (wave - 4) * 4096;
#pragma unroll
    for (int i = 0; i < 4; ++i) { const u32 c = lane & 7; dma16<false>(src + i * 8 * 2688, (lane >> 3) * 2688 + 16 * (c == 7 ? 6 : c), stg + i * 1024); }
    BAR();
    WAIT_VM(0);

#pragma unroll
    for (int e = 0; e < 4; ++e) {
      const u32 a = stg + (16 * (e >> 1) + 8 * (grp >> 1) + tq4) * 128 + (e & 1) * 56 + (grp & 1) * 32 + tp4 * 8;
      u32x4 f = cat(ldstr(a), ldstr(a + 512));
      if (l31 >= 28) f = (u32x4){0u, 0u, 0u, 0u};
      st128(wsr, lane * 16, OFF_WQT + (u32)((2 * hp + (e & 1)) * 84 + 3 * (s0 + (e >> 1)) + part) * 1024, f);
    }
    WAIT_VM(0);
    if (lane < 2) flag32(ws + OFF_WQF + ((2 * hp + lane) * 64 + 3 * (s0 >> 1) + part) * 128, epoch_v + 1u);
  } else {
    BAR();
  }
  tag = __builtin_amdgcn_readfirstlane(epoch_v) + 1u;
  STAMPL(7, "unit done");
  const int pj = max(b - 42, 0), cg = pj / 7, kg = pj - cg * 7;
  const bool typeA = b >= 42 && b < 238, typeB = b >= 238;
  if (wave >= 6) {
#pragma nounroll
    for (int i = 0; i < 4096 && __builtin_amdgcn_s_memrealtime() - t_entry < PREP_START; ++i) __builtin_amdgcn_s_sleep(1);
#pragma unroll
    for (int ph = 0; ph < 2; ++ph) {
      const int pw = wave & 1, tw = 2 * ph + pw;
      const u32 stg = lb + L_STG + tw * 8192;
      if (typeA) {
#pragma unroll
        for (int i = 0; i < 2; ++i) {
          const u32 vo = (u32)(64 * kg + 32 * ph + 16 * i + (lane >> 2)) * 3584 + (64 * cg + 32 * pw) * 2 + 16 * (lane & 3);
          dma16<false>(wg, vo, stg + (2 * i) * 1024); dma16<false>(wu, vo, stg + (2 * i + 1) * 1024);
        }
        const int p = 4 * pj + tw, ds_ = p / 7, np = p - ds_ * 7;
#pragma unroll
        for (int i = 0; i < 2; ++i) dma16<false>(wd, (u32)(16 * ds_ + (lane >> 2)) * 896 + np * 128 + i * 64 + 16 * (lane & 3), stg + (4 + i) * 1024);
      } else if (typeB) {
#pragma unroll
        for (int e = 0; e < 4; ++e) {
          const int q = min(4 * (b - 238) + tw + 72 * e, 223), h = q / 14, nq = q - h * 14;
#pragma unroll
          for (int i = 0; i < 2; ++i) dma16<false>(wo, (u32)min(28 * h + 16 * i + (lane >> 2), 447) * 896 + nq * 64 + 16 * (lane & 3), stg + (2 * e + i) * 1024);
        }
      }
    }
    STAMPL(6, "prep issued");
    WAIT_VM(0);
    if (lane == 0) __hip_atomic_fetch_add((LAS u32*)(lb + L_SYNC + 52), 1u, __ATOMIC_RELAXED, __HIP_MEMORY_SCOPE_WORKGROUP);
    STAMPL(5, "prep landed");
  } else {
#pragma nounroll
    for (int i = 0; i < 4096 && __builtin_amdgcn_s_memrealtime() - t_entry < (wave == 4 ? MOD_POLL : wave >= 3 ? FIRST_POLL : TOUCH_AT); ++i) __builtin_amdgcn_s_sleep(1);
    if (TOUCH_AT && wave < 3) {
      dma4_touch(ws + OFF_WQT + (size_t)ah * 86016 + (slot & 3) * 21504, min(wave * 64 + lane, 167) * 128, lb + L_ST + wave * 256);
      if (wave < 2) dma4_touch(ws + OFF_XH + (size_t)ai * 114688 + (slot >> 2) * 14336, min(wave * 64 + lane, 111) * 128, lb + L_ST + 768 + wave * 256);
    }
  }

  if (wave < 4) {
    u32x4 xf[28], pf[8]; u32 pfo[8], uvo[2]; float uvv[2];
    const int pw = wave & 1, ph = (wave >> 1) & 1;
    auto tile_read = [&]() __attribute__((always_inline)) {
      const u32 stg = lb + L_STG + wave * 8192;
      const int p = 4 * pj + wave, ds_ = p / 7, np = p - ds_ * 7;
      lds_wait(lb + L_SYNC + 52, 2);
      const int pi = ((tp4 & 1) << 1) | (tp4 >> 1);
      const u32 aA = stg + (grp & 1) * 1024 + (8 * (grp >> 1) + tq4) * 64 + pi * 8, aD = stg + 4096 + (8 * (grp >> 1) + tq4) * 64 + (grp & 1) * 32 + tp4 * 8;
      const u32 aB = stg + (16 * (grp >> 1) + 4 * (grp & 1) + tq4) * 64 + tp4 * 8;
#pragma unroll
      for (int f = 0; f < 8; ++f) {
        const int i = f >> 1, e = f & 1;
        const u32 a = typeB ? aB + i * 2048 + e * 32 : f < 4 ? aA + i * 2048 + e * 32 : aD + e * 1024;
        u32x4 v = cat(ldstr(a), ldstr(a + (typeB ? 512 : 256)));
        if (typeB && grp == 3) { v[2] = 0u; v[3] = 0u; }
        pf[f] = v;
        const int q = 4 * (b - 238) + wave + 72 * i, h = q / 14, nq = q - h * 14;
        pfo[f] = lane * 16 + (typeB ? (q < 224 ? OFF_WOT + (u32)((2 * nq + e) * 16 + h) * 1024 : 0x80000000u)
                              : !typeA || f >= 6 ? 0x80000000u
                              : f < 4 ? OFF_WGU + (u32)((4 * cg + 2 * pw + e) * 28 + 4 * kg + 2 * ph + i) * 1024 : OFF_WDT + (u32)((2 * np + e) * 112 + ds_) * 1024);
      }
#pragma unroll
      for (int i = 0; i < 2; ++i) uvo[i] = lane * 4 + (typeA ? OFF_UV + (u32)((2 * kg + ph) * 112 + 4 * cg + 2 * pw + i) * 256 : 0x80000000u);
      asm volatile("" : "+v"(pf[0]), "+v"(pf[1]), "+v"(pf[2]), "+v"(pf[3]), "+v"(pf[4]), "+v"(pf[5]), "+v"(pf[6]), "+v"(pf[7]));
    };
    if (wave == 3) { wait_flags(ws + OFF_XF + (32 * ai + l31) * 128, tag); if (GATE) lds_wait(lb + L_SYNC + 36, 1); lds_st(lb + L_SYNC, 1u); }
    else { tile_read(); lds_wait(lb + L_SYNC, 1); }

    constexpr int XPRE = 12;
#define LDX(s) xf[s] = ld128(wsr, lane * 16 + ((s) & 3) * 1024, OFF_XH + (u32)((4 * ai + wave) * 28 + ((s) & ~3)) * 1024)
#pragma unroll
    for (int s = 0; s < XP0; ++s) { LDX(s); SB(); }
    if (wave == 3) tile_read();
    if (!GATE) lds_wait(lb + L_SYNC + 36, 1);
    uvv[0] = 0.f; uvv[1] = 0.f;
    if (typeA) {
      float uu[2] = {0.f, 0.f}, vv[2] = {0.f, 0.f};
#pragma unroll
      for (int i = 0; i < 2; ++i) {
        const u32x4 be = lds128(lb + L_G2 + (2 * ph + i) * 32 + hi * 16), ga = lds128(lb + L_G2 + 128 + (2 * ph + i) * 32 + hi * 16);
#pragma unroll
        for (int e = 0; e < 2; ++e)
#pragma unroll
          for (int j = 0; j < 4; ++j) {
            const u32 w = pf[2 * i + e][j];
            vv[e] = dot2(w, be[j], vv[e]);
            pf[2 * i + e][j] = pk2(bflo(w) * bflo(ga[j]), bfhi(w) * bfhi(ga[j]));
            uu[e] = dot2(pf[2 * i + e][j], 0x3F803F80u, uu[e]);
          }
      }
#pragma unroll
      for (int e = 0; e < 2; ++e) { const float us = xhalf_sum(uu[e]), vs = xhalf_sum(vv[e]); uvv[e] = hi ? vs : us; }
      asm volatile("" : "+v"(pf[0]), "+v"(pf[1]), "+v"(pf[2]), "+v"(pf[3]), "+v"(uvv[0]), "+v"(uvv[1]));
      STAMPL(10, "prep tiled");
    }
#pragma unroll
    for (int s = XP0; s < XPRE; ++s) { LDX(s); SB(); }
    lds_wait(lb + L_SYNC + 4, 4);

    STAMPL(8, "W1 ready");
    accQ = (f32x16){}; accK = (f32x16){}; accV = (f32x16){};
    {
      u32 gb = lb + L_GAM + hi * 16; asm volatile("" : "+v"(gb));
      u32 wb = lb + lane * 16, wb2 = wb + 43008; asm volatile("" : "+v"(wb), "+v"(wb2));
      u32x4 wf[3][3], hf[2], gs[3], be[3]; float ht[8];
#define RDG(s) gs[(s) % 3] = lds128(gb + (s) * 32)
#define RDB(s) be[(s) % 3] = lds128(gb + 896 + (s) * 32)
#define RDW1(s, p) wf[(s) % 3][p] = lds128(((s) < 14 ? wb : wb2) + (3 * ((s) % 14) + (p)) * 1024)
#define RDW(s) do { RDW1(s, 0); RDW1(s, 1); RDW1(s, 2); } while (0)
#define DOTS(s, h) do { _Pragma("unroll") for (int j = 2 * (h); j < 2 * (h) + 2; ++j) { \
        asm volatile("v_fma_mix_f32 %0, %1, %2, %3 op_sel_hi:[1,1,1]" : "=v"(ht[2 * j]) : "v"(xf[s][j]), "v"(gs[(s) % 3][j]), "v"(be[(s) % 3][j])); \
        asm volatile("v_fma_mix_f32 %0, %1, %2, %3 op_sel:[1,1,1] op_sel_hi:[1,1,1]" : "=v"(ht[2 * j + 1]) : "v"(xf[s][j]), "v"(gs[(s) % 3][j]), "v"(be[(s) % 3][j])); } } while (0)
#define CVTS(s) do { hf[(s) & 1] = (u32x4){pk2(ht[0], ht[1]), pk2(ht[2], ht[3]), pk2(ht[4], ht[5]), pk2(ht[6], ht[7])}; asm volatile("" : "+v"(hf[(s) & 1])); } while (0)
      RDG(0); RDB(0); RDW(0); RDG(1); RDB(1); RDW(1); RDG(2); RDB(2); RDW(2); DOTS(0, 0); DOTS(0, 1); CVTS(0); SB();
#pragma unroll
      for (int s = 0; s < 28; ++s) {
        if (s == 1) lds_wait(lb + L_SYNC + 8, 3);
        if (s == 13) lds_wait(lb + L_SYNC + 24, 3);
        SB(); accQ = mfma32(wf[s % 3][0], hf[s & 1], accQ); SB();
        if (s < 25) RDG(s + 3);
        if (s < 25) RDW1(s + 3, 0);
        if (s < 27) DOTS(s + 1, 0);
        SB(); accK = mfma32(wf[s % 3][1], hf[s & 1], accK); SB();
        if (s < 25) RDB(s + 3);
        if (s < 25) RDW1(s + 3, 1);
        if (s < 27) DOTS(s + 1, 1);
        SB(); accV = mfma32(hf[s & 1], wf[s % 3][2], accV); SB();
        if (s < 25) RDW1(s + 3, 2);
        if (s + XPRE < 28) LDX(s + XPRE);
        if (s >= PST && s < PST + 8) st128(wsr, pfo[s - PST], 0, pf[s - PST]);
        else if (s >= PST + 8 && s < PST + 10) st32(wsr, uvo[s - PST - 8], 0, __float_as_uint(uvv[s - PST - 8]));
        if (s < 27) CVTS(s + 1);
        SB();
      }
    }
    STAMPL(5, "qkv mfma done");
    const float unscale = __uint_as_float(__builtin_amdgcn_readfirstlane(lds_ld(lb + L_SYNC + 28)));
    if (unscale != 1.f) { accQ *= unscale; accK *= unscale; accV *= unscale; }
    u32x4 k0, k1, v0, v1, q0, q1;
#pragma unroll
    for (int j = 0; j < 4; ++j) {
      k0[j] = pk2(accK[2 * j], accK[2 * j + 1]); k1[j] = pk2(accK[8 + 2 * j], accK[9 + 2 * j]);
      v0[j] = pk2(accV[2 * j], accV[2 * j + 1]); v1[j] = pk2(accV[8 + 2 * j], accV[9 + 2 * j]);
    }
    if (hi) { k1[2] = 0x3F803F80u; k1[3] = 0x00003F80u; }
    if (l31 == 28) { v0 = (u32x4){0x3F803F80u, 0x3F803F80u, 0x3F803F80u, 0x3F803F80u}; v1 = v0; }
    const u32 kvo = OFF_KV + (u32)(ah * 64 + ai * 4 + wave) * 4096;
    st128(wsr, lane * 16, kvo, k0); st128(wsr, lane * 16 + 1024, kvo, k1); st128(wsr, lane * 16 + 2048, kvo, v0); st128(wsr, lane * 16 + 3072, kvo, v1);
    SB();
    float kn = 0.f, qn = 0.f;
    constexpr float QS = 0.18898223650461362f * 1.4426950408889634f;
#pragma unroll
    for (int r = 0; r < 16; ++r) { kn = fmaf(accK[r], accK[r], kn); accQ[r] *= QS; qn = fmaf(accQ[r], accQ[r], qn); }
#pragma unroll
    for (int j = 0; j < 4; ++j) { q0[j] = pk2(accQ[2 * j], accQ[2 * j + 1]); q1[j] = pk2(accQ[8 + 2 * j], accQ[9 + 2 * j]); }
    *(LAS u32x4*)(lb + L_Q + wave * 2048 + lane * 16) = q0; *(LAS u32x4*)(lb + L_Q + wave * 2048 + 1024 + lane * 16) = q1;
    qn = xhalf_sum(qn); kn = wave_max(xhalf_sum(kn));
    if (!hi) *(LAS float*)(lb + L_QN + (wave * 32 + l31) * 4) = qn;
    WAIT_VM(0);
    if (lane == 0) flag64(ws + OFF_KVG + (ah * 64 + ai * 4 + wave) * 128, __float_as_uint(kn), tag);
    if (b < 42) lds_wait(lb + L_SYNC + 32, 1);
    if (lane == 0 && __hip_atomic_fetch_add((LAS u32*)(lb + L_SYNC + 44), 1u, __ATOMIC_RELAXED, __HIP_MEMORY_SCOPE_WORKGROUP) == 4u) flag32(ws + OFF_PREPF + b * 128, tag);
    STAMPL(6, "kv flagged");
  } else {
    if (wave == 4) {
      u64 gv[8];
      __builtin_amdgcn_s_setprio(3);
#pragma nounroll
      for (int i = 0; i < SPIN_CAP; ++i) {
        bool ok = true;
#pragma unroll
        for (int n = 0; n < 7; ++n) gv[n] = poll64(ws + OFF_MODG + (64 * n + lane) * 8);
        gv[7] = poll64(ws + OFF_MODG + (672 + 224 * hi + 32 * kg + l31) * 8);
#pragma unroll
        for (int n = 0; n < 8; ++n) ok = ok && (u32)(gv[n] >> 32) == tag;
        if (__all(ok)) break;
        __builtin_amdgcn_s_sleep(POLL_SLEEP);
      }
      CFENCE();
      __builtin_amdgcn_s_setprio(0);

      *(LAS u32*)(lb + L_G2 + lane * 4) = hi ? pk2(1.f + bflo((u32)gv[7]), 1.f + bfhi((u32)gv[7])) : (u32)gv[7];
      WAIT_LGKM();
      lds_st(lb + L_SYNC + 36, 1u);

      float lo[7], hi2[7], mxv = 0.f;
#pragma unroll
      for (int n = 0; n < 7; ++n) {
        u32 v = (u32)gv[n];
        if (64 * n + lane >= 224) v = pk2(1.f + bflo(v), 1.f + bfhi(v));
        lo[n] = bflo(v); hi2[n] = bfhi(v); mxv = fmaxf(mxv, fmaxf(fabsf(lo[n]), fabsf(hi2[n])));
      }
      const u32 ex = max((int)(__float_as_uint(wave_max(mxv)) >> 23) - (127 + 14), 0);
      const float scl = __uint_as_float((127u - min(ex, 114u)) << 23);
#pragma unroll
      for (int n = 0; n < 7; ++n) {
        u32 o; asm("v_cvt_pk_f16_f32 %0, %1, %2" : "=v"(o) : "v"(lo[n] * scl), "v"(hi2[n] * scl));
        const int g = 64 * n + lane;
        *(LAS u32*)(lb + L_GAM + (g >= 224 ? g - 224 : g + 224) * 4) = o;
      }
      lds_st(lb + L_SYNC + 28, (127u + min(ex, 114u)) << 23);
      WAIT_LGKM();
      if (lane == 0) __hip_atomic_fetch_add((LAS u32*)(lb + L_SYNC + 4), 1u, __ATOMIC_RELAXED, __HIP_MEMORY_SCOPE_WORKGROUP);
#pragma unroll
      for (int k = 0; k < 15; ++k)
        if (k < 8 || (ort & 3) < 2) st128(wsr, lane * 16, k < 8 ? OFF_OB + (u32)((4 * ai + (k >> 1)) * 16 + ah) * 2048 + (k & 1) * 1024 : OFF_X2 + (u32)(ort * 28 + 7 * ocq + k - 8) * 1024, (u32x4){POISON, POISON, POISON, POISON});
      WAIT_VM(0);
      if (lane == 0 && __hip_atomic_fetch_add((LAS u32*)(lb + L_SYNC + 44), 1u, __ATOMIC_RELAXED, __HIP_MEMORY_SCOPE_WORKGROUP) == 4u) flag32(ws + OFF_PREPF + b * 128, tag);
    } else {
      if (wave == 5) { wait_flags(ws + OFF_WQF + (ah * 64 + min(lane, 41)) * 128, tag);  if (GATE) lds_wait(lb + L_SYNC + 36, 1); lds_st(lb + L_SYNC + 48, 1u); }
      else lds_wait(lb + L_SYNC + 48, 1);

      const char* wsrc = ws + OFF_WQT + (size_t)ah * 84 * 1024 + (wave - 5) * 4096;
#define WDMA(n) dma16x4(wsrc + (n) * 12288, lane * 16, lb + (wave - 5) * 4096 + (n) * 12288)
      WDMA(0); WDMA(1);
      WAIT_VM(4);
      if (lane == 0) __hip_atomic_fetch_add((LAS u32*)(lb + L_SYNC + 4), 1u, __ATOMIC_RELAXED, __HIP_MEMORY_SCOPE_WORKGROUP);
      WDMA(2); WDMA(3); WDMA(4);
      WAIT_VM(4);
      if (lane == 0) __hip_atomic_fetch_add((LAS u32*)(lb + L_SYNC + 8), 1u, __ATOMIC_RELAXED, __HIP_MEMORY_SCOPE_WORKGROUP);
      WDMA(5); WDMA(6);
      WAIT_VM(0);
      if (lane == 0) __hip_atomic_fetch_add((LAS u32*)(lb + L_SYNC + 24), 1u, __ATOMIC_RELAXED, __HIP_MEMORY_SCOPE_WORKGROUP);
      STAMPL(9, "W landed");
    }
  }
  BAR();
  STAMPL(11, "B3");

  const int gi = ai, gfj = ah;
  u32x4 wo_f[16], wgu[28], xres, gaA, gaB;
  {
    u32x4 qf[4][2];
#pragma unroll
    for (int t = 0; t < 4; ++t) { qf[t][0] = lds128(lb + L_Q + t * 2048 + lane * 16); qf[t][1] = lds128(lb + L_Q + t * 2048 + 1024 + lane * 16); }
    float qn2[4];
#pragma unroll
    for (int t = 0; t < 4; ++t) qn2[t] = lds32f(lb + L_QN + (t * 32 + l31) * 4);
    WAIT_LGKM();
    if (lane == 0) __hip_atomic_fetch_add((LAS u32*)(lb + L_SYNC + 40), 1u, __ATOMIC_RELAXED, __HIP_MEMORY_SCOPE_WORKGROUP);
    if (wave == 3) {
      __builtin_amdgcn_s_sleep(KV_SLEEP);
      const float km = wave_max(__uint_as_float(wait_gran(ws + OFF_KVG + (ah * 64 + lane) * 128, tag)));
      lds_st(lb + L_SYNC + 60, __float_as_uint(km));
      WAIT_LGKM();
      lds_st(lb + L_SYNC + 56, 1u);
    } else lds_wait(lb + L_SYNC + 56, 1);
    const float kmax2 = __uint_as_float(lds_ld(lb + L_SYNC + 60));
    STAMPL(12, "kv flags seen");
    const u32 kvb = OFF_KV + (u32)(ah * 64 + 8 * wave) * 4096;
    u32x4 kf[2][2], vf[2][2];
    const u32 kvl = lane * 16;
#define LDKV(D, kt, off) asm volatile("buffer_load_dwordx4 %0, %2, %3, %4 offen offset:" #off " sc1\n\tbuffer_load_dwordx4 %1, %2, %3, %4 offen offset:" #off "+1024 sc1" \
      : "=&v"(D[(kt) & 1][0]), "=&v"(D[(kt) & 1][1]) : "v"(kvl), "s"(wsr), "s"(kvb + (kt) * 4096) : "memory")
#define WAITN(n) asm volatile("s_waitcnt vmcnt(%0)" :: "i"(n) : "memory")
#define PINKV(D, kt) asm volatile("" : "+v"(D[(kt) & 1][0]), "+v"(D[(kt) & 1][1]))
#define WAITKV(D, kt, n) do { WAITN(n); SB(); PINKV(D, kt); } while (0)
#define WAITKV2(D, kt, n0, n1) do { if (wave < 4) WAITN(n0); else WAITN(n1); SB(); PINKV(D, kt); } while (0)
    asm volatile("s_nop 4");
    LDKV(kf, 0, 0); LDKV(vf, 0, 2048); LDKV(kf, 1, 0);
    float sh[4]; bool safe = true;
#pragma unroll
    for (int t = 0; t < 4; ++t) { sh[t] = 1.02f * __builtin_amdgcn_sqrtf(qn2[t] * kmax2); safe = safe && (sh[t] <= 48.f); }
    if (!__all(safe)) {
      BAR();
      float mx[4] = {-INFINITY, -INFINITY, -INFINITY, -INFINITY};
#pragma unroll 1
      for (int kt = 0; kt < 8; ++kt) {
        const u32x4 ka = ld128(wsr, lane * 16, kvb + kt * 4096), kb = ld128(wsr, lane * 16 + 1024, kvb + kt * 4096);
#pragma unroll
        for (int t = 0; t < 4; ++t) {
          f32x16 s = mfma32(ka, qf[t][0], (f32x16){}); s = mfma32(kb, qf[t][1], s);
#pragma unroll
          for (int r = 0; r < 16; ++r) mx[t] = fmaxf(mx[t], s[r]);
        }
      }
#pragma unroll
      for (int t = 0; t < 4; ++t) { const float m = xhalf_max(mx[t]); if (!hi) *(LAS float*)(lb + L_MX + (wave * 128 + t * 32 + l31) * 4) = m; }
      BAR();
#pragma unroll
      for (int t = 0; t < 4; ++t) {
        float m = -INFINITY;
#pragma unroll
        for (int w = 0; w < 8; ++w) m = fmaxf(m, lds32f(lb + L_MX + (w * 128 + t * 32 + l31) * 4));
        sh[t] = m;
      }
    }
    if (hi) {
#pragma unroll
      for (int t = 0; t < 4; ++t) {
        const float a = rbf(-sh[t]), r1 = -sh[t] - a, c = rbf(r1);
        qf[t][1][2] = pk2(a, c); qf[t][1][3] = pk2(r1 - c, 0.f);
      }
    }
    f32x16 o[4] = {(f32x16){}, (f32x16){}, (f32x16){}, (f32x16){}};
    WAITKV(kf, 0, 4);
    f32x16 sc = mfma32(kf[0][0], qf[0][0], (f32x16){}); sc = mfma32(kf[0][1], qf[0][1], sc);
    u32x4 p0, p1;
#define EXP2(v) __builtin_amdgcn_exp2f(v)
    float ec, ed;
#define GRP(r, PREV, MF, CUR) do { float ea = EXP2(sc[r]), eb = EXP2(sc[(r) + 1]); asm volatile("" : "+v"(ea), "+v"(eb)); PREV; SB(); MF; SB(); \
      ec = EXP2(sc[(r) + 2]); ed = EXP2(sc[(r) + 3]); asm volatile("" : "+v"(ec), "+v"(ed)); { u32 pa = pk2(ea, eb); asm volatile("" : "+v"(pa)); CUR = pa; } SB(); } while (0)
#define PKP(D) do { u32 pb = pk2(ec, ed); asm volatile("" : "+v"(pb)); D = pb; } while (0)
    u32 pwb = lb + wave * 8192 + lane * 16, lwb = lb + L_LP + wave * 512 + l31 * 4; asm volatile("" : "+v"(pwb), "+v"(lwb));
#define WRP(t) do { _Pragma("unroll") for (int g = 0; g < 2; ++g) *(LAS u32x4*)(pwb + ((t) * 2 + g) * 1024) = (u32x4){pk2(o[t][8 * g], o[t][8 * g + 1]), pk2(o[t][8 * g + 2], o[t][8 * g + 3]), pk2(o[t][8 * g + 4], o[t][8 * g + 5]), pk2(o[t][8 * g + 6], o[t][8 * g + 7])}; \
      if (hi) *(LAS float*)(lwb + (t) * 128) = o[t][12]; } while (0)
    const int w6 = min(wave, 6);
    const u32 wo_base = OFF_WOT + (u32)(7 * ocq + w6) * 16384, wg_base = OFF_WGU + (u32)(7 * gfj + w6) * 28672;
    auto nslot = [](int kt, int t, bool) constexpr { const int sl = 4 * (kt - 2) + t;
      return (t == 0 && kt < 7 ? 2 : 0) + (t == 2 && kt < 6 ? 2 : 0) + (kt >= 2 ? (sl < 16) + (sl >= 6 && sl < 18) + (sl >= HPS && sl < HPS + 4) : 0); };
    auto nafter = [&](int k0, int t0, int k1, int t1, bool young) constexpr { int n = -2; for (int i = 4 * k0 + t0; i <= 4 * k1 + t1; ++i) n += nslot(i >> 2, i & 3, young); return n; };
#pragma unroll
    for (int kt = 0; kt < 8; ++kt) {
      if (kt == 0) __builtin_amdgcn_s_setprio(3);
      if (kt == 2) __builtin_amdgcn_s_setprio(2);
      if (kt == 4) __builtin_amdgcn_s_setprio(1);
      if (kt == 6 && wave < 4) __builtin_amdgcn_s_setprio(0);
      if (kt == 7) __builtin_amdgcn_s_setprio(0);
#pragma unroll
      for (int t = 0; t < 4; ++t) {
        f32x16 sn;
        const bool more = t < 3 || kt < 7, first = t == 0 && kt == 0;
        const int kn = t < 3 ? (kt & 1) : ((kt + 1) & 1), tn = (t + 1) & 3;
        if (t == 3 && kt < 7) {
          if (kt == 0) WAITKV(kf, 1, nafter(0, 0, 0, 2, false) + 2);
          else if (kt == 1) WAITKV(kf, 2, nafter(0, 2, 1, 2, false));
          else WAITKV(kf, kt + 1, nafter(kt - 1, 2, kt, 2, false));
        }
        GRP(0, if (!first) PKP(p1[3]), if (more) sn = mfma32(kf[kn][0], qf[tn][0], (f32x16){}), p0[0]);
        GRP(4, PKP(p0[1]), if (more) sn = mfma32(kf[kn][1], qf[tn][1], sn), p0[2]);
        GRP(8, PKP(p0[3]), if (t > 0) o[t - 1] = mfma32(vf[kt & 1][1], p1, o[t - 1]); else if (kt > 0) o[3] = mfma32(vf[(kt + 1) & 1][1], p1, o[3]), p1[0]);
        if (t == 0 && kt < 7) LDKV(vf, kt + 1, 2048);
        if (t == 2 && kt < 6) LDKV(kf, kt + 2, 0);
        if (kt == 1 && t == 0 && wave == 3) {
#pragma nounroll
          for (int i = 0; i < SPIN_CAP; ++i) {
            const u32 a = poll32(ws + OFF_PREPF + lane * 128), c = poll32(ws + OFF_PREPF + (64 + lane) * 128), d = poll32(ws + OFF_PREPF + (128 + lane) * 128), e = poll32(ws + OFF_PREPF + (192 + lane) * 128);
            if (__all(a == tag && c == tag && d == tag && e == tag)) break;
            __builtin_amdgcn_s_sleep(8);
          }
          CFENCE();
          if (lane == 0) __hip_atomic_fetch_add((LAS u32*)(lb + L_SYNC + 40), 1u, __ATOMIC_RELAXED, __HIP_MEMORY_SCOPE_WORKGROUP);
        }
        if (t == 0) {
        }
        if (kt >= 2) {
          const int sl = 4 * (kt - 2) + t;
          if (sl == 0) { STAMPL(1, "sl0"); lds_wait(lb + L_SYNC + 40, 9); STAMPL(3, "sl0 ok"); }
          if (sl == 12) STAMPL(4, "sl12");
          if (sl >= HPS && sl < HPS + 4) st128(wsr, wave < 7 ? lane * 16 : 0x80000000u, OFF_HB + (u32)((4 * gi + sl - HPS) * 112 + 7 * gfj + w6) * 1024, (u32x4){POISON, POISON, POISON, POISON});
          if (sl < 10) wo_f[sl] = ld128(wsr, lane * 16, wo_base + sl * 1024);
          else if (sl < 16) wgu[sl - 10] = ld128(wsr, lane * 16, wg_base + (sl - 10) * 1024);
          if (sl >= 6 && sl < 18) dma16<true>(ws + (sl < 12 ? wo_base + (4 + sl) * 1024 : wg_base + (sl - 6) * 1024), lane * 16, lb + L_PARK + w6 * 12288 + (sl - 6) * 1024);
        }
        if (t == 0) {
          if (kt == 0) WAITKV(vf, 0, nafter(0, 0, 0, 0, false) + 4);
          else if (kt == 1) WAITKV(vf, 1, nafter(0, 0, 1, 0, false));
          else WAITKV(vf, kt, nafter(kt - 1, 0, kt, 0, false));
        }
        SB();
        GRP(12, PKP(p1[1]), o[t] = mfma32(vf[kt & 1][0], p0, o[t]), p1[2]);
        if (kt == 7 && t > 0) { WRP(t - 1); SB(); }
        if (more) sc = sn;
      }
    }
    PKP(p1[3]);
    o[3] = mfma32(vf[1][1], p1, o[3]);
    WRP(3);
    STAMPL(13, "attn done");
    BAR();
    const int qt = wave >> 1, sf = wave & 1;
    u32x4 pa[8]; float pl[8];
    {
      u32 base = lb + qt * 2048 + sf * 1024 + lane * 16, lbase = lb + L_LP + qt * 128 + l31 * 4;
      asm volatile("" : "+v"(base), "+v"(lbase));
#pragma unroll
      for (int w = 0; w < 8; ++w) { pa[w] = lds128(base + w * 8192); pl[w] = lds32f(lbase + w * 512); }
      SB();
    }
    float a8[8]; u32 sel0 = 0x00003F80u, sel1 = 0x3F800000u; asm volatile("" : "+v"(sel0), "+v"(sel1));
#pragma unroll
    for (int j = 0; j < 4; ++j) {
      a8[2 * j] = bflo(pa[0][j]); a8[2 * j + 1] = bfhi(pa[0][j]);
#pragma unroll
      for (int w = 1; w < 8; ++w) { a8[2 * j] = dot2(pa[w][j], sel0, a8[2 * j]); a8[2 * j + 1] = dot2(pa[w][j], sel1, a8[2 * j + 1]); }
    }
    const float l = ((pl[0] + pl[4]) + (pl[1] + pl[5])) + ((pl[2] + pl[6]) + (pl[3] + pl[7]));
    const float rl = __builtin_amdgcn_rcpf(l);
    const u32x4 of = {pk2(a8[0] * rl, a8[1] * rl), pk2(a8[2] * rl, a8[3] * rl), pk2(a8[4] * rl, a8[5] * rl), pk2(a8[6] * rl, a8[7] * rl)};
    st128(wsr, (l31 >> 4) * 1024 + ((l31 & 15) + 16 * (2 * sf + hi)) * 16, OFF_OB + (u32)((4 * ai + qt) * 16 + ah) * 2048, nopoison(of));
    WAIT_VM(1);
    BAR();
    if (tid == 0) flag32(ws + OFF_HF + ((gi * 16 + gfj) * 8 + 7) * 128, tag);
    STAMPL(14, "O flagged");
  }

  u32 uvp[14];
  const int otok = 16 * ((lane >> 4) & 1) + (lane & 15), ofe = 112 * ocq + 16 * min(wave, 6) + 8 * hi;
  if (wave < 7) {
#pragma unroll
    for (int s = 12; s < NBA; ++s) wgu[s] = ld128(wsr, lane * 16 + (s & 3) * 1024, OFF_WGU + (u32)((7 * gfj + wave) * 28 + (s & ~3)) * 1024);
#pragma unroll
    for (int j = 0; j < 6; ++j) wo_f[10 + j] = lds128(lb + L_PARK + wave * 12288 + j * 1024 + lane * 16);
#pragma unroll
    for (int j = 6; j < 12; ++j) wgu[j] = lds128(lb + L_PARK + wave * 12288 + j * 1024 + lane * 16);
  } else __builtin_amdgcn_s_sleep(O_SLEEP);
  STAMPL(15, "O first try");
  {
    u32x4 o4[4];
#pragma nounroll
    for (int i = 0; i < SPIN_CAP; ++i) {
      CFENCE();
#pragma unroll
      for (int k = 0; k < 4; ++k) o4[k] = ld128(wsr, lane * 16 + k * 1024, OFF_OB + (u32)(ort * 16 + 2 * wave) * 2048);
      if (!__any(max(max(umax4(o4[0]), umax4(o4[1])), max(umax4(o4[2]), umax4(o4[3]))) == POISON)) break;
      __builtin_amdgcn_s_sleep(2);
    }
#pragma unroll
    for (int k = 0; k < 4; ++k) *(LAS u32x4*)(lb + (4 * wave + k) * 1024 + lane * 16) = o4[k];
    xres = *(const u32x4*)(x + (size_t)(32 * ort + otok) * 448 + ofe);
    gaA = ld128(wsr, ofe * 4, OFF_MODG + 448 * 8); gaB = ld128(wsr, ofe * 4 + 16, OFF_MODG + 448 * 8);
  }
  BAR();
  STAMPL(16, "O landed");
  if (wave < 7) {
    f32x4 y0 = {0.f, 0.f, 0.f, 0.f}, y1 = y0;
    u32x4 oa[10];
#pragma unroll
    for (int i = 0; i < 8; ++i) oa[i] = lds128(lb + i * 1024 + lane * 16);
#pragma unroll
    for (int h = 0; h < 16; ++h) {
      if (h < 12) { oa[(2 * h + 8) % 10] = lds128(lb + (2 * h + 8) * 1024 + lane * 16); oa[(2 * h + 9) % 10] = lds128(lb + (2 * h + 9) * 1024 + lane * 16); }
      SB(); y0 = mfma16(wo_f[h], oa[(2 * h) % 10], y0); y1 = mfma16(wo_f[h], oa[(2 * h + 1) % 10], y1); SB();
    }
    float yv[8];
#pragma unroll
    for (int r = 0; r < 4; ++r) { auto sw = __builtin_amdgcn_permlane16_swap(__float_as_uint(y0[r]), __float_as_uint(y1[r]), false, false); yv[r] = __uint_as_float(sw[0]); yv[4 + r] = __uint_as_float(sw[1]); }
    const u32 gw[4] = {gaA[0], gaA[2], gaB[0], gaB[2]};
    u32x4 x2p; float s1 = 0.f, s2 = 0.f;
#pragma unroll
    for (int j = 0; j < 4; ++j) {
      x2p[j] = pk2(fmaf(bflo(gw[j]), yv[2 * j], bflo(xres[j])), fmaf(bfhi(gw[j]), yv[2 * j + 1], bfhi(xres[j])));
      s1 = dot2(x2p[j], 0x3F803F80u, s1); s2 = dot2(x2p[j], x2p[j], s2);
    }
    const u32 fo = (u32)(ort * 28 + 7 * ocq + wave) * 1024;
    x2p = nopoison(x2p);
    st128(wsr, (otok + 32 * hi) * 16, OFF_X2 + fo, x2p);
    s1 = xhalf_sum(s1); s2 = xhalf_sum(s2);
    *(LAS float*)(lb + L_ST + wave * 256 + (hi * 32 + otok) * 4) = hi ? s2 : s1;
    WAIT_VM(0);
  }
  BAR();
  if (wave == 7) {
    float t = 0.f;
#pragma unroll
    for (int w = 0; w < 7; ++w) t += lds32f(lb + L_ST + w * 256 + lane * 4);
    const u32 nv = __builtin_amdgcn_update_dpp(0u, __float_as_uint(t), 0xB1, 0xf, 0xf, false);
    st128(wsr, !(lane & 1) ? (u32)lane * 8 : 0x80000000u, OFF_X2G + (u32)(ort * 4 + ocq) * 512, (u32x4){__float_as_uint(t), tag, nv, tag});
  }
  if (wave < 7) {
#pragma unroll
    for (int s = NBA; s < 28; ++s) wgu[s] = ld128(wsr, lane * 16 + (s & 3) * 1024, OFF_WGU + (u32)((7 * gfj + wave) * 28 + (s & ~3)) * 1024);
  } else __builtin_amdgcn_s_sleep(X_SLEEP);
  STAMPL(17, "x2 flagged");

  const int dsl = slot & 15, dblk = 2 * xg + (slot >> 4), drt = 4 * dblk + dsl / 7, dc = dsl % 7;
  const bool dact = dsl < 14;
  u32x4 wdf[28], x2r, gbA, gbB;
  const u32 xa0 = OFF_X2 + (u32)(4 * gi) * 28672;
  u32x4 a1[3];
  {
    u32x4 a0[4];
#pragma nounroll
    for (int i = 0; i < SPIN_CAP; ++i) {
      CFENCE();
#pragma unroll
      for (int k = 0; k < 4; ++k) a0[k] = ld128(wsr, lane * 16 + k * 1024, xa0 + wave * 4096);
      if (!__any(max(max(umax4(a0[0]), umax4(a0[1])), max(umax4(a0[2]), umax4(a0[3]))) == POISON)) break;
      __builtin_amdgcn_s_sleep(2);
    }
    CFENCE();
#pragma unroll
    for (int k = 0; k < 4; ++k) *(LAS u32x4*)(lb + (4 * wave + k) * 1024 + lane * 16) = a0[k];
#pragma unroll
    for (int k = 0; k < 3; ++k) a1[k] = ld128(wsr, lane * 16 + k * 1024, xa0 + 32768 + wave * 3072);
  }
  auto tile1 = [&]() __attribute__((always_inline)) {
#pragma nounroll
    for (int i = 0; i < SPIN_CAP && __any(max(max(umax4(a1[0]), umax4(a1[1])), umax4(a1[2])) == POISON); ++i) {
      CFENCE();
#pragma unroll
      for (int k = 0; k < 3; ++k) a1[k] = ld128(wsr, lane * 16 + k * 1024, xa0 + 32768 + wave * 3072);
    }
#pragma unroll
    for (int k = 0; k < 3; ++k) *(LAS u32x4*)(lb + 32768 + (3 * wave + k) * 1024 + lane * 16) = a1[k];
  };
#define ADMA(n) do { const int g_ = 14 + min(wave + 8 * (n), 13); dma16x4(ws + xa0 + g_ * 4096, lane * 16, lb + g_ * 4096); } while (0)
  BAR();
  STAMPL(19, "A0 landed");
  const int dm = wave >> 2, dn = (wave >> 1) & 1, dgp = wave & 1;
  const u32 wdo = OFF_WDT + (u32)(2 * dc * 112 + 14 * wave) * 1024;
  const u32 pk_free = lb + 114688 + wave * 4096, pk_a = lb + wave * 3072;
#define PARKA(i) ((i) < 4 ? pk_free + (i) * 1024 : pk_a + ((i) < 7 ? (i) - 4 : 28 + (i) - 7) * 1024)
#define PARKD(i) dma16<true>(ws + wdo + (((i) & 1) * 112 + 9 + ((i) >> 1)) * 1024, lane * 16, PARKA(i))
  if (wave < 7) {
    u32 sb = lb + L_SP2 + l31 * 4, ub = lb + L_UV + wave * 256 + hi * 16; asm volatile("" : "+v"(sb), "+v"(ub));
    u32 ab = lb + lane * 16, ab2 = ab + 57344; asm volatile("" : "+v"(ab), "+v"(ab2));
    constexpr int RD = GU_RING;
    static_assert(RD == 6, "the counted waits below assume it");
    f32x16 acc[2]; u32x4 af[RD + 1]; f32x4 uv[4]; float st[8], rs = 0.f, nm = 0.f, hv[8];
#define EP_RD(T) do { _Pragma("unroll") for (int c = 0; c < 4; ++c) { st[c] = lds32f(sb + ((T) * 4 + c) * 256); st[4 + c] = lds32f(sb + ((T) * 4 + c) * 256 + 128); } } while (0)
#define EP_UV(g) do { uv[0] = lds128f(ub + 32 * (g)); uv[1] = lds128f(ub + 32 * (g) + 64); uv[2] = lds128f(ub + 32 * (g) + 128); uv[3] = lds128f(ub + 32 * (g) + 192); } while (0)
#define EP_ST() do { float m = ((st[0] + st[1]) + (st[2] + st[3])) * (1.f / 448.f); asm volatile("" : "+v"(m)); const float e2 = (((st[4] + st[5]) + st[6]) + st[7]) * (1.f / 448.f); \
      rs = __builtin_amdgcn_rsqf(fmaxf(e2 - m * m, 0.f) + 1e-5f); nm = -m * rs; } while (0)
    constexpr float K1 = -2.f * 0.7978845608028654f * 1.4426950408889634f, K3 = K1 * 0.044715f;
    float ea[8], eb[8], k1v = K1; asm volatile("" : "+v"(k1v));
#define PIN1(v) asm volatile("" : "+v"(v))
#define EP_STAGE(A, e, j) do { \
      if ((j) == 0) { ea[e] = fma1(nm, uv[0][(e) & 3], uv[2][(e) & 3]); eb[e] = fma1(nm, uv[1][(e) & 3], uv[3][(e) & 3]); PIN1(ea[e]); PIN1(eb[e]); } \
      if ((j) == 1) { ea[e] = fma1(A[e], rs, ea[e]); eb[e] = fma1(A[8 + (e)], rs, eb[e]); PIN1(ea[e]); PIN1(eb[e]); } \
      if ((j) == 2) { hv[e] = ea[e] * ea[e]; PIN1(hv[e]); eb[e] *= ea[e]; PIN1(eb[e]); } \
      if ((j) == 3) { hv[e] = fmaf(hv[e], K3, k1v); PIN1(hv[e]); } \
      if ((j) == 4) { hv[e] *= ea[e]; PIN1(hv[e]); } \
      if ((j) == 5) { hv[e] = __builtin_amdgcn_exp2f(hv[e]); PIN1(hv[e]); } \
      if ((j) == 6) { hv[e] += 1.f; PIN1(hv[e]); } \
      if ((j) == 7) { hv[e] = __builtin_amdgcn_rcpf(hv[e]); PIN1(hv[e]); } \
      if ((j) == 8) { hv[e] *= eb[e]; PIN1(hv[e]); } } while (0)
#define EP_OUT(T) st128(wsr, lane * 16, OFF_HB + (u32)((4 * gi + (T)) * 112 + 7 * gfj + wave) * 1024, nopoison((u32x4){pk2(hv[0], hv[1]), pk2(hv[2], hv[3]), pk2(hv[4], hv[5]), pk2(hv[6], hv[7])}))
#pragma unroll
    for (int i = 0; i < RD; ++i) af[i] = lds128(ab + i * 1024);
    static_for<0, 112>([&](auto NN) __attribute__((always_inline)) {
      {
        constexpr int n = NN.value, t = n / 28, s = n % 28;
        if (n == 8) tile1();
        if constexpr (n >= 11 && n < 18) { uvp[2 * (n - 11)] = ld32(wsr, lane * 4, OFF_UV + (u32)((2 * (n - 11)) * 112 + 7 * gfj + wave) * 256); uvp[2 * (n - 11) + 1] = ld32(wsr, lane * 4, OFF_UV + (u32)((2 * (n - 11) + 1) * 112 + 7 * gfj + wave) * 256); }
        if (n == 25) {
          float uvs = 0.f;
#pragma unroll
          for (int k = 0; k < 14; ++k) uvs += __uint_as_float(uvp[k]);
          *(LAS float*)(lb + L_UV + wave * 256 + lane * 4) = uvs;
        }
        if (n == 32 - RD) { STAMPL(28, "gu n25"); BAR(); STAMPL(18, "B7c"); ADMA(0); }
        if (n == 31) ADMA(1);
        if (n == 56 - RD) { WAIT_VM(4); asm volatile("s_barrier" ::: "memory"); }
        if constexpr (n + RD < 112) af[(n + RD) % (RD + 1)] = (n + RD < 56) ? lds128(ab + (n + RD) * 1024) : lds128(ab2 + (n + RD - 56) * 1024);
        if (s == 0) __builtin_amdgcn_s_setprio(3 - t);
        SB();
        if (s == 0) acc[t & 1] = mfma32(wgu[s], af[n % (RD + 1)], (f32x16){}); else acc[t & 1] = mfma32(wgu[s], af[n % (RD + 1)], acc[t & 1]);
        SB();
        if constexpr (t > 0) {
          if (s == 0) { EP_RD(t - 1); EP_UV(0); }
          if (s == 2) EP_ST();
          static_for<0, 8>([&](auto EE) __attribute__((always_inline)) { constexpr int e = EE.value, j = s - 3 - 2 * e; if constexpr (j >= 0 && j < 9) EP_STAGE(acc[(t - 1) & 1], e, j); });
          if (s == 9) { SB(); EP_UV(1); }
          if (s == 26) EP_OUT(t - 1);
          if (s == 2 && t == 2) asm volatile("s_barrier" ::: "memory");
          if constexpr (t == 1 && s % 6 == 1 && s >= 7) PARKD(s / 6 - 1);
          if constexpr (t == 2 && s % 4 == 0 && s >= 4 && s < 28) PARKD(3 + s / 4);
          if (s == 27 && t == 1) STAMPL(20, "gu tile1");
          if (s == 27 && t == 2) STAMPL(21, "gu tile2");
          if (s == 27 && t == 3) STAMPL(22, "gu tile3");
          if constexpr (t < 3 ? (s % 12 == 1) : (s % 2 == 1 && s < 24)) { constexpr int f = t < 3 ? 3 * (t - 1) + s / 12 : 6 + s / 2; wdf[f] = ld128(wsr, lane * 16, wdo + (u32)((f & 1) * 112 + (f >> 1)) * 1024); }
          SB();
        }
      }
    });
    asm volatile("s_nop 11" : "+v"(acc[1]));
    EP_RD(3); EP_UV(0); EP_ST();
    {
      f32x4 uw[4];
      uw[0] = lds128f(ub + 32); uw[1] = lds128f(ub + 96); uw[2] = lds128f(ub + 160); uw[3] = lds128f(ub + 224);
      float g8[8], u8[8];
#pragma unroll
      for (int e = 0; e < 8; ++e) {
        const f32x4* W = e < 4 ? uv : uw;
        g8[e] = fma1(acc[1][e], rs, fma1(nm, W[0][e & 3], W[2][e & 3])); u8[e] = fma1(acc[1][8 + e], rs, fma1(nm, W[1][e & 3], W[3][e & 3]));
      }
#pragma unroll
      for (int e = 0; e < 8; ++e) hv[e] = (g8[e] * u8[e]) * __builtin_amdgcn_rcpf(1.f + __builtin_amdgcn_exp2f(g8[e] * fmaf(g8[e] * g8[e], K3, k1v)));
    }
    EP_OUT(3);
    if (!dact) { WAIT_VM(0); if (lane == 0) flag32(ws + OFF_HF + ((gi * 16 + gfj) * 8 + wave) * 128, tag); }
  } else {
    __builtin_amdgcn_s_setprio(3);
    tile1();
    {
    u64 g[16]; const char* gp = ws + OFF_X2G + (16 * gi * 64 + lane) * 8;
#pragma nounroll
    for (int i = 0; i < SPIN_CAP; ++i) {
      bool ok = true;
#pragma unroll
      for (int p = 0; p < 16; ++p) g[p] = poll64(gp + p * 512);
#pragma unroll
      for (int p = 0; p < 16; ++p) ok = ok && (u32)(g[p] >> 32) == tag;
      if (__all(ok)) break;
      __builtin_amdgcn_s_sleep(1);
    }
    CFENCE();
#pragma unroll
    for (int p = 0; p < 16; ++p) *(LAS u32*)(lb + L_SP2 + p * 256 + lane * 4) = (u32)g[p];
    }
    BAR();
    ADMA(0); ADMA(1);
#pragma unroll
    for (int f = 0; f < 18; ++f) wdf[f] = ld128(wsr, lane * 16, wdo + (u32)((f & 1) * 112 + (f >> 1)) * 1024);
#pragma unroll
    for (int i = 0; i < 4; ++i) PARKD(i);
    WAIT_VM(22);
    asm volatile("s_barrier" ::: "memory");
    asm volatile("s_barrier" ::: "memory");
#pragma unroll
    for (int i = 4; i < 10; ++i) PARKD(i);
    if (dact) {

      wait_flags(ws + OFF_HF + ((dblk * 16 + (lane & 15)) * 8 + 7) * 128, tag);
    }
    __builtin_amdgcn_s_waitcnt(0x0F70);
    CFENCE();
    __builtin_amdgcn_s_setprio(0);
  }
  STAMPL(23, "h flagged");
  if (dact) {
    BAR();
    constexpr int HR = 9;
    u32x4 hf[HR];
    const int dcol = 64 * dc + 32 * dn + 16 * dgp + 8 * hi;
    const u32 hb0 = OFF_HB + (u32)(drt * 112 + 14 * wave) * 1024;
#define LDH(i) hf[(i) % HR] = ld128(wsr, lane * 16 + (((i) % 14) & 3) * 1024, hb0 + (u32)(((i) / 14) * 224 + (((i) % 14) & ~3)) * 1024)
#pragma unroll
    for (int i = 0; i < 4; ++i) { LDH(i); SB(); }
    WAIT_VM(20);
#pragma unroll
    for (int f = 18; f < 28; ++f) wdf[f] = lds128(PARKA(f - 18) + lane * 16);
    BAR();
#pragma unroll
    for (int i = 4; i < HR - 1; ++i) { LDH(i); SB(); }
    STAMPL(24, "down go");
    f32x16 c0, c1; u32 seen[2] = {0u, 0u};
    u32 pwb = lb + wave * 16384 + lane * 16; asm volatile("" : "+v"(pwb));
#define WRC(m) do { _Pragma("unroll") for (int g = 0; g < 4; ++g) { \
          *(LAS f32x4*)(pwb + ((2 * (m)) * 4 + g) * 1024) = (f32x4){c0[4 * g], c0[4 * g + 1], c0[4 * g + 2], c0[4 * g + 3]}; \
          *(LAS f32x4*)(pwb + ((2 * (m) + 1) * 4 + g) * 1024) = (f32x4){c1[4 * g], c1[4 * g + 1], c1[4 * g + 2], c1[4 * g + 3]}; } } while (0)
    static_for<0, 28>([&](auto II) __attribute__((always_inline)) {
      constexpr int i = II.value, m = i / 14, j = i % 14;
      if constexpr (i + HR - 1 < 28) LDH(i + HR - 1);
      if constexpr (i == 20) {
        gbA = ld128(wsr, dcol * 4, OFF_MODG + 1120 * 8); gbB = ld128(wsr, dcol * 4 + 16, OFF_MODG + 1120 * 8);
        x2r = ld128(wsr, lane * 16, OFF_X2 + (u32)((drt + 2 * dm) * 28 + 4 * dc + 2 * dn + dgp) * 1024);
      }
      SB();
      if constexpr (j == 0) { c0 = mfma32(wdf[0], hf[i % HR], (f32x16){}); c1 = mfma32(wdf[1], hf[i % HR], (f32x16){}); }
      else { c0 = mfma32(wdf[2 * j], hf[i % HR], c0); c1 = mfma32(wdf[2 * j + 1], hf[i % HR], c1); }
      SB();
      seen[m] = max(seen[m], umax4(hf[i % HR])); pin32(seen[m]);
      if constexpr (i == 1) { WAIT_VM(8); if (wave < 7 && lane == 0) flag32(ws + OFF_HF + ((gi * 16 + gfj) * 8 + wave) * 128, tag); }
      if constexpr (j == 13) WRC(m);
      if constexpr (i == 13) STAMPL(27, "early done");
    });
#pragma nounroll
    for (int m = 0; m < 2; ++m) {
      if (__any((m ? seen[1] : seen[0]) == POISON) || FORCE_REDO) {
        STAMPL(2, "redo");
        wait_flags(ws + OFF_HF + ((dblk * 16 + 2 * wave + ((lane >> 3) & 1)) * 8 + min(lane & 7, 6)) * 128, tag);
        x2r = ld128(wsr, lane * 16, OFF_X2 + (u32)((drt + 2 * dm) * 28 + 4 * dc + 2 * dn + dgp) * 1024);
        c0 = (f32x16){}; c1 = (f32x16){};
#pragma unroll
        for (int j = 0; j < 14; ++j) { const u32x4 hh = ld128(wsr, lane * 16, hb0 + (u32)(m * 224 + j) * 1024); c0 = mfma32(wdf[2 * j], hh, c0); c1 = mfma32(wdf[2 * j + 1], hh, c1); }
        pwb += m * 8192; WRC(0); pwb -= m * 8192;
      }
    }
    STAMPL(25, "down mfma done");
    BAR();
    f32x4 pa[8], pb[8];
    {
      u32 base = lb + (2 * dm + dn) * 4096 + dgp * 2048 + lane * 16, base2 = base + 65536;
      asm volatile("" : "+v"(base), "+v"(base2));
#pragma unroll
      for (int w = 0; w < 4; ++w) {
        pa[w] = lds128f(base + w * 16384); pb[w] = lds128f(base + w * 16384 + 1024);
        pa[4 + w] = lds128f(base2 + w * 16384); pb[4 + w] = lds128f(base2 + w * 16384 + 1024);
      }
      SB();
    }
    f32x4 a0 = ((pa[0] + pa[4]) + (pa[1] + pa[5])) + ((pa[2] + pa[6]) + (pa[3] + pa[7])), a1 = ((pb[0] + pb[4]) + (pb[1] + pb[5])) + ((pb[2] + pb[6]) + (pb[3] + pb[7]));
    asm volatile("" : "+v"(a0), "+v"(a1));
    float yv[8];
#pragma unroll
    for (int r = 0; r < 4; ++r) { auto sw = __builtin_amdgcn_permlane32_swap(__float_as_uint(a0[r]), __float_as_uint(a1[r]), false, false); yv[r] = __uint_as_float(sw[0]); yv[4 + r] = __uint_as_float(sw[1]); }
    const u32 gw[4] = {gbA[0], gbA[2], gbB[0], gbB[2]};
    u32x4 ov;
#pragma unroll
    for (int j = 0; j < 4; ++j) ov[j] = pk2(fmaf(bflo(gw[j]), yv[2 * j], bflo(x2r[j])), fmaf(bfhi(gw[j]), yv[2 * j + 1], bfhi(x2r[j])));
    *(u32x4*)(out + (size_t)(32 * (drt + 2 * dm) + l31) * 448 + dcol) = ov;
    if (b == 0 && tid == 0) *(GAS u32*)ws = tag;
  }
  STAMPL(26, "end");
}

extern "C" void kernel_launch(void* const* d_in, const int* in_sizes, int n_in, void* d_out, int out_size, void* d_ws, size_t ws_size, hipStream_t stream) {
  fused_block<<<256, 512, 0, stream>>>((const u16*)d_in[0], (const u16*)d_in[1], (const u16*)d_in[2], (const u16*)d_in[4], (const u16*)d_in[5],
                                       (const u16*)d_in[6], (const u16*)d_in[7], (const u16*)d_in[8], (u16*)d_out, (char*)d_ws);
}
```
